```python
import torch

def _softmax(x, axis=-1):
    return torch.softmax(x, dim=axis)

def _gelu(x):
    return torch.nn.functional.gelu(x, approximate='tanh')

def _silu(x):
    return torch.nn.functional.silu(x)

def _sigmoid(x):
    return torch.sigmoid(x)

def _layernorm(x, axis=-1, eps=1e-05):
    m = x.mean(dim=axis, keepdim=True)
    v = x.var(dim=axis, keepdim=True, unbiased=False)
    return (x - m) / torch.sqrt(v + eps)

def _scatter_add(tgt, idx, src, axis=0):
    return tgt.clone().index_add_(axis, idx, src)

def _segment_sum(src, seg, n):
    out = torch.zeros((n,) + src.shape[1:], dtype=src.dtype)
    return out.index_add_(0, seg, src)

def _pad(x, pw):
    flat = tuple((v for pair in reversed(pw) for v in pair))
    return torch.nn.functional.pad(x, flat)
kernel_name = 'gated_mix__memory_aug_recurrent_mem_tr__glu_gated_geglu__prompt'

def setup_inputs():

    _w = lambda a: (lambda t: t.to(torch.bfloat16) if t.is_floating_point() else t)(torch.as_tensor(a))

    def _A_setup():
        B, S, Dm = (1, 2048, 640)
        H, Dh = (32, 128)
        M, seg = (8, 32)
        seg = min(seg, S)
        S = S // seg * seg
        return {'x': _w((lambda *s: torch.randn(*s, dtype=torch.bfloat16) * 0.05)(B, S, Dm)), 'mem0': _w((lambda *s: torch.randn(*s, dtype=torch.bfloat16) * 0.4)(B, M, Dm)), 'Wq': _w((lambda *s: torch.randn(*s, dtype=torch.bfloat16) * 6.4)(Dm, H, Dh) * Dm ** (-0.5)), 'Wk': _w((lambda *s: torch.randn(*s, dtype=torch.bfloat16) * 6.4)(Dm, H, Dh) * Dm ** (-0.5)), 'Wv': _w((lambda *s: torch.randn(*s, dtype=torch.bfloat16) * 0.4)(Dm, H, Dh) * Dm ** (-0.5)), 'Wo': _w((lambda *s: torch.randn(*s, dtype=torch.bfloat16) * 0.4)(H, Dh, Dm) * (H * Dh) ** (-0.5)), 'seg_len': seg}
    _A_in = _A_setup()

    def _B_setup():
        B, S, Dm, Df = (1, 2048, 640, 1728)
        return {'x': _w((lambda *s: torch.randn(*s, dtype=torch.bfloat16) * 0.05)(B, S, Dm)), 'w_gate': _w((lambda *s: torch.randn(*s, dtype=torch.bfloat16) * 1.6)(Dm, Df) * Dm ** (-0.5)), 'w_up': _w((lambda *s: torch.randn(*s, dtype=torch.bfloat16) * 1.6)(Dm, Df) * Dm ** (-0.5)), 'w_down': _w((lambda *s: torch.randn(*s, dtype=torch.bfloat16) * 1.6)(Df, Dm) * Df ** (-0.5))}
    _B_in = _B_setup()
    return {'A_x': _A_in['x'], 'A_mem0': _A_in['mem0'], 'A_Wq': _A_in['Wq'], 'A_Wk': _A_in['Wk'], 'A_Wv': _A_in['Wv'], 'A_Wo': _A_in['Wo'], 'A_seg_len': _A_in['seg_len'], 'B_x': _B_in['x'], 'B_w_gate': _B_in['w_gate'], 'B_w_up': _B_in['w_up'], 'B_w_down': _B_in['w_down'], 'gate_w': _w((lambda *s: torch.randn(*s, dtype=torch.bfloat16) * 0.05)(640) * 16.0)}

def reference(A_x, A_mem0, A_Wq, A_Wk, A_Wv, A_Wo, A_seg_len, B_x, B_w_gate, B_w_up, B_w_down, gate_w):

    def _A_ref(x, mem0, Wq, Wk, Wv, Wo, seg_len):
        B, S, Dm = x.shape
        H, Dh = (Wq.shape[1], Wq.shape[2])
        M = mem0.shape[1]
        scale = Dh ** (-0.5)
        n_seg = S // seg_len
        L = M + seg_len
        idx = torch.arange(L)
        mask = (idx[:, None] < M) | (idx[:, None] >= idx[None, :])
        mem = mem0
        outs = []
        for s in range(n_seg):
            xs = x[:, s * seg_len:(s + 1) * seg_len, :].clone()
            h = torch.concatenate([mem, xs], axis=1)
            q = torch.einsum('btd,dhe->bhte', h, Wq)
            k = torch.einsum('btd,dhe->bhte', h, Wk)
            v = torch.einsum('btd,dhe->bhte', h, Wv)
            scores = torch.einsum('bhqe,bhke->bhqk', q, k) * scale
            scores = torch.where(mask[None, None], scores, -1000000000.0)
            p = _softmax(scores, axis=-1)
            o = torch.einsum('bhqk,bhke->bhqe', p, v)
            o = torch.einsum('bhte,hed->btd', o, Wo)
            mem = o[:, :M, :].clone()
            outs.append(o[:, M:, :])
        return torch.concatenate(outs, axis=1)

    def _B_ref(x, w_gate, w_up, w_down):
        g = torch.einsum('bsd,df->bsf', x, w_gate)
        u = torch.einsum('bsd,df->bsf', x, w_up)
        h = _gelu(g) * u
        return torch.einsum('bsf,fd->bsd', h, w_down)
    x = A_x
    g = _sigmoid((x * gate_w).sum(-1, keepdims=True))
    a = _A_ref(x, A_mem0, A_Wq, A_Wk, A_Wv, A_Wo, A_seg_len)
    b = _B_ref(x, B_w_gate, B_w_up, B_w_down)
    return g * a + (1 - g) * b
```

```cpp
#include <hip/hip_runtime.h>
#include <hip/hip_bf16.h>
#include <stdint.h>
#include <stdio.h>

typedef unsigned short u16;
typedef __bf16 b16x8 __attribute__((ext_vector_type(8)));
typedef short s16x4 __attribute__((ext_vector_type(4)));
typedef float f32x4 __attribute__((ext_vector_type(4)));
typedef unsigned u32x4 __attribute__((ext_vector_type(4)));
typedef unsigned u32x2 __attribute__((ext_vector_type(2)));
#define LAS __attribute__((address_space(3)))
#define DI __device__ __forceinline__

constexpr int S = 2048, D = 640, H = 32, E = 128, HE = 4096, MM = 8, SEG = 32, NSEG = 64, F = 1728;
constexpr float SCALE = 0.08838834764831845f;

constexpr size_t OFF_QX = 0;
constexpr size_t OFF_KX = OFF_QX + (size_t)S * HE * 2;
constexpr size_t OFF_VT = OFF_KX + (size_t)S * HE * 2;
constexpr size_t OFF_O  = OFF_VT + (size_t)S * HE * 2;
constexpr size_t OFF_HH = OFF_O + (size_t)S * HE * 2;
constexpr size_t OFF_G  = OFF_HH + (size_t)S * F * 2;
constexpr size_t OFF_P  = OFF_G + (size_t)S * 4;
constexpr size_t SZ_P   = (size_t)H * 640 * 8 * 4;
constexpr size_t OFF_SENT = OFF_P + SZ_P;
constexpr size_t OFF_MEM = OFF_SENT;
constexpr size_t OFF_QM = OFF_MEM + (size_t)NSEG * 10240;
constexpr size_t OFF_KM = OFF_QM + (size_t)NSEG * H * 2048;
constexpr size_t OFF_VM = OFF_KM + (size_t)NSEG * H * 2048;
constexpr size_t OFF_END = OFF_VM + (size_t)NSEG * H * 2048;
constexpr size_t SZ_SENT = OFF_END - OFF_SENT;
constexpr size_t OFF_DBG = OFF_END;

DI float bf2f(u16 v) { return __builtin_bit_cast(float, (unsigned)v << 16); }
DI u16 f2bf(float f) { return __builtin_bit_cast(u16, (__bf16)f); }
DI float rbf(float f) { return (float)(__bf16)f; }
DI unsigned pack2(float lo, float hi) { return (unsigned)f2bf(lo) | ((unsigned)f2bf(hi) << 16); }
DI f32x4 mfma16(b16x8 a, b16x8 b, f32x4 c) { return __builtin_amdgcn_mfma_f32_16x16x32_bf16(a, b, c, 0, 0, 0); }
DI b16x8 asb(u32x4 v) { return __builtin_bit_cast(b16x8, v); }

DI unsigned offA(unsigned row, unsigned ch) { return 128u * row + 16u * (ch ^ (row & 7)); }
DI unsigned offB(unsigned row, unsigned ch) { return 256u * row + 16u * (ch ^ (((row & 3) << 2) | ((row >> 2) & 3))); }

template <int BMODE, bool SWAP>
DI void gemm_tile(const u16* __restrict__ A, int lda, const u16* __restrict__ B, const u16* __restrict__ B2, int ldb, int K,
                  int m0, int n0, char* lds, f32x4 (&acc)[4][4]) {
  const int tid = threadIdx.x, lane = tid & 63, wave = tid >> 6, wr = wave >> 1, wc = wave & 1;
  const int fr = lane & 15, g = lane >> 4;
  char* sA = lds; char* sB = lds + 16384;
  const unsigned sBbase = (unsigned)(size_t)sB;
  for (int k0 = 0; k0 < K; k0 += 64) {
    u32x4 ra[4], rb[4];
#pragma unroll
    for (int i = 0; i < 4; ++i) {
      int row = (tid >> 3) + 32 * i, ch = tid & 7;
      ra[i] = *(const u32x4*)(A + (size_t)(m0 + row) * lda + k0 + ch * 8);
      int brow = (tid >> 4) + 16 * i, bch = tid & 15;
      if (BMODE == 0) rb[i] = *(const u32x4*)(B + (size_t)(k0 + brow) * ldb + n0 + bch * 8);
      else {
        const u16* src = ((bch >> 2) & 1) ? B2 : B;
        int col = n0 + (bch >> 3) * 32 + (bch & 3) * 8;
        rb[i] = *(const u32x4*)(src + (size_t)(k0 + brow) * ldb + col);
      }
    }
    __syncthreads();
#pragma unroll
    for (int i = 0; i < 4; ++i) {
      int row = (tid >> 3) + 32 * i, ch = tid & 7;
      *(u32x4*)(sA + offA(row, ch)) = ra[i];
      int brow = (tid >> 4) + 16 * i, bch = tid & 15;
      *(u32x4*)(sB + offB(brow, bch)) = rb[i];
    }
    __syncthreads();
#pragma unroll
    for (int ks = 0; ks < 2; ++ks) {
      b16x8 af[4], bfr[4];
#pragma unroll
      for (int i = 0; i < 4; ++i) af[i] = *(const b16x8*)(sA + offA(wr * 64 + i * 16 + fr, ks * 4 + g));
#pragma unroll
      for (int j = 0; j < 4; ++j) {
        const unsigned q = fr >> 2, p = lane & 3, c = wc * 4 + j;
        s16x4 lo = __builtin_amdgcn_ds_read_tr16_b64_v4i16((LAS s16x4*)(sBbase + offB(32 * ks + 8 * g + q, 2 * c + (p >> 1)) + 8 * (p & 1)));
        s16x4 hi = __builtin_amdgcn_ds_read_tr16_b64_v4i16((LAS s16x4*)(sBbase + offB(32 * ks + 8 * g + 4 + q, 2 * c + (p >> 1)) + 8 * (p & 1)));
        typedef short s16x8 __attribute__((ext_vector_type(8)));
        s16x8 t = {lo[0], lo[1], lo[2], lo[3], hi[0], hi[1], hi[2], hi[3]};
        bfr[j] = __builtin_bit_cast(b16x8, t);
      }
#pragma unroll
      for (int i = 0; i < 4; ++i)
#pragma unroll
        for (int j = 0; j < 4; ++j)
          acc[i][j] = SWAP ? mfma16(bfr[j], af[i], acc[i][j]) : mfma16(af[i], bfr[j], acc[i][j]);
    }
  }
}

DI float gelu_tanh(float x) {
  float y = 0.7978845608028654f * (x + 0.044715f * x * x * x);
  float t = 1.f - 2.f / (__expf(2.f * y) + 1.f);
  return 0.5f * x * (1.f + t);
}

constexpr int NB_QKV = 16 * 96, NB_GLU = 16 * 27, NB_GATE = 2048 / 4, NB_FILL = 256;
__global__ __launch_bounds__(256) void k_pre(const u16* __restrict__ x, const u16* __restrict__ Wq, const u16* __restrict__ Wk,
                                             const u16* __restrict__ Wv, const u16* __restrict__ wg, const u16* __restrict__ wu,
                                             const u16* __restrict__ gate_w, char* __restrict__ ws) {
  __shared__ __attribute__((aligned(16))) char lds[32768];
  int b = blockIdx.x;
  const int tid = threadIdx.x, lane = tid & 63, wave = tid >> 6, wr = wave >> 1, wc = wave & 1, fr = lane & 15, g = lane >> 4;
  f32x4 acc[4][4];
#pragma unroll
  for (int i = 0; i < 4; ++i)
#pragma unroll
    for (int j = 0; j < 4; ++j) acc[i][j] = (f32x4){0, 0, 0, 0};
  if (b < NB_QKV) {
    int mt = b & 15, nt = b >> 4, which = nt / 32, n0 = (nt % 32) * 128, m0 = mt * 128;
    if (which < 2) {
      gemm_tile<0, true>(x, D, which ? Wk : Wq, nullptr, HE, D, m0, n0, lds, acc);
      u16* out = (u16*)(ws + (which ? OFF_KX : OFF_QX));
#pragma unroll
      for (int i = 0; i < 4; ++i)
#pragma unroll
        for (int j = 0; j < 4; ++j) {
          u32x2 v = {pack2(acc[i][j][0], acc[i][j][1]), pack2(acc[i][j][2], acc[i][j][3])};
          *(u32x2*)(out + (size_t)(m0 + wr * 64 + i * 16 + fr) * HE + n0 + wc * 64 + j * 16 + 4 * g) = v;
        }
    } else {
      gemm_tile<0, false>(x, D, Wv, nullptr, HE, D, m0, n0, lds, acc);
      u16* out = (u16*)(ws + OFF_VT);
#pragma unroll
      for (int i = 0; i < 4; ++i)
#pragma unroll
        for (int j = 0; j < 4; ++j) {
          u32x2 v = {pack2(acc[i][j][0], acc[i][j][1]), pack2(acc[i][j][2], acc[i][j][3])};
          *(u32x2*)(out + (size_t)(n0 + wc * 64 + j * 16 + fr) * S + m0 + wr * 64 + i * 16 + 4 * g) = v;
        }
    }
    return;
  }
  b -= NB_QKV;
  if (b < NB_GLU) {
    int mt = b & 15, ft = b >> 4, f0 = ft * 64, m0 = mt * 128;
    gemm_tile<1, true>(x, D, wg, wu, F, D, m0, f0, lds, acc);
    u16* out = (u16*)(ws + OFF_HH);
#pragma unroll
    for (int i = 0; i < 4; ++i)
#pragma unroll
      for (int j = 0; j < 2; ++j) {
        float hv[4];
#pragma unroll
        for (int r = 0; r < 4; ++r) {
          float gg = rbf(acc[i][j][r]), uu = rbf(acc[i][j + 2][r]);
          hv[r] = rbf(gelu_tanh(gg)) * uu;
        }
        u32x2 v = {pack2(hv[0], hv[1]), pack2(hv[2], hv[3])};
        *(u32x2*)(out + (size_t)(m0 + wr * 64 + i * 16 + fr) * F + f0 + wc * 32 + j * 16 + 4 * g) = v;
      }
    return;
  }
  b -= NB_GLU;
  if (b < NB_GATE) {
    int t = b * 4 + wave;
    float s = 0.f;
    for (int d = lane; d < D; d += 64) s += rbf(bf2f(x[(size_t)t * D + d]) * bf2f(gate_w[d]));
#pragma unroll
    for (int o = 32; o >= 1; o >>= 1) s += __shfl_xor(s, o);
    s = rbf(s);
    float gg = rbf(1.f / (1.f + __expf(-s)));
    if (lane == 0) ((float*)(ws + OFF_G))[t] = gg;
    return;
  }
  b -= NB_GATE;
  {
    u32x4 ones = {0xFFFFFFFFu, 0xFFFFFFFFu, 0xFFFFFFFFu, 0xFFFFFFFFu}, zero = {0, 0, 0, 0};
    for (size_t i = (size_t)b * 256 + tid; i < SZ_SENT / 16; i += (size_t)NB_FILL * 256) *(u32x4*)(ws + OFF_SENT + i * 16) = ones;
    for (size_t i = (size_t)b * 256 + tid; i < SZ_P / 16; i += (size_t)NB_FILL * 256) *(u32x4*)(ws + OFF_P + i * 16) = zero;
  }
}

#define SC1 16
DI u32x4 ld128(__amdgpu_buffer_rsrc_t rs, unsigned off) { return __builtin_amdgcn_raw_buffer_load_b128(rs, off, 0, SC1); }
DI u32x2 ld64(__amdgpu_buffer_rsrc_t rs, unsigned off) { return __builtin_amdgcn_raw_buffer_load_b64(rs, off, 0, SC1); }
DI void st128(__amdgpu_buffer_rsrc_t rs, unsigned off, u32x4 v) { __builtin_amdgcn_raw_buffer_store_b128(v, rs, off, 0, SC1); }
DI void st64(__amdgpu_buffer_rsrc_t rs, unsigned off, u32x2 v) { __builtin_amdgcn_raw_buffer_store_b64(v, rs, off, 0, SC1); }
DI unsigned nosent(unsigned v) { return v == 0xFFFFFFFFu ? 0xFFFEFFFFu : v; }
DI unsigned umax3(unsigned a, unsigned b, unsigned c) { return max(max(a, b), c); }

constexpr unsigned SPIN_CAP = 1u << 17;

__global__ __launch_bounds__(256, 1) void k_chain(const u16* __restrict__ mem0, const u16* __restrict__ Wq, const u16* __restrict__ Wk,
                                                   const u16* __restrict__ Wv, const u16* __restrict__ Wo, char* __restrict__ ws) {
  extern __shared__ __attribute__((aligned(16))) char dyn_lds[];
  const int b = blockIdx.x, xcd = b & 7, y = b >> 3, h = xcd * 4 + (y >> 3), j = y & 7;
  const int lane = threadIdx.x & 63, wave = __builtin_amdgcn_readfirstlane(threadIdx.x >> 6);
  const int ml = lane & 15, g = lane >> 4;
  const __amdgpu_buffer_rsrc_t rs = __builtin_amdgcn_make_buffer_rsrc(ws + OFF_P, 0, (int)(OFF_END - OFF_P), 0x00020000);
  constexpr unsigned R_P = 0, R_MEM = (unsigned)(OFF_MEM - OFF_P), R_QM = (unsigned)(OFF_QM - OFF_P), R_KM = (unsigned)(OFF_KM - OFF_P),
                     R_VM = (unsigned)(OFF_VM - OFF_P);
  const u16* KX = (const u16*)(ws + OFF_KX);
  const u16* VT = (const u16*)(ws + OFF_VT);

  b16x8 w1[20];
  {
    const u16* W = wave == 0 ? Wq : (wave == 1 ? Wk : Wv);
    if (wave < 3) {
#pragma unroll
      for (int kk = 0; kk < 20; ++kk) {
        typedef u16 u16x8 __attribute__((ext_vector_type(8)));
        u16x8 t;
#pragma unroll
        for (int jj = 0; jj < 8; ++jj) t[jj] = W[(size_t)(32 * kk + 8 * g + jj) * HE + h * E + 16 * j + ml];
        w1[kk] = __builtin_bit_cast(b16x8, t);
      }
    } else {
#pragma unroll
      for (int kk = 0; kk < 20; ++kk) w1[kk] = asb((u32x4){0, 0, 0, 0});
    }
  }
  b16x8 w2[2][4];
#pragma unroll
  for (int t = 0; t < 2; ++t) {
    const int ct = (t == 0) ? wave : 4;
#pragma unroll
    for (int ks = 0; ks < 4; ++ks) {
      typedef u16 u16x8 __attribute__((ext_vector_type(8)));
      u16x8 tt;
#pragma unroll
      for (int jj = 0; jj < 8; ++jj) {
        int e = 32 * ks + (jj >> 2) * 16 + 4 * g + (jj & 3);
        tt[jj] = Wo[(size_t)(h * E + e) * D + 80 * j + 16 * ct + ml];
      }
      w2[t][ks] = __builtin_bit_cast(b16x8, tt);
    }
  }
  bool dead = false;
  int runit = -1;
  if (wave < 2) runit = b * 2 + wave; else if (wave == 2 && b < 128) runit = 512 + b;

#define DBG_STEPS NSEG
  for (int s = 0; s < DBG_STEPS; ++s) {
    b16x8 kx[2][4]; u32x2 vx[8][2];
    if (s < NSEG - 1) {
#pragma unroll
      for (int kt = 0; kt < 2; ++kt)
#pragma unroll
        for (int ks = 0; ks < 4; ++ks) kx[kt][ks] = *(const b16x8*)(KX + (size_t)(SEG * s + 16 * kt + ml) * HE + h * E + 32 * ks + 8 * g);
#pragma unroll
      for (int et = 0; et < 8; ++et)
#pragma unroll
        for (int hh = 0; hh < 2; ++hh) vx[et][hh] = *(const u32x2*)(VT + (size_t)(h * E + 16 * et + ml) * S + SEG * s + 16 * hh + 4 * g);
    }
    if (wave < 3) {
      u32x4 mf[20];
      if (s == 0) {
#pragma unroll
        for (int kk = 0; kk < 20; ++kk) mf[kk] = (ml < 8) ? *(const u32x4*)(mem0 + ml * D + 32 * kk + 8 * g) : (u32x4){0, 0, 0, 0};
      } else {
        const unsigned base = R_MEM + (unsigned)s * 10240u + (unsigned)(g * 8 + (ml & 7)) * 16u;
        for (unsigned spin = 0;; ++spin) {
          asm volatile("" ::: "memory");
          unsigned mx = 0;
#pragma unroll
          for (int kk = 0; kk < 20; ++kk) mf[kk] = ld128(rs, base + kk * 512u);
#pragma unroll
          for (int kk = 0; kk < 20; ++kk) mx = umax3(mx, mf[kk][0], mf[kk][2]);
          if (__all(mx != 0xFFFFFFFFu) || dead) break; if (spin > SPIN_CAP) { dead = true; break; }
        }
      }
      f32x4 acc = {0, 0, 0, 0};
      if (wave < 2) {
#pragma unroll
        for (int kk = 0; kk < 20; ++kk) acc = mfma16(w1[kk], asb(mf[kk]), acc);
        u32x2 v = {nosent(pack2(acc[0], acc[1])), nosent(pack2(acc[2], acc[3]))};
        unsigned off = (wave == 0 ? R_QM : R_KM) + (unsigned)(s * H + h) * 2048u + (unsigned)(((j >> 1) * 4 + 2 * (j & 1) + (g >> 1)) * 8 + ml) * 16u + (g & 1) * 8u;
        if (ml < 8) st64(rs, off, v);
      } else {
#pragma unroll
        for (int kk = 0; kk < 20; ++kk) acc = mfma16(asb(mf[kk]), w1[kk], acc);
        u32x2 v = {nosent(pack2(acc[0], acc[1])), nosent(pack2(acc[2], acc[3]))};
        unsigned off = R_VM + (unsigned)(s * H + h) * 2048u + (unsigned)(16 * j + ml) * 16u + (unsigned)g * 8u;
        if (g < 2) st64(rs, off, v);
      }
    }
    if (s == NSEG - 1) break;
    {
      u32x4 qf[4], kf[4]; u32x2 vm[8];
      const unsigned hb = (unsigned)(s * H + h) * 2048u;
      const unsigned fo = (unsigned)(g * 8 + (ml & 7)) * 16u;
      for (unsigned spin = 0;; ++spin) {
        asm volatile("" ::: "memory");
        unsigned mx = 0;
#pragma unroll
        for (int ks = 0; ks < 4; ++ks) { qf[ks] = ld128(rs, R_QM + hb + ks * 512u + fo); kf[ks] = ld128(rs, R_KM + hb + ks * 512u + fo); }
#pragma unroll
        for (int et = 0; et < 8; ++et) vm[et] = ld64(rs, R_VM + hb + (unsigned)(16 * et + ml) * 16u + (unsigned)(g & 1) * 8u);
#pragma unroll
        for (int ks = 0; ks < 4; ++ks) { mx = umax3(mx, qf[ks][0], qf[ks][2]); mx = umax3(mx, kf[ks][0], kf[ks][2]); }
#pragma unroll
        for (int et = 0; et < 8; et += 2) mx = umax3(mx, vm[et][0], vm[et + 1][0]);
        if (__all(mx != 0xFFFFFFFFu) || dead) break; if (spin > SPIN_CAP) { dead = true; break; }
      }
      f32x4 s0 = {0, 0, 0, 0}, s1 = {0, 0, 0, 0}, sm = {0, 0, 0, 0};
#pragma unroll
      for (int ks = 0; ks < 4; ++ks) {
        s0 = mfma16(kx[0][ks], asb(qf[ks]), s0);
        s1 = mfma16(kx[1][ks], asb(qf[ks]), s1);
        sm = mfma16(asb(kf[ks]), asb(qf[ks]), sm);
      }
      float sv[12];
#pragma unroll
      for (int r = 0; r < 4; ++r) {
        sv[r] = rbf(rbf(s0[r]) * SCALE); sv[4 + r] = rbf(rbf(s1[r]) * SCALE);
        sv[8 + r] = (g < 2) ? rbf(rbf(sm[r]) * SCALE) : -INFINITY;
      }
      float mxv = sv[0];
#pragma unroll
      for (int r = 1; r < 12; ++r) mxv = fmaxf(mxv, sv[r]);
      mxv = fmaxf(mxv, __shfl_xor(mxv, 16)); mxv = fmaxf(mxv, __shfl_xor(mxv, 32));
      float sum = 0.f;
#pragma unroll
      for (int r = 0; r < 12; ++r) { sv[r] = __expf(sv[r] - mxv); sum += sv[r]; }
      sum += __shfl_xor(sum, 16); sum += __shfl_xor(sum, 32);
      const float inv = 1.f / sum;
      u32x4 px = {pack2(sv[0] * inv, sv[1] * inv), pack2(sv[2] * inv, sv[3] * inv), pack2(sv[4] * inv, sv[5] * inv), pack2(sv[6] * inv, sv[7] * inv)};
      u32x4 pm = {pack2(sv[8] * inv, sv[9] * inv), pack2(sv[10] * inv, sv[11] * inv), 0u, 0u};
      u32x2 ob[8];
#pragma unroll
      for (int et = 0; et < 8; ++et) {
        f32x4 o = {0, 0, 0, 0};
        u32x4 va = {vx[et][0][0], vx[et][0][1], vx[et][1][0], vx[et][1][1]};
        o = mfma16(asb(va), asb(px), o);
        u32x4 vb = {g < 2 ? vm[et][0] : 0u, g < 2 ? vm[et][1] : 0u, 0u, 0u};
        o = mfma16(asb(vb), asb(pm), o);
        ob[et] = (u32x2){pack2(o[0], o[1]), pack2(o[2], o[3])};
      }
      const unsigned par = (unsigned)((s + 1) & 1);
#pragma unroll
      for (int t = 0; t < 2; ++t) {
        if (t == 1 && wave != 3) break;
        const int ct = (t == 0) ? wave : 4;
        f32x4 a = {0, 0, 0, 0};
#pragma unroll
        for (int ks = 0; ks < 4; ++ks) {
          u32x4 of = {ob[2 * ks][0], ob[2 * ks][1], ob[2 * ks + 1][0], ob[2 * ks + 1][1]};
          a = mfma16(w2[t][ks], asb(of), a);
        }
        u32x4 v;
#pragma unroll
        for (int r = 0; r < 4; ++r) v[r] = (__float_as_uint(a[r]) & ~1u) | par;
        const int T = 5 * j + ct;
        unsigned off = R_P + (unsigned)h * 20480u + (unsigned)(((T >> 1) * 4 + 2 * (T & 1) + (g >> 1)) * 8 + ml) * 32u + (unsigned)(g & 1) * 16u;
        if (ml < 8) st128(rs, off, v);
      }
    }
    if (runit >= 0) {
      const unsigned par = (unsigned)((s + 1) & 1);
      const unsigned off = R_P + (unsigned)(lane >> 1) * 20480u + (unsigned)runit * 32u + (unsigned)(lane & 1) * 16u;
      u32x4 v;
      for (unsigned spin = 0;; ++spin) {
        asm volatile("" ::: "memory");
        v = ld128(rs, off);
        unsigned a = v[0] & v[1] & v[2] & v[3], o = v[0] | v[1] | v[2] | v[3];
        bool ok = par ? (a & 1u) : !(o & 1u);
        if (__all(ok) || dead) break; if (spin > SPIN_CAP) { dead = true; break; }
      }
      float f[4];
#pragma unroll
      for (int r = 0; r < 4; ++r) f[r] = __builtin_bit_cast(float, v[r] & ~1u);
#pragma unroll
      for (int o = 2; o <= 32; o <<= 1)
#pragma unroll
        for (int r = 0; r < 4; ++r) f[r] += __shfl_xor(f[r], o);
      u32x2 w = {nosent(pack2(f[0], f[1])), nosent(pack2(f[2], f[3]))};
      if (lane < 2) st64(rs, R_MEM + (unsigned)(s + 1) * 10240u + (unsigned)runit * 16u + (unsigned)lane * 8u, w);
    }
  }
}

__global__ __launch_bounds__(256) void k_attn(char* __restrict__ ws) {
  const int lane = threadIdx.x & 63, wave = threadIdx.x >> 6, ml = lane & 15, g = lane >> 4;
  const int unit = blockIdx.x * 4 + wave, s = unit >> 5, h = unit & 31;
  const u16* QX = (const u16*)(ws + OFF_QX);
  const u16* KX = (const u16*)(ws + OFF_KX);
  const u16* VT = (const u16*)(ws + OFF_VT);
  const u16* KM = (const u16*)(ws + OFF_KM) + (size_t)(s * H + h) * 1024;
  const u16* VM = (const u16*)(ws + OFF_VM) + (size_t)(s * H + h) * 1024;
  u16* O = (u16*)(ws + OFF_O);
  b16x8 qf[2][4], kx[2][4], km[4];
#pragma unroll
  for (int ks = 0; ks < 4; ++ks) {
#pragma unroll
    for (int t = 0; t < 2; ++t) {
      qf[t][ks] = *(const b16x8*)(QX + (size_t)(SEG * s + 16 * t + ml) * HE + h * E + 32 * ks + 8 * g);
      kx[t][ks] = *(const b16x8*)(KX + (size_t)(SEG * s + 16 * t + ml) * HE + h * E + 32 * ks + 8 * g);
    }
    km[ks] = *(const b16x8*)(KM + ((ks * 4 + g) * 8 + (ml & 7)) * 8);
  }
  u32x4 px[2], pm[2];
#pragma unroll
  for (int qt = 0; qt < 2; ++qt) {
    f32x4 s0 = {0, 0, 0, 0}, s1 = {0, 0, 0, 0}, sm = {0, 0, 0, 0};
#pragma unroll
    for (int ks = 0; ks < 4; ++ks) {
      s0 = mfma16(kx[0][ks], qf[qt][ks], s0);
      s1 = mfma16(kx[1][ks], qf[qt][ks], s1);
      sm = mfma16(km[ks], qf[qt][ks], sm);
    }
    const int qi = 16 * qt + ml;
    float sv[12];
#pragma unroll
    for (int r = 0; r < 4; ++r) {
      sv[r] = (4 * g + r <= qi) ? rbf(rbf(s0[r]) * SCALE) : -INFINITY;
      sv[4 + r] = (16 + 4 * g + r <= qi) ? rbf(rbf(s1[r]) * SCALE) : -INFINITY;
      sv[8 + r] = (g < 2) ? rbf(rbf(sm[r]) * SCALE) : -INFINITY;
    }
    float mxv = sv[0];
#pragma unroll
    for (int r = 1; r < 12; ++r) mxv = fmaxf(mxv, sv[r]);
    mxv = fmaxf(mxv, __shfl_xor(mxv, 16)); mxv = fmaxf(mxv, __shfl_xor(mxv, 32));
    float sum = 0.f;
#pragma unroll
    for (int r = 0; r < 12; ++r) { sv[r] = __expf(sv[r] - mxv); sum += sv[r]; }
    sum += __shfl_xor(sum, 16); sum += __shfl_xor(sum, 32);
    const float inv = 1.f / sum;
    px[qt] = (u32x4){pack2(sv[0] * inv, sv[1] * inv), pack2(sv[2] * inv, sv[3] * inv), pack2(sv[4] * inv, sv[5] * inv), pack2(sv[6] * inv, sv[7] * inv)};
    pm[qt] = (u32x4){pack2(sv[8] * inv, sv[9] * inv), pack2(sv[10] * inv, sv[11] * inv), 0u, 0u};
  }
#pragma unroll
  for (int et = 0; et < 8; ++et) {
    u32x2 v0 = *(const u32x2*)(VT + (size_t)(h * E + 16 * et + ml) * S + SEG * s + 4 * g);
    u32x2 v1 = *(const u32x2*)(VT + (size_t)(h * E + 16 * et + ml) * S + SEG * s + 16 + 4 * g);
    u32x2 vmm = *(const u32x2*)(VM + (16 * et + ml) * 8 + (g & 1) * 4);
    u32x4 va = {v0[0], v0[1], v1[0], v1[1]};
    u32x4 vb = {g < 2 ? vmm[0] : 0u, g < 2 ? vmm[1] : 0u, 0u, 0u};
#pragma unroll
    for (int qt = 0; qt < 2; ++qt) {
      f32x4 o = {0, 0, 0, 0};
      o = mfma16(asb(va), asb(px[qt]), o);
      o = mfma16(asb(vb), asb(pm[qt]), o);
      u32x2 w = {pack2(o[0], o[1]), pack2(o[2], o[3])};
      *(u32x2*)(O + (size_t)(SEG * s + 16 * qt + ml) * HE + h * E + 16 * et + 4 * g) = w;
    }
  }
}

__global__ __launch_bounds__(256) void k_final(const u16* __restrict__ Wo, const u16* __restrict__ wd, char* __restrict__ ws, u16* __restrict__ out) {
  __shared__ __attribute__((aligned(16))) char lds[32768];
  const int tid = threadIdx.x, lane = tid & 63, wave = tid >> 6, wr = wave >> 1, wc = wave & 1, fr = lane & 15, g = lane >> 4;
  const int m0 = (blockIdx.x & 15) * 128, n0 = (blockIdx.x >> 4) * 128;
  f32x4 acc[4][4], acc2[4][4];
#pragma unroll
  for (int i = 0; i < 4; ++i)
#pragma unroll
    for (int j = 0; j < 4; ++j) { acc[i][j] = (f32x4){0, 0, 0, 0}; acc2[i][j] = (f32x4){0, 0, 0, 0}; }
  gemm_tile<0, true>((const u16*)(ws + OFF_O), HE, Wo, nullptr, D, HE, m0, n0, lds, acc);
  gemm_tile<0, true>((const u16*)(ws + OFF_HH), F, wd, nullptr, D, F, m0, n0, lds, acc2);
  const float* G = (const float*)(ws + OFF_G);
#pragma unroll
  for (int i = 0; i < 4; ++i) {
    const int t = m0 + wr * 64 + i * 16 + fr;
    const float gg = G[t], og = rbf(1.f - gg);
#pragma unroll
    for (int j = 0; j < 4; ++j) {
      float r[4];
#pragma unroll
      for (int q = 0; q < 4; ++q) r[q] = rbf(gg * rbf(acc[i][j][q])) + rbf(og * rbf(acc2[i][j][q]));
      u32x2 v = {pack2(r[0], r[1]), pack2(r[2], r[3])};
      *(u32x2*)(out + (size_t)t * D + n0 + wc * 64 + j * 16 + 4 * g) = v;
    }
  }
}

extern "C" void kernel_launch(void* const* d_in, const int* in_sizes, int n_in, void* d_out, int out_size, void* d_ws, size_t ws_size,
                              hipStream_t stream) {
  const u16* x = (const u16*)d_in[0];
  const u16* mem0 = (const u16*)d_in[1];
  const u16* Wq = (const u16*)d_in[2];
  const u16* Wk = (const u16*)d_in[3];
  const u16* Wv = (const u16*)d_in[4];
  const u16* Wo = (const u16*)d_in[5];
  const u16* wg = (const u16*)d_in[8];
  const u16* wu = (const u16*)d_in[9];
  const u16* wd = (const u16*)d_in[10];
  const u16* gw = (const u16*)d_in[11];
  char* ws = (char*)d_ws;
  k_pre<<<NB_QKV + NB_GLU + NB_GATE + NB_FILL, 256, 0, stream>>>(x, Wq, Wk, Wv, wg, wu, gw, ws);
  hipFuncSetAttribute((const void*)k_chain, hipFuncAttributeMaxDynamicSharedMemorySize, 96 * 1024);
  k_chain<<<256, 256, 96 * 1024, stream>>>(mem0, Wq, Wk, Wv, Wo, ws);
  k_attn<<<512, 256, 0, stream>>>(ws);
  k_final<<<80, 256, 0, stream>>>(Wo, wd, ws, (u16*)d_out);
}
```

```cpp
#include <hip/hip_runtime.h>
#include <hip/hip_bf16.h>
#include <stdint.h>
#include <stdio.h>

typedef unsigned short u16;
typedef __bf16 b16x8 __attribute__((ext_vector_type(8)));
typedef short s16x4 __attribute__((ext_vector_type(4)));
typedef float f32x4 __attribute__((ext_vector_type(4)));
typedef unsigned u32x4 __attribute__((ext_vector_type(4)));
typedef unsigned u32x2 __attribute__((ext_vector_type(2)));
#define LAS __attribute__((address_space(3)))
#define DI __device__ __forceinline__

constexpr int S = 2048, D = 640, H = 32, E = 128, HE = 4096, MM = 8, SEG = 32, NSEG = 64, F = 1728;
constexpr float SCALE = 0.08838834764831845f;

constexpr size_t OFF_QX = 0;
constexpr size_t OFF_KX = OFF_QX + (size_t)S * HE * 2;
constexpr size_t OFF_VT = OFF_KX + (size_t)S * HE * 2;
constexpr size_t OFF_VX = OFF_VT + (size_t)S * HE * 2;
constexpr size_t OFF_O  = OFF_VX + (size_t)S * HE * 2;
constexpr size_t OFF_HH = OFF_O + (size_t)S * HE * 2;
constexpr size_t OFF_G  = OFF_HH + (size_t)S * F * 2;
constexpr size_t OFF_P  = OFF_G + (size_t)S * 4;
constexpr size_t SZ_P1  = (size_t)H * 640 * 8 * 4;
constexpr size_t SZ_P   = 2 * SZ_P1;
constexpr size_t OFF_SENT = OFF_P;
constexpr size_t OFF_MEM = OFF_P + SZ_P;
constexpr size_t OFF_QM = OFF_MEM + (size_t)NSEG * 10240;
constexpr size_t OFF_KM = OFF_QM + (size_t)NSEG * H * 2048;
constexpr size_t OFF_VM = OFF_KM + (size_t)NSEG * H * 2048;
constexpr size_t OFF_END = OFF_VM + (size_t)NSEG * H * 2048;
constexpr size_t SZ_SENT = OFF_END - OFF_SENT;
constexpr size_t OFF_DBG = OFF_END;

DI float bf2f(u16 v) { return __builtin_bit_cast(float, (unsigned)v << 16); }
DI u16 f2bf(float f) { return __builtin_bit_cast(u16, (__bf16)f); }
DI float rbf(float f) { return (float)(__bf16)f; }
typedef float f32x2 __attribute__((ext_vector_type(2)));
typedef __bf16 b16x2 __attribute__((ext_vector_type(2)));
DI unsigned pack2(float lo, float hi) { f32x2 v = {lo, hi}; return __builtin_bit_cast(unsigned, __builtin_convertvector(v, b16x2)); }
DI f32x4 mfma16(b16x8 a, b16x8 b, f32x4 c) { return __builtin_amdgcn_mfma_f32_16x16x32_bf16(a, b, c, 0, 0, 0); }
DI b16x8 asb(u32x4 v) { return __builtin_bit_cast(b16x8, v); }

DI unsigned offA(unsigned row, unsigned ch) { return 128u * row + 16u * (ch ^ (row & 7)); }
DI unsigned offB(unsigned row, unsigned ch) { return 256u * row + 16u * (ch ^ (((row & 3) << 2) | ((row >> 2) & 3))); }

template <int BMODE, bool SWAP>
DI void gemm_tile(const u16* __restrict__ A, int lda, const u16* __restrict__ B, const u16* __restrict__ B2, int ldb, int K,
                  int m0, int n0, char* lds, f32x4 (&acc)[4][4]) {
  const int tid = threadIdx.x, lane = tid & 63, wave = tid >> 6, wr = wave >> 1, wc = wave & 1;
  const int fr = lane & 15, g = lane >> 4;
  char* sA = lds; char* sB = lds + 16384;
  const unsigned sBbase = (unsigned)(size_t)sB;
  for (int k0 = 0; k0 < K; k0 += 64) {
    u32x4 ra[4], rb[4];
#pragma unroll
    for (int i = 0; i < 4; ++i) {
      int row = (tid >> 3) + 32 * i, ch = tid & 7;
      ra[i] = *(const u32x4*)(A + (size_t)(m0 + row) * lda + k0 + ch * 8);
      int brow = (tid >> 4) + 16 * i, bch = tid & 15;
      if (BMODE == 0) rb[i] = *(const u32x4*)(B + (size_t)(k0 + brow) * ldb + n0 + bch * 8);
      else {
        const u16* src = ((bch >> 2) & 1) ? B2 : B;
        int col = n0 + (bch >> 3) * 32 + (bch & 3) * 8;
        rb[i] = *(const u32x4*)(src + (size_t)(k0 + brow) * ldb + col);
      }
    }
    __syncthreads();
#pragma unroll
    for (int i = 0; i < 4; ++i) {
      int row = (tid >> 3) + 32 * i, ch = tid & 7;
      *(u32x4*)(sA + offA(row, ch)) = ra[i];
      int brow = (tid >> 4) + 16 * i, bch = tid & 15;
      *(u32x4*)(sB + offB(brow, bch)) = rb[i];
    }
    __syncthreads();
#pragma unroll
    for (int ks = 0; ks < 2; ++ks) {
      b16x8 af[4], bfr[4];
#pragma unroll
      for (int i = 0; i < 4; ++i) af[i] = *(const b16x8*)(sA + offA(wr * 64 + i * 16 + fr, ks * 4 + g));
#pragma unroll
      for (int j = 0; j < 4; ++j) {
        const unsigned q = fr >> 2, p = lane & 3, c = wc * 4 + j;
        s16x4 lo = __builtin_amdgcn_ds_read_tr16_b64_v4i16((LAS s16x4*)(sBbase + offB(32 * ks + 8 * g + q, 2 * c + (p >> 1)) + 8 * (p & 1)));
        s16x4 hi = __builtin_amdgcn_ds_read_tr16_b64_v4i16((LAS s16x4*)(sBbase + offB(32 * ks + 8 * g + 4 + q, 2 * c + (p >> 1)) + 8 * (p & 1)));
        typedef short s16x8 __attribute__((ext_vector_type(8)));
        s16x8 t = {lo[0], lo[1], lo[2], lo[3], hi[0], hi[1], hi[2], hi[3]};
        bfr[j] = __builtin_bit_cast(b16x8, t);
      }
#pragma unroll
      for (int i = 0; i < 4; ++i)
#pragma unroll
        for (int j = 0; j < 4; ++j)
          acc[i][j] = SWAP ? mfma16(bfr[j], af[i], acc[i][j]) : mfma16(af[i], bfr[j], acc[i][j]);
    }
  }
}

DI float gelu_tanh(float x) {
  float y = 0.7978845608028654f * (x + 0.044715f * x * x * x);
  float t = 1.f - 2.f / (__expf(2.f * y) + 1.f);
  return 0.5f * x * (1.f + t);
}

#define RBAR() asm volatile("s_waitcnt lgkmcnt(0)\n\ts_barrier" ::: "memory")
DI unsigned fB(unsigned row) { return (row & 3) | (((row >> 3) & 1) << 2); }
template <int BMODE>
DI void gemm256(const u16* __restrict__ A, int lda, const u16* __restrict__ B0, const u16* __restrict__ B1, int ldb, int ncols, int K,
                int m0, int n0, char* lds, f32x4 (&acc)[8][4]) {
  const int tid = threadIdx.x, lane = tid & 63, wave = __builtin_amdgcn_readfirstlane(tid >> 6), wr = wave >> 2, wc = wave & 3;
  const int fr = lane & 15, g = lane >> 4;
  unsigned offA[4], offB[2];
#pragma unroll
  for (int i = 0; i < 4; ++i) {
    const unsigned L = (unsigned)(i * 8 + wave) * 1024u + lane * 16u;
    const unsigned row = L >> 7, ch = (L >> 4) & 7; offA[i] = (m0 + row) * lda + ((ch ^ (row & 7)) * 8);
  }
#pragma unroll
  for (int i = 0; i < 2; ++i) {
    const unsigned L = (unsigned)(i * 8 + wave) * 1024u + lane * 16u;
    const unsigned row = L >> 8, chp = (L >> 4) & 15, ch = chp ^ (fB(row) << 1);
    int col = n0 + ch * 8; if (BMODE == 1) col = col < ncols ? col : ncols - 8;
    offB[i] = row * ldb + col;
  }
  const u16* Bs1 = BMODE == 0 ? B0 + 128 : B1;
  auto stage = [&](int buf, int k0) {
    const u16* Ak = A + k0; const u16* B0k = B0 + (size_t)k0 * ldb; const u16* B1k = Bs1 + (size_t)k0 * ldb;
#pragma unroll
    for (int i = 0; i < 4; ++i) {
      __builtin_amdgcn_global_load_lds((const unsigned*)(Ak + offA[i]), (LAS unsigned*)(lds + buf * 65536 + (i * 8 + wave) * 1024), 16, 0, 0);
      __builtin_amdgcn_global_load_lds((const unsigned*)((i < 2 ? B0k : B1k) + offB[i & 1]), (LAS unsigned*)(lds + buf * 65536 + 32768 + (i * 8 + wave) * 1024), 16, 0, 0);
    }
  };
  const unsigned ldsb = (unsigned)(size_t)lds;
  const unsigned q4 = fr >> 2, p4 = lane & 3;
  const int nt = K / 64;
  stage(0, 0);
  asm volatile("s_waitcnt vmcnt(0)" ::: "memory"); RBAR();
  if (wr == 1) RBAR();
  for (int t = 0; t < nt; ++t) {
    const int buf = t & 1;
    stage(buf ^ 1, t + 1 < nt ? (t + 1) * 64 : 0);
    const char* sA = lds + buf * 65536;
    const unsigned sB = ldsb + buf * 65536 + 32768;
#pragma unroll
    for (int ks = 0; ks < 2; ++ks) {
      b16x8 af[8], bfr[4];
#pragma unroll
      for (int jn = 0; jn < 4; ++jn) {
        const unsigned sub = BMODE == 0 ? (wc >> 1) : (jn >> 1), c = BMODE == 0 ? ((wc & 1) * 4 + jn) : (2 * wc + (jn & 1));
        const unsigned r0 = 32 * ks + 8 * g + q4, r1 = r0 + 4;
        s16x4 lo = __builtin_amdgcn_ds_read_tr16_b64_v4i16((LAS s16x4*)(sB + sub * 16384 + r0 * 256 + (((2 * c + (p4 >> 1)) ^ (fB(r0) << 1)) << 4) + 8 * (p4 & 1)));
        s16x4 hi = __builtin_amdgcn_ds_read_tr16_b64_v4i16((LAS s16x4*)(sB + sub * 16384 + r1 * 256 + (((2 * c + (p4 >> 1)) ^ (fB(r1) << 1)) << 4) + 8 * (p4 & 1)));
        typedef short s16x8 __attribute__((ext_vector_type(8)));
        s16x8 tt = {lo[0], lo[1], lo[2], lo[3], hi[0], hi[1], hi[2], hi[3]};
        bfr[jn] = __builtin_bit_cast(b16x8, tt);
      }
#pragma unroll
      for (int i = 0; i < 8; ++i) { const unsigned row = wr * 128 + i * 16 + fr; af[i] = *(const b16x8*)(sA + row * 128 + (((ks * 4 + g) ^ (row & 7)) << 4)); }
      if (ks == 1 && wr == 1) asm volatile("s_waitcnt vmcnt(0)" ::: "memory");
      RBAR();
      __builtin_amdgcn_s_setprio(1);
#pragma unroll
      for (int i = 0; i < 8; ++i)
#pragma unroll
        for (int jn = 0; jn < 4; ++jn) acc[i][jn] = mfma16(bfr[jn], af[i], acc[i][jn]);
      __builtin_amdgcn_s_setprio(0);
      if (ks == 1 && wr == 0) asm volatile("s_waitcnt vmcnt(0)" ::: "memory");
      RBAR();
    }
  }
  if (wr == 0) RBAR();
}

constexpr int NT_QKV = 8 * 48, NT_GLU = 8 * 14, NB_MISC = 64;
__global__ __launch_bounds__(512, 1) void k_pre(const u16* __restrict__ x, const u16* __restrict__ Wq, const u16* __restrict__ Wk,
                                                const u16* __restrict__ Wv, const u16* __restrict__ wg, const u16* __restrict__ wu,
                                                const u16* __restrict__ gate_w, char* __restrict__ ws) {
  extern __shared__ __attribute__((aligned(16))) char lds[];
  int b = blockIdx.x;
  const int tid = threadIdx.x, lane = tid & 63, wave = tid >> 6, wr = wave >> 2, wc = wave & 3, fr = lane & 15, g = lane >> 4;
  {
    constexpr int NB = NT_QKV + NT_GLU;
    for (int t = b * 8 + wave; t < S; t += NB * 8) {
      float s = 0.f;
#pragma unroll
      for (int i = 0; i < 5; ++i) {
        const unsigned xv = *(const unsigned*)(x + (size_t)t * D + i * 128 + lane * 2), gv = *(const unsigned*)(gate_w + i * 128 + lane * 2);
        s += rbf(__uint_as_float(xv << 16) * __uint_as_float(gv << 16)) + rbf(__uint_as_float(xv & 0xFFFF0000u) * __uint_as_float(gv & 0xFFFF0000u));
      }
#pragma unroll
      for (int o = 32; o >= 1; o >>= 1) s += __shfl_xor(s, o);
      s = rbf(s);
      float gg = rbf(1.f / (1.f + __expf(-s)));
      if (lane == 0) ((float*)(ws + OFF_G))[t] = gg;
    }
    u32x4 ones = {0xFFFFFFFFu, 0xFFFFFFFFu, 0xFFFFFFFFu, 0xFFFFFFFFu};
    for (size_t i = (size_t)b * 512 + tid; i < SZ_SENT / 16; i += (size_t)NB * 512) *(u32x4*)(ws + OFF_SENT + i * 16) = ones;
  }
  {
    const int xc = b & 7, idx = b >> 3;
    int ntile, mt;
    if (idx < 56) { ntile = (idx >> 3) * 8 + xc; mt = idx & 7; }
    else { const int r = xc * 6 + (idx - 56); ntile = 56 + (r >> 3); mt = r & 7; }
    b = ntile * 8 + mt;
  }
  f32x4 acc[8][4];
#pragma unroll
  for (int i = 0; i < 8; ++i)
#pragma unroll
    for (int j = 0; j < 4; ++j) acc[i][j] = (f32x4){0, 0, 0, 0};
  if (b < NT_QKV) {
    const int mt = b & 7, nt = b >> 3, which = nt >> 4, n0 = (nt & 15) * 256, m0 = mt * 256;
    gemm256<0>(x, D, which == 0 ? Wq : (which == 1 ? Wk : Wv), nullptr, HE, HE, D, m0, n0, lds, acc);
    char* out = ws + (which == 0 ? OFF_QX : (which == 1 ? OFF_KX : OFF_VX));
    int tz = threadIdx.x; asm volatile("" : "+v"(tz));
    const int wr = tz >> 8, wc = (tz >> 6) & 3, fr = tz & 15, g = (tz >> 4) & 3;
#pragma unroll
    for (int i = 0; i < 8; ++i)
#pragma unroll
      for (int j = 0; j < 4; ++j) {
        u32x2 v = {pack2(acc[i][j][0], acc[i][j][1]), pack2(acc[i][j][2], acc[i][j][3])};
        const int tok = m0 + wr * 128 + i * 16 + fr, n = n0 + wc * 64 + j * 16 + 4 * g;
        const int seg = tok >> 5, kt = (tok >> 4) & 1, hh = n >> 7, e = n & 127;
        *(u32x2*)(out + (size_t)(seg * H + hh) * 8192 + (size_t)(((kt * 4 + (e >> 5)) * 64 + (tok & 15) + 16 * ((e & 31) >> 3)) * 16 + (e & 4) * 2)) = v;
      }
    return;
  }
  b -= NT_QKV;
  {
    const int mt = b & 7, ft = b >> 3, f0 = ft * 128, m0 = mt * 256;
    gemm256<1>(x, D, wg, wu, F, F, D, m0, f0, lds, acc);
    u16* out = (u16*)(ws + OFF_HH);
    int tz = threadIdx.x; asm volatile("" : "+v"(tz));
    const int wr = tz >> 8, wc = (tz >> 6) & 3, fr = tz & 15, g = (tz >> 4) & 3;
    if (f0 + wc * 32 < F) {
#pragma unroll
      for (int i = 0; i < 8; ++i)
#pragma unroll
        for (int j = 0; j < 2; ++j) {
          float hv[4];
#pragma unroll
          for (int r = 0; r < 4; ++r) {
            float gg = rbf(acc[i][j][r]), uu = rbf(acc[i][j + 2][r]);
            hv[r] = rbf(gelu_tanh(gg)) * uu;
          }
          u32x2 v = {pack2(hv[0], hv[1]), pack2(hv[2], hv[3])};
          *(u32x2*)(out + (size_t)(m0 + wr * 128 + i * 16 + fr) * F + f0 + wc * 32 + j * 16 + 4 * g) = v;
        }
    }
  }
}

#define SC1 16
DI u32x4 ld128(__amdgpu_buffer_rsrc_t rs, unsigned off) { return __builtin_amdgcn_raw_buffer_load_b128(rs, off, 0, SC1); }
DI u32x2 ld64(__amdgpu_buffer_rsrc_t rs, unsigned off) { return __builtin_amdgcn_raw_buffer_load_b64(rs, off, 0, SC1); }
DI void st128(__amdgpu_buffer_rsrc_t rs, unsigned off, u32x4 v) { __builtin_amdgcn_raw_buffer_store_b128(v, rs, off, 0, SC1); }
DI void st64(__amdgpu_buffer_rsrc_t rs, unsigned off, u32x2 v) { __builtin_amdgcn_raw_buffer_store_b64(v, rs, off, 0, SC1); }
DI unsigned nosent(unsigned v) { return v == 0xFFFFFFFFu ? 0xFFFEFFFFu : v; }
DI unsigned umax3(unsigned a, unsigned b, unsigned c) { return max(max(a, b), c); }
DI float xsum16(float x) { unsigned u = __float_as_uint(x); auto r = __builtin_amdgcn_permlane16_swap(u, u, false, false); return __uint_as_float(r[0]) + __uint_as_float(r[1]); }
DI float xsum32(float x) { unsigned u = __float_as_uint(x); auto r = __builtin_amdgcn_permlane32_swap(u, u, false, false); return __uint_as_float(r[0]) + __uint_as_float(r[1]); }
DI float xmax16(float x) { unsigned u = __float_as_uint(x); auto r = __builtin_amdgcn_permlane16_swap(u, u, false, false); return fmaxf(__uint_as_float(r[0]), __uint_as_float(r[1])); }
DI float xmax32(float x) { unsigned u = __float_as_uint(x); auto r = __builtin_amdgcn_permlane32_swap(u, u, false, false); return fmaxf(__uint_as_float(r[0]), __uint_as_float(r[1])); }
DI float ror4(float x) { return __uint_as_float(__builtin_amdgcn_update_dpp(0u, __float_as_uint(x), 0x124, 0xf, 0xf, false)); }
DI float ror8(float x) { return __uint_as_float(__builtin_amdgcn_update_dpp(0u, __float_as_uint(x), 0x128, 0xf, 0xf, false)); }
#define LBAR() asm volatile("s_waitcnt lgkmcnt(0)\n\ts_barrier" ::: "memory")
template <int N> DI void slp() { if constexpr (N > 0) { __builtin_amdgcn_s_sleep(N > 15 ? 15 : N); if constexpr (N > 15) slp<N - 15>(); } }

#define TS(i)
#define SPN(k)
constexpr unsigned SPIN_CAP = 1u << 17;
#define SLP_A 18
#define SLP_B 10
#define SLP_B3 4
#define SLP_C 11

__global__ __launch_bounds__(320, 1) void k_chain(const u16* __restrict__ mem0, const u16* __restrict__ Wq, const u16* __restrict__ Wk,
                                                   const u16* __restrict__ Wv, const u16* __restrict__ Wo, char* __restrict__ ws) {
  extern __shared__ __attribute__((aligned(16))) char dyn_lds[];
  const int b = blockIdx.x, xcd = b & 7, y = b >> 3, h = xcd * 4 + (y >> 3), j = y & 7;
  const int tid = threadIdx.x, lane = tid & 63, wave = __builtin_amdgcn_readfirstlane(tid >> 6);
  const int ml = lane & 15, g = lane >> 4;
  const __amdgpu_buffer_rsrc_t rs = __builtin_amdgcn_make_buffer_rsrc(ws + OFF_P, 0, (int)(OFF_END - OFF_P), 0x00020000);
  constexpr unsigned R_P = 0, R_MEM = (unsigned)(OFF_MEM - OFF_P), R_QM = (unsigned)(OFF_QM - OFF_P);
  f32x4* X1 = (f32x4*)dyn_lds;
  char* KV = dyn_lds + 16384;
  const f32x4 z4 = {0, 0, 0, 0};
  auto fetch_kv = [&](int sn) {
    const char* kb = ws + OFF_KX + (size_t)(sn * H + h) * 8192 + lane * 16;
    const char* vb = ws + OFF_VX + (size_t)(sn * H + h) * 8192 + lane * 16;
    u32x4 t[16];
#pragma unroll
    for (int i = 0; i < 8; ++i) { t[i] = *(const u32x4*)(kb + i * 1024); t[8 + i] = *(const u32x4*)(vb + i * 1024); }
#pragma unroll
    for (int i = 0; i < 16; ++i) *(u32x4*)(KV + (sn & 1) * 16384 + i * 1024 + lane * 16) = t[i];
  };

  unsigned spin_cap = SPIN_CAP;
  if (wave == 4) {
    const int cc = lane & 7, hg = lane >> 3;
    fetch_kv(0); fetch_kv(1);
    LBAR();
    for (int s = 0; s < NSEG - 1; ++s) {
      LBAR();
      LBAR();
      const unsigned off = cc < 5 ? R_P + (unsigned)(s & 1) * (unsigned)SZ_P1 + (unsigned)hg * 81920u + (unsigned)(5 * b + cc) * 16u : 0x7FFFFF00u;
      slp<SLP_C>();
      u32x4 v[4];
      for (unsigned spin = 0;; ++spin) {
        asm volatile("" ::: "memory");
#pragma unroll
        for (int i = 0; i < 4; ++i) v[i] = ld128(rs, off + i * 20480u);
        unsigned mx = 0;
#pragma unroll
        for (int i = 0; i < 4; ++i) { mx = umax3(mx, v[i][0], v[i][1]); mx = umax3(mx, v[i][2], v[i][3]); }
        if (__all(mx != 0xFFFFFFFFu) || spin >= spin_cap) { SPN(2); if (spin >= spin_cap) spin_cap = 0; break; }
      }
      TS(5);
      float f[4];
#pragma unroll
      for (int r = 0; r < 4; ++r) {
        float x = (__uint_as_float(v[0][r]) + __uint_as_float(v[1][r])) + (__uint_as_float(v[2][r]) + __uint_as_float(v[3][r]));
        x += ror8(x); x = xsum32(xsum16(x)); f[r] = x;
      }
      u32x2 w = {nosent(pack2(f[0], f[1])), nosent(pack2(f[2], f[3]))};
      if (lane < 5) st64(rs, R_MEM + (unsigned)(s + 1) * 10240u + (unsigned)(5 * b + lane) * 8u, w);
      TS(6);
      LBAR();
      const u32x4 ones = {0xFFFFFFFFu, 0xFFFFFFFFu, 0xFFFFFFFFu, 0xFFFFFFFFu};
      if (cc < 5) {
#pragma unroll
        for (int i = 0; i < 4; ++i) st128(rs, off + i * 20480u, ones);
      }
      if (s + 2 < NSEG - 1) fetch_kv(s + 2);
    }
    LBAR();
    return;
  }
  b16x8 w1[3][5], w2[2][4];
  {
    char* WL = dyn_lds + 49152;
    char* my = WL + wave * 24576;
    u32x4 t1[15], t2[8];
#pragma unroll
    for (int t = 0; t < 3; ++t) {
      const u16* W = t == 0 ? Wq : (t == 1 ? Wk : Wv);
#pragma unroll
      for (int i = 0; i < 5; ++i) t1[t * 5 + i] = *(const u32x4*)(W + (size_t)(160 * wave + 32 * i + (lane >> 1)) * HE + h * E + 16 * j + (lane & 1) * 8);
    }
#pragma unroll
    for (int t = 0; t < 2; ++t) {
      const int ct = t == 0 ? wave : 4;
#pragma unroll
      for (int i = 0; i < 4; ++i) t2[t * 4 + i] = *(const u32x4*)(Wo + (size_t)(h * E + 32 * i + (lane >> 1)) * D + 80 * j + 16 * ct + (lane & 1) * 8);
    }
#pragma unroll
    for (int i = 0; i < 15; ++i) *(u32x4*)(my + i * 1024 + lane * 16) = t1[i];
#pragma unroll
    for (int i = 0; i < 8; ++i) *(u32x4*)(my + 15360 + i * 1024 + lane * 16) = t2[i];
    asm volatile("s_waitcnt lgkmcnt(0)" ::: "memory");
    const unsigned base = (unsigned)(size_t)my + (8 * g + (ml >> 2)) * 32 + (lane & 3) * 8;
    typedef short s16x8 __attribute__((ext_vector_type(8)));
#pragma unroll
    for (int i = 0; i < 23; ++i) {
      s16x4 lo = __builtin_amdgcn_ds_read_tr16_b64_v4i16((LAS s16x4*)(base + i * 1024));
      s16x4 hi = __builtin_amdgcn_ds_read_tr16_b64_v4i16((LAS s16x4*)(base + i * 1024 + 128));
      s16x8 tt = {lo[0], lo[1], lo[2], lo[3], hi[0], hi[1], hi[2], hi[3]};
      if (i < 15) w1[i / 5][i % 5] = __builtin_bit_cast(b16x8, tt); else w2[(i - 15) / 4][(i - 15) % 4] = __builtin_bit_cast(b16x8, tt);
    }
  }

  b16x8 kx[2][4]; u32x4 vwx[2];
  auto prep = [&](int sn) {
    const char* kb = KV + (sn & 1) * 16384 + lane * 16;
    b16x8 vf[2][4];
#pragma unroll
    for (int kt = 0; kt < 2; ++kt)
#pragma unroll
      for (int ks = 0; ks < 4; ++ks) { kx[kt][ks] = *(const b16x8*)(kb + (kt * 4 + ks) * 1024); vf[kt][ks] = *(const b16x8*)(kb + 8192 + (kt * 4 + ks) * 1024); }
#pragma unroll
    for (int t = 0; t < 2; ++t) {
      if (t == 1 && wave != 0) break;
      f32x4 d0 = z4, d1 = z4;
#pragma unroll
      for (int ks = 0; ks < 4; ++ks) { d0 = mfma16(vf[0][ks], w2[t][ks], d0); d1 = mfma16(vf[1][ks], w2[t][ks], d1); }
      vwx[t] = (u32x4){pack2(d0[0], d0[1]), pack2(d0[2], d0[3]), pack2(d1[0], d1[1]), pack2(d1[2], d1[3])};
    }
  };
  LBAR();
  prep(0);
  const unsigned OOB = 0x7FFFFF00u;
  const unsigned lo_off = ml < 8 ? (unsigned)(g * 8 + ml) * 16u : OOB;
  for (int s = 0; s < NSEG; ++s) {
    TS(0);
    {
      f32x4 aq, ak, av;
      if (s == 0) {
        aq = ak = av = z4;
#pragma unroll
        for (int kk = 0; kk < 5; ++kk) {
          u32x4 mf = *(const u32x4*)(mem0 + (ml & 7) * D + 32 * (5 * wave + kk) + 8 * g);
          aq = mfma16(w1[0][kk], asb(mf), aq); ak = mfma16(w1[1][kk], asb(mf), ak); av = mfma16(w1[2][kk], asb(mf), av);
        }
      } else {
        const unsigned base = R_MEM + (unsigned)s * 10240u + (unsigned)wave * 2560u + lo_off;
        slp<SLP_A>();
        for (unsigned spin = 0;; ++spin) {
          asm volatile("" ::: "memory");
          u32x4 mf[5];
#pragma unroll
          for (int kk = 0; kk < 5; ++kk) mf[kk] = ld128(rs, base + kk * 512u);
          __builtin_amdgcn_sched_barrier(0);
          unsigned mx = 0;
          aq = ak = av = z4;
#pragma unroll
          for (int kk = 0; kk < 5; ++kk) {
            aq = mfma16(w1[0][kk], asb(mf[kk]), aq); ak = mfma16(w1[1][kk], asb(mf[kk]), ak); av = mfma16(w1[2][kk], asb(mf[kk]), av);
            mx = umax3(mx, mf[kk][0], mf[kk][2]);
          }
          if (__all(mx != 0xFFFFFFFFu) || spin >= spin_cap) { SPN(0); if (spin >= spin_cap) spin_cap = 0; break; }
        }
      }
      TS(1);
      X1[0 * 256 + tid] = aq; X1[1 * 256 + tid] = ak; X1[2 * 256 + tid] = av;
      LBAR();
      if (wave < 3) {
        f32x4 a = (X1[wave * 256 + lane] + X1[wave * 256 + 64 + lane]) + (X1[wave * 256 + 128 + lane] + X1[wave * 256 + 192 + lane]);
        u32x2 v = {nosent(pack2(a[0], a[1])), nosent(pack2(a[2], a[3]))};
        unsigned off = R_QM + (unsigned)(s * H + h) * 6144u + (unsigned)wave * 2048u + (unsigned)(((j >> 1) * 4 + 2 * (j & 1) + (g >> 1)) * 8 + ml) * 16u + (g & 1) * 8u;
        if (ml < 8) st64(rs, off, v);
      }
    }
    TS(2);
    if (s == NSEG - 1) break;
    {
      const unsigned hb = R_QM + (unsigned)(s * H + h) * 6144u + lo_off;
      if (wave < 3) slp<SLP_B>(); else slp<SLP_B + SLP_B3>();
      f32x4 a0, a1;
      for (unsigned spin = 0;; ++spin) {
        asm volatile("" ::: "memory");
        u32x4 qf[4], kf[4], vf[4];
#pragma unroll
        for (int ks = 0; ks < 4; ++ks) qf[ks] = ld128(rs, hb + ks * 512u);
        __builtin_amdgcn_sched_barrier(0);
#pragma unroll
        for (int ks = 0; ks < 4; ++ks) kf[ks] = ld128(rs, hb + 2048u + ks * 512u);
        __builtin_amdgcn_sched_barrier(0);
#pragma unroll
        for (int ks = 0; ks < 4; ++ks) vf[ks] = ld128(rs, hb + 4096u + ks * 512u);
        __builtin_amdgcn_sched_barrier(0);
        f32x4 s0 = z4, s1 = z4, sm = z4, dm0 = z4, dm1 = z4;
#pragma unroll
        for (int ks = 0; ks < 4; ++ks) { s0 = mfma16(kx[0][ks], asb(qf[ks]), s0); s1 = mfma16(kx[1][ks], asb(qf[ks]), s1); }
#pragma unroll
        for (int ks = 0; ks < 4; ++ks) sm = mfma16(asb(kf[ks]), asb(qf[ks]), sm);
        __builtin_amdgcn_sched_barrier(0);
        TS(3);
#pragma unroll
        for (int ks = 0; ks < 4; ++ks) dm0 = mfma16(asb(vf[ks]), w2[0][ks], dm0);
        if (wave == 0) {
#pragma unroll
          for (int ks = 0; ks < 4; ++ks) dm1 = mfma16(asb(vf[ks]), w2[1][ks], dm1);
        }
        float sv[12];
        const float NEG = -INFINITY, PINF = INFINITY;
#pragma unroll
        for (int r = 0; r < 4; ++r) { sv[r] = s0[r]; sv[4 + r] = s1[r]; sv[8 + r] = (g < 2) ? sm[r] : NEG; }
        const float c2 = SCALE * 1.4426950408889634f;
#define FMX(a, b) __builtin_amdgcn_fmed3f((a), (b), PINF)
        float mxv = FMX(FMX(FMX(sv[0], sv[1]), FMX(sv[2], sv[3])), FMX(FMX(sv[4], sv[5]), FMX(sv[6], sv[7])));
        mxv = FMX(mxv, FMX(FMX(sv[8], sv[9]), FMX(sv[10], sv[11])));
        { unsigned u = __float_as_uint(mxv); auto r = __builtin_amdgcn_permlane16_swap(u, u, false, false); mxv = FMX(__uint_as_float(r[0]), __uint_as_float(r[1])); }
        { unsigned u = __float_as_uint(mxv); auto r = __builtin_amdgcn_permlane32_swap(u, u, false, false); mxv = FMX(__uint_as_float(r[0]), __uint_as_float(r[1])); }
        const float mc = -mxv * c2;
#pragma unroll
        for (int r = 0; r < 12; ++r) sv[r] = __builtin_amdgcn_exp2f(fmaf(sv[r], c2, mc));
        u32x4 px = {pack2(sv[0], sv[1]), pack2(sv[2], sv[3]), pack2(sv[4], sv[5]), pack2(sv[6], sv[7])};
        u32x4 pm = {pack2(sv[8], sv[9]), pack2(sv[10], sv[11]), 0u, 0u};
        {
          u32x4 vwm = {pack2(dm0[0], dm0[1]), pack2(dm0[2], dm0[3]), 0u, 0u};
          a0 = mfma16(asb(vwx[0]), asb(px), z4);
          a0 = mfma16(asb(vwm), asb(pm), a0);
        }
        if (wave == 0) {
          u32x4 vwm = {pack2(dm1[0], dm1[1]), pack2(dm1[2], dm1[3]), 0u, 0u};
          a1 = mfma16(asb(vwx[1]), asb(px), z4);
          a1 = mfma16(asb(vwm), asb(pm), a1);
        }
        __builtin_amdgcn_sched_barrier(0);
        float sum = ((sv[0] + sv[1]) + (sv[2] + sv[3])) + ((sv[4] + sv[5]) + (sv[6] + sv[7])) + ((sv[8] + sv[9]) + (sv[10] + sv[11]));
        sum = xsum32(xsum16(sum));
        const float inv = __builtin_amdgcn_rcpf(sum);
        unsigned mx = 0;
#pragma unroll
        for (int ks = 0; ks < 4; ++ks) { mx = umax3(mx, qf[ks][0], qf[ks][2]); mx = umax3(mx, kf[ks][0], kf[ks][2]); mx = umax3(mx, vf[ks][0], vf[ks][2]); }
        a0 *= inv; a1 *= inv;
        if (__all(mx != 0xFFFFFFFFu) || spin >= spin_cap) { SPN(1); if (spin >= spin_cap) spin_cap = 0; break; }
      }
#pragma unroll
      for (int t = 0; t < 2; ++t) {
        if (t == 1 && wave != 0) break;
        const f32x4 a = t == 0 ? a0 : a1;
        u32x4 v;
#pragma unroll
        for (int r = 0; r < 4; ++r) v[r] = min(__float_as_uint(a[r]), 0xFFFFFFFEu);
        const int T = 5 * j + (t == 0 ? wave : 4);
        unsigned off = R_P + (unsigned)(s & 1) * (unsigned)SZ_P1 + (unsigned)h * 20480u + (unsigned)(((T >> 1) * 4 + 2 * (T & 1) + (g >> 1)) * 8 + ml) * 32u + (unsigned)(g & 1) * 16u;
        if (ml < 8) st128(rs, off, v);
      }
    }
    TS(4);
    LBAR();
    if (s + 1 < NSEG - 1) prep(s + 1);
    LBAR();
  }
}

__global__ __launch_bounds__(256) void k_attn(char* __restrict__ ws) {
  __shared__ __attribute__((aligned(16))) char vl[4][48 * 272];
  const int lane = threadIdx.x & 63, wave = threadIdx.x >> 6, ml = lane & 15, g = lane >> 4;
  const int unit = blockIdx.x * 4 + wave, s = unit >> 5, h = unit & 31;
  const char* QX = ws + OFF_QX + (size_t)(s * H + h) * 8192 + lane * 16;
  const char* KX = ws + OFF_KX + (size_t)(s * H + h) * 8192 + lane * 16;
  const char* VX = ws + OFF_VX + (size_t)(s * H + h) * 8192 + lane * 16;
  const char* KM = ws + OFF_QM + (size_t)(s * H + h) * 6144 + 2048 + (g * 8 + (ml & 7)) * 16;
  u16* O = (u16*)(ws + OFF_O);
  char* V = vl[wave];
  b16x8 qf[2][4], kx[2][4], km[4];
#pragma unroll
  for (int ks = 0; ks < 4; ++ks) {
#pragma unroll
    for (int t = 0; t < 2; ++t) {
      qf[t][ks] = *(const b16x8*)(QX + (t * 4 + ks) * 1024);
      kx[t][ks] = *(const b16x8*)(KX + (t * 4 + ks) * 1024);
      *(u32x4*)(V + (16 * t + ml) * 272 + (32 * ks + 8 * g) * 2) = *(const u32x4*)(VX + (t * 4 + ks) * 1024);
    }
    km[ks] = *(const b16x8*)(KM + ks * 512);
    u32x4 vmr = *(const u32x4*)(KM + 2048 + ks * 512);
    if (ml >= 8) vmr = (u32x4){0, 0, 0, 0};
    *(u32x4*)(V + (32 + ml) * 272 + (32 * ks + 8 * g) * 2) = vmr;
  }
  u32x4 px[2], pm[2];
#pragma unroll
  for (int qt = 0; qt < 2; ++qt) {
    f32x4 s0 = {0, 0, 0, 0}, s1 = {0, 0, 0, 0}, sm = {0, 0, 0, 0};
#pragma unroll
    for (int ks = 0; ks < 4; ++ks) {
      s0 = mfma16(kx[0][ks], qf[qt][ks], s0);
      s1 = mfma16(kx[1][ks], qf[qt][ks], s1);
      sm = mfma16(km[ks], qf[qt][ks], sm);
    }
    const int qi = 16 * qt + ml;
    float sv[12];
#pragma unroll
    for (int r = 0; r < 4; ++r) {
      sv[r] = (4 * g + r <= qi) ? rbf(rbf(s0[r]) * SCALE) : -INFINITY;
      sv[4 + r] = (16 + 4 * g + r <= qi) ? rbf(rbf(s1[r]) * SCALE) : -INFINITY;
      sv[8 + r] = (g < 2) ? rbf(rbf(sm[r]) * SCALE) : -INFINITY;
    }
    float mxv = sv[0];
#pragma unroll
    for (int r = 1; r < 12; ++r) mxv = fmaxf(mxv, sv[r]);
    mxv = xmax32(xmax16(mxv));
    float sum = 0.f;
#pragma unroll
    for (int r = 0; r < 12; ++r) { sv[r] = __expf(sv[r] - mxv); sum += sv[r]; }
    sum = xsum32(xsum16(sum));
    const float inv = 1.f / sum;
    px[qt] = (u32x4){pack2(sv[0] * inv, sv[1] * inv), pack2(sv[2] * inv, sv[3] * inv), pack2(sv[4] * inv, sv[5] * inv), pack2(sv[6] * inv, sv[7] * inv)};
    pm[qt] = (u32x4){pack2(sv[8] * inv, sv[9] * inv), pack2(sv[10] * inv, sv[11] * inv), 0u, 0u};
  }
  asm volatile("s_waitcnt lgkmcnt(0)" ::: "memory");
  const unsigned vb = (unsigned)(size_t)V + (4 * g + (ml >> 2)) * 272 + (lane & 3) * 8;
#pragma unroll
  for (int et = 0; et < 8; ++et) {
    s16x4 v0 = __builtin_amdgcn_ds_read_tr16_b64_v4i16((LAS s16x4*)(vb + et * 32));
    s16x4 v1 = __builtin_amdgcn_ds_read_tr16_b64_v4i16((LAS s16x4*)(vb + 16 * 272 + et * 32));
    s16x4 v2 = __builtin_amdgcn_ds_read_tr16_b64_v4i16((LAS s16x4*)(vb + 32 * 272 + et * 32));
    typedef short s16x8 __attribute__((ext_vector_type(8)));
    s16x8 va = {v0[0], v0[1], v0[2], v0[3], v1[0], v1[1], v1[2], v1[3]};
    s16x8 vbm = {v2[0], v2[1], v2[2], v2[3], 0, 0, 0, 0};
#pragma unroll
    for (int qt = 0; qt < 2; ++qt) {
      f32x4 o = {0, 0, 0, 0};
      o = mfma16(__builtin_bit_cast(b16x8, va), asb(px[qt]), o);
      o = mfma16(__builtin_bit_cast(b16x8, vbm), asb(pm[qt]), o);
      u32x2 w = {pack2(o[0], o[1]), pack2(o[2], o[3])};
      *(u32x2*)(O + (size_t)(SEG * s + 16 * qt + ml) * HE + h * E + 16 * et + 4 * g) = w;
    }
  }
}

__global__ __launch_bounds__(512, 1) void k_final(const u16* __restrict__ Wo, const u16* __restrict__ wd, char* __restrict__ ws) {
  extern __shared__ __attribute__((aligned(16))) char lds[];
  const int tid = threadIdx.x, lane = tid & 63, wave = __builtin_amdgcn_readfirstlane(tid >> 6), kgp = wave >> 2, wr = (wave >> 1) & 1, wc = wave & 1;
  const int fr = lane & 15, g = lane >> 4;
  const int b = blockIdx.x, grp = (b & 7) * 6 + (b >> 3) / 5, part = grp % 3, m0 = (grp / 3) * 128, n0 = ((b >> 3) % 5) * 128;
  const u16* A; const u16* B; int lda, nt;
  if (part < 2) { A = (const u16*)(ws + OFF_O) + part * 2048; lda = HE; B = Wo + (size_t)part * 2048 * D; nt = 32; }
  else { A = (const u16*)(ws + OFF_HH); lda = F; B = wd; nt = 27; }
  const u16* srcA[2]; const u16* srcB[2];
#pragma unroll
  for (int i = 0; i < 2; ++i) {
    const unsigned L = (unsigned)(i * 8 + wave) * 1024u + lane * 16u;
    { const unsigned row = L >> 7, ch = (L >> 4) & 7; srcA[i] = A + (size_t)(m0 + row) * lda + ((ch ^ (row & 7)) * 8); }
    { const unsigned row = L >> 8, chp = (L >> 4) & 15, ch = chp ^ (fB(row) << 1); srcB[i] = B + (size_t)row * D + n0 + ch * 8; }
  }
  auto stage = [&](int buf, int k0) {
#pragma unroll
    for (int i = 0; i < 2; ++i) {
      __builtin_amdgcn_global_load_lds((const unsigned*)(srcA[i] + k0), (LAS unsigned*)(lds + buf * 32768 + (i * 8 + wave) * 1024), 16, 0, 0);
      __builtin_amdgcn_global_load_lds((const unsigned*)(srcB[i] + (size_t)k0 * D), (LAS unsigned*)(lds + buf * 32768 + 16384 + (i * 8 + wave) * 1024), 16, 0, 0);
    }
  };
  f32x4 acc[4][4];
#pragma unroll
  for (int i = 0; i < 4; ++i)
#pragma unroll
    for (int jn = 0; jn < 4; ++jn) acc[i][jn] = (f32x4){0, 0, 0, 0};
  const unsigned ldsb = (unsigned)(size_t)lds;
  const unsigned q4 = fr >> 2, p4 = lane & 3;
  stage(0, 0); stage(1, 64); stage(2, 128);
  for (int t = 0; t < nt; ++t) {
    const int buf = t & 3;
    if (t + 2 < nt) asm volatile("s_waitcnt vmcnt(8)" ::: "memory"); else if (t + 1 < nt) asm volatile("s_waitcnt vmcnt(4)" ::: "memory"); else asm volatile("s_waitcnt vmcnt(0)" ::: "memory");
    RBAR();
    if (t + 3 < nt) stage((t + 3) & 3, (t + 3) * 64);
    const char* sA = lds + buf * 32768;
    const unsigned sB = ldsb + buf * 32768 + 16384;
    const int ks = kgp;
    b16x8 af[4], bfr[4];
#pragma unroll
    for (int jn = 0; jn < 4; ++jn) {
      const unsigned c = wc * 4 + jn, r0 = 32 * ks + 8 * g + q4, r1 = r0 + 4;
      s16x4 lo = __builtin_amdgcn_ds_read_tr16_b64_v4i16((LAS s16x4*)(sB + r0 * 256 + (((2 * c + (p4 >> 1)) ^ (fB(r0) << 1)) << 4) + 8 * (p4 & 1)));
      s16x4 hi = __builtin_amdgcn_ds_read_tr16_b64_v4i16((LAS s16x4*)(sB + r1 * 256 + (((2 * c + (p4 >> 1)) ^ (fB(r1) << 1)) << 4) + 8 * (p4 & 1)));
      typedef short s16x8 __attribute__((ext_vector_type(8)));
      s16x8 tt = {lo[0], lo[1], lo[2], lo[3], hi[0], hi[1], hi[2], hi[3]};
      bfr[jn] = __builtin_bit_cast(b16x8, tt);
    }
#pragma unroll
    for (int i = 0; i < 4; ++i) { const unsigned row = wr * 64 + i * 16 + fr; af[i] = *(const b16x8*)(sA + row * 128 + (((ks * 4 + g) ^ (row & 7)) << 4)); }
#pragma unroll
    for (int i = 0; i < 4; ++i)
#pragma unroll
      for (int jn = 0; jn < 4; ++jn) acc[i][jn] = mfma16(bfr[jn], af[i], acc[i][jn]);
  }
  RBAR();
  f32x4* X = (f32x4*)lds;
  if (kgp == 1) {
#pragma unroll
    for (int i = 0; i < 4; ++i)
#pragma unroll
      for (int jn = 0; jn < 4; ++jn) X[((wave & 3) * 16 + i * 4 + jn) * 64 + lane] = acc[i][jn];
  }
  RBAR();
  if (kgp == 0) {
    float* P = (float*)(ws + OFF_QX) + (size_t)part * S * D;
#pragma unroll
    for (int i = 0; i < 4; ++i)
#pragma unroll
      for (int jn = 0; jn < 4; ++jn) {
        f32x4 v = acc[i][jn] + X[((wave & 3) * 16 + i * 4 + jn) * 64 + lane];
        *(f32x4*)(P + (size_t)(m0 + wr * 64 + i * 16 + fr) * D + n0 + wc * 64 + jn * 16 + 4 * g) = v;
      }
  }
}

__global__ __launch_bounds__(256) void k_mix(const char* __restrict__ ws, u16* __restrict__ out) {
  const size_t i = (size_t)blockIdx.x * 256 + threadIdx.x;
  const float* P = (const float*)(ws + OFF_QX);
  const f32x4 a0 = *(const f32x4*)(P + i * 4), a1 = *(const f32x4*)(P + (size_t)S * D + i * 4), bb = *(const f32x4*)(P + (size_t)2 * S * D + i * 4);
  const float gg = ((const float*)(ws + OFF_G))[(i * 4) / D], og = rbf(1.f - gg);
  float r[4];
#pragma unroll
  for (int q = 0; q < 4; ++q) r[q] = rbf(gg * rbf(a0[q] + a1[q])) + rbf(og * rbf(bb[q]));
  *(u32x2*)(out + i * 4) = (u32x2){pack2(r[0], r[1]), pack2(r[2], r[3])};
}

extern "C" void kernel_launch(void* const* d_in, const int* in_sizes, int n_in, void* d_out, int out_size, void* d_ws, size_t ws_size,
                              hipStream_t stream) {
  const u16* x = (const u16*)d_in[0];
  const u16* mem0 = (const u16*)d_in[1];
  const u16* Wq = (const u16*)d_in[2];
  const u16* Wk = (const u16*)d_in[3];
  const u16* Wv = (const u16*)d_in[4];
  const u16* Wo = (const u16*)d_in[5];
  const u16* wg = (const u16*)d_in[8];
  const u16* wu = (const u16*)d_in[9];
  const u16* wd = (const u16*)d_in[10];
  const u16* gw = (const u16*)d_in[11];
  char* ws = (char*)d_ws;
  hipFuncSetAttribute((const void*)k_pre, hipFuncAttributeMaxDynamicSharedMemorySize, 128 * 1024);
  k_pre<<<NT_QKV + NT_GLU, 512, 128 * 1024, stream>>>(x, Wq, Wk, Wv, wg, wu, gw, ws);
  hipFuncSetAttribute((const void*)k_chain, hipFuncAttributeMaxDynamicSharedMemorySize, 144 * 1024);
  k_chain<<<256, 320, 144 * 1024, stream>>>(mem0, Wq, Wk, Wv, Wo, ws);
  k_attn<<<512, 256, 0, stream>>>(ws);
  hipFuncSetAttribute((const void*)k_final, hipFuncAttributeMaxDynamicSharedMemorySize, 128 * 1024);
  k_final<<<240, 512, 128 * 1024, stream>>>(Wo, wd, ws);
  k_mix<<<S * D / 4 / 256, 256, 0, stream>>>(ws, (u16*)d_out);
}
```

```cpp
#include <hip/hip_runtime.h>
#include <hip/hip_bf16.h>
#include <stdint.h>
#include <stdio.h>

typedef unsigned short u16;
typedef __bf16 b16x8 __attribute__((ext_vector_type(8)));
typedef short s16x4 __attribute__((ext_vector_type(4)));
typedef float f32x4 __attribute__((ext_vector_type(4)));
typedef unsigned u32x4 __attribute__((ext_vector_type(4)));
typedef unsigned u32x2 __attribute__((ext_vector_type(2)));
#define LAS __attribute__((address_space(3)))
#define DI __device__ __forceinline__

constexpr int S = 2048, D = 640, H = 32, E = 128, HE = 4096, MM = 8, SEG = 32, NSEG = 64, F = 1728;
constexpr float SCALE = 0.08838834764831845f;

constexpr size_t OFF_KX = 0;
constexpr size_t OFF_VX = OFF_KX + (size_t)S * HE * 2;
constexpr size_t OFF_O  = OFF_VX + (size_t)S * HE * 2;
constexpr size_t OFF_HH = OFF_O + (size_t)S * HE * 2;
constexpr size_t OFF_WF = OFF_HH + (size_t)S * F * 2;
constexpr size_t OFF_G  = OFF_WF + (size_t)40 * 182 * 1024;
constexpr size_t OFF_P  = OFF_G + (size_t)S * 4;
constexpr size_t SZ_P1  = (size_t)H * 640 * 8 * 4;
constexpr size_t SZ_P   = 2 * SZ_P1;
constexpr size_t OFF_SENT = OFF_P;
constexpr size_t OFF_MEM = OFF_P + SZ_P;
constexpr size_t OFF_QM = OFF_MEM + (size_t)NSEG * 10240;
constexpr size_t OFF_END = OFF_QM + (size_t)NSEG * H * 6144;
constexpr size_t SZ_SENT = OFF_END - OFF_SENT;
constexpr size_t OFF_DBG = OFF_END;

DI float bf2f(u16 v) { return __builtin_bit_cast(float, (unsigned)v << 16); }
DI u16 f2bf(float f) { return __builtin_bit_cast(u16, (__bf16)f); }
DI float rbf(float f) { return (float)(__bf16)f; }
typedef float f32x2 __attribute__((ext_vector_type(2)));
typedef __bf16 b16x2 __attribute__((ext_vector_type(2)));
DI unsigned pack2(float lo, float hi) { f32x2 v = {lo, hi}; return __builtin_bit_cast(unsigned, __builtin_convertvector(v, b16x2)); }
DI f32x4 mfma16(b16x8 a, b16x8 b, f32x4 c) { return __builtin_amdgcn_mfma_f32_16x16x32_bf16(a, b, c, 0, 0, 0); }
DI b16x8 asb(u32x4 v) { return __builtin_bit_cast(b16x8, v); }

DI float gelu_tanh(float x) {
  float y = 0.7978845608028654f * (x + 0.044715f * x * x * x);
  float t = 1.f - 2.f / (__expf(2.f * y) + 1.f);
  return 0.5f * x * (1.f + t);
}

#define RBAR() asm volatile("s_waitcnt lgkmcnt(0)\n\ts_barrier" ::: "memory")
DI unsigned fB(unsigned row) { return (row & 3) | (((row >> 3) & 1) << 2); }
template <int BMODE, typename PRE>
DI void gemm256(const u16* __restrict__ A, int lda, const u16* __restrict__ B0, const u16* __restrict__ B1, int ldb, int ncols, int K,
                int m0, int n0, char* lds, f32x4 (&acc)[8][4], PRE pre) {
  const int tid = threadIdx.x, lane = tid & 63, wave = __builtin_amdgcn_readfirstlane(tid >> 6), wr = wave >> 2, wc = wave & 3;
  const int fr = lane & 15, g = lane >> 4;
  unsigned offA[4], offB[2];
#pragma unroll
  for (int i = 0; i < 4; ++i) {
    const unsigned L = (unsigned)(i * 8 + wave) * 1024u + lane * 16u;
    const unsigned row = L >> 7, ch = (L >> 4) & 7; offA[i] = (m0 + row) * lda + ((ch ^ (row & 7)) * 8);
  }
#pragma unroll
  for (int i = 0; i < 2; ++i) {
    const unsigned L = (unsigned)(i * 8 + wave) * 1024u + lane * 16u;
    const unsigned row = L >> 8, chp = (L >> 4) & 15, ch = chp ^ (fB(row) << 1);
    int col = n0 + ch * 8; if (BMODE == 1) col = col < ncols ? col : ncols - 8;
    offB[i] = row * ldb + col;
  }
  const u16* Bs1 = BMODE == 0 ? B0 + 128 : B1;
  auto stage = [&](int buf, int k0) {
    const u16* Ak = A + k0; const u16* B0k = B0 + (size_t)k0 * ldb; const u16* B1k = Bs1 + (size_t)k0 * ldb;
#pragma unroll
    for (int i = 0; i < 4; ++i) {
      __builtin_amdgcn_global_load_lds((const unsigned*)(Ak + offA[i]), (LAS unsigned*)(lds + buf * 65536 + (i * 8 + wave) * 1024), 16, 0, 0);
      __builtin_amdgcn_global_load_lds((const unsigned*)((i < 2 ? B0k : B1k) + offB[i & 1]), (LAS unsigned*)(lds + buf * 65536 + 32768 + (i * 8 + wave) * 1024), 16, 0, 0);
    }
  };
  const unsigned ldsb = (unsigned)(size_t)lds;
  const unsigned q4 = fr >> 2, p4 = lane & 3;
  const int nt = K / 64;
  stage(0, 0);
  pre();
  asm volatile("s_waitcnt vmcnt(0)" ::: "memory"); RBAR();
  if (wr == 1) RBAR();
  for (int t = 0; t < nt; ++t) {
    const int buf = t & 1;
    stage(buf ^ 1, t + 1 < nt ? (t + 1) * 64 : 0);
    const char* sA = lds + buf * 65536;
    const unsigned sB = ldsb + buf * 65536 + 32768;
#pragma unroll
    for (int ks = 0; ks < 2; ++ks) {
      b16x8 af[8], bfr[4];
#pragma unroll
      for (int jn = 0; jn < 4; ++jn) {
        const unsigned sub = BMODE == 0 ? (wc >> 1) : (jn >> 1), c = BMODE == 0 ? ((wc & 1) * 4 + jn) : (2 * wc + (jn & 1));
        const unsigned r0 = 32 * ks + 8 * g + q4, r1 = r0 + 4;
        s16x4 lo = __builtin_amdgcn_ds_read_tr16_b64_v4i16((LAS s16x4*)(sB + sub * 16384 + r0 * 256 + (((2 * c + (p4 >> 1)) ^ (fB(r0) << 1)) << 4) + 8 * (p4 & 1)));
        s16x4 hi = __builtin_amdgcn_ds_read_tr16_b64_v4i16((LAS s16x4*)(sB + sub * 16384 + r1 * 256 + (((2 * c + (p4 >> 1)) ^ (fB(r1) << 1)) << 4) + 8 * (p4 & 1)));
        typedef short s16x8 __attribute__((ext_vector_type(8)));
        s16x8 tt = {lo[0], lo[1], lo[2], lo[3], hi[0], hi[1], hi[2], hi[3]};
        bfr[jn] = __builtin_bit_cast(b16x8, tt);
      }
#pragma unroll
      for (int i = 0; i < 8; ++i) { const unsigned row = wr * 128 + i * 16 + fr; af[i] = *(const b16x8*)(sA + row * 128 + (((ks * 4 + g) ^ (row & 7)) << 4)); }
      if (ks == 1 && wr == 1) asm volatile("s_waitcnt vmcnt(0)" ::: "memory");
      RBAR();
      __builtin_amdgcn_s_setprio(1);
#pragma unroll
      for (int i = 0; i < 8; ++i)
#pragma unroll
        for (int jn = 0; jn < 4; ++jn) acc[i][jn] = mfma16(bfr[jn], af[i], acc[i][jn]);
      __builtin_amdgcn_s_setprio(0);
      if (ks == 1 && wr == 0) asm volatile("s_waitcnt vmcnt(0)" ::: "memory");
      RBAR();
    }
  }
  if (wr == 0) RBAR();
}

__global__ __launch_bounds__(512, 1) void k_pre(const u16* __restrict__ x, const u16* __restrict__ Wk, const u16* __restrict__ Wv,
                                                const u16* __restrict__ Wo, const u16* __restrict__ wd,
                                                const u16* __restrict__ gate_w, char* __restrict__ ws) {
  extern __shared__ __attribute__((aligned(16))) char lds[];
  const int b = blockIdx.x;
  const int tid = threadIdx.x, lane = tid & 63, wave = tid >> 6;
  auto misc = [&] {
    constexpr int NB = 256;
    const int t = b * 8 + wave;
    float s = 0.f;
#pragma unroll
    for (int i = 0; i < 5; ++i) {
      const unsigned xv = *(const unsigned*)(x + (size_t)t * D + i * 128 + lane * 2), gv = *(const unsigned*)(gate_w + i * 128 + lane * 2);
      s += rbf(__uint_as_float(xv << 16) * __uint_as_float(gv << 16)) + rbf(__uint_as_float(xv & 0xFFFF0000u) * __uint_as_float(gv & 0xFFFF0000u));
    }
#pragma unroll
    for (int o = 32; o >= 1; o >>= 1) s += __shfl_xor(s, o);
    s = rbf(s);
    float gg = rbf(1.f / (1.f + __expf(-s)));
    if (lane == 0) ((float*)(ws + OFF_G))[t] = gg;
    u32x4 ones = {0xFFFFFFFFu, 0xFFFFFFFFu, 0xFFFFFFFFu, 0xFFFFFFFFu};
    for (size_t i = (size_t)b * 512 + tid; i < SZ_SENT / 16; i += (size_t)NB * 512) *(u32x4*)(ws + OFF_SENT + i * 16) = ones;
    {
      char* my = lds + 131072 + wave * 4096;
      const int ml = lane & 15, g = lane >> 4;
      u32x4 t4[4];
#pragma unroll
      for (int i = 0; i < 4; ++i) {
        int f = (b * 8 + wave) + i * 2048; f = f < 7280 ? f : 0;
        const int ct = f / 182, kk = f % 182;
        const u16* W = kk < 128 ? Wo + (size_t)32 * kk * D : wd + (size_t)32 * (kk - 128) * D;
        t4[i] = *(const u32x4*)(W + (size_t)(lane >> 1) * D + 16 * ct + (lane & 1) * 8);
      }
#pragma unroll
      for (int i = 0; i < 4; ++i) *(u32x4*)(my + i * 1024 + lane * 16) = t4[i];
      asm volatile("s_waitcnt lgkmcnt(0)" ::: "memory");
      const unsigned trb = (unsigned)(size_t)my + (4 * g + (ml >> 2)) * 32 + (lane & 3) * 8;
#pragma unroll
      for (int i = 0; i < 4; ++i) {
        const int f = (b * 8 + wave) + i * 2048;
        s16x4 lo = __builtin_amdgcn_ds_read_tr16_b64_v4i16((LAS s16x4*)(trb + i * 1024));
        s16x4 hi = __builtin_amdgcn_ds_read_tr16_b64_v4i16((LAS s16x4*)(trb + i * 1024 + 512));
        typedef short s16x8 __attribute__((ext_vector_type(8)));
        s16x8 tt = {lo[0], lo[1], lo[2], lo[3], hi[0], hi[1], hi[2], hi[3]};
        if (f < 7280) *(s16x8*)(ws + OFF_WF + (size_t)f * 1024 + lane * 16) = tt;
      }
    }
  };
  const int nt = (b & 7) * 4 + (b >> 6), mt = (b >> 3) & 7, which = nt >> 4, n0 = (nt & 15) * 256, m0 = mt * 256;
  f32x4 acc[8][4];
#pragma unroll
  for (int i = 0; i < 8; ++i)
#pragma unroll
    for (int j = 0; j < 4; ++j) acc[i][j] = (f32x4){0, 0, 0, 0};
  gemm256<0>(x, D, which == 0 ? Wk : Wv, nullptr, HE, HE, D, m0, n0, lds, acc, misc);
  char* out = ws + (which == 0 ? OFF_KX : OFF_VX);
  int tz = threadIdx.x; asm volatile("" : "+v"(tz));
  const int wr = tz >> 8, wc = (tz >> 6) & 3, fr = tz & 15, g = (tz >> 4) & 3;
#pragma unroll
  for (int i = 0; i < 8; ++i)
#pragma unroll
    for (int j = 0; j < 4; ++j) {
      u32x2 v = {pack2(acc[i][j][0], acc[i][j][1]), pack2(acc[i][j][2], acc[i][j][3])};
      const int tok = m0 + wr * 128 + i * 16 + fr, n = n0 + wc * 64 + j * 16 + 4 * g;
      const int seg = tok >> 5, kt = (tok >> 4) & 1, hh = n >> 7, e = n & 127;
      *(u32x2*)(out + (size_t)(seg * H + hh) * 8192 + (size_t)(((kt * 4 + (e >> 5)) * 64 + (tok & 15) + 16 * ((e & 31) >> 3)) * 16 + (e & 4) * 2)) = v;
    }
}

#define SC1 16
DI u32x4 ld128(__amdgpu_buffer_rsrc_t rs, unsigned off) { return __builtin_amdgcn_raw_buffer_load_b128(rs, off, 0, SC1); }
DI u32x2 ld64(__amdgpu_buffer_rsrc_t rs, unsigned off) { return __builtin_amdgcn_raw_buffer_load_b64(rs, off, 0, SC1); }
DI void st128(__amdgpu_buffer_rsrc_t rs, unsigned off, u32x4 v) { __builtin_amdgcn_raw_buffer_store_b128(v, rs, off, 0, SC1); }
DI void st64(__amdgpu_buffer_rsrc_t rs, unsigned off, u32x2 v) { __builtin_amdgcn_raw_buffer_store_b64(v, rs, off, 0, SC1); }
DI unsigned nosent(unsigned v) { return v == 0xFFFFFFFFu ? 0xFFFEFFFFu : v; }
DI unsigned umax3(unsigned a, unsigned b, unsigned c) { return max(max(a, b), c); }
DI float xsum16(float x) { unsigned u = __float_as_uint(x); auto r = __builtin_amdgcn_permlane16_swap(u, u, false, false); return __uint_as_float(r[0]) + __uint_as_float(r[1]); }
DI float xsum32(float x) { unsigned u = __float_as_uint(x); auto r = __builtin_amdgcn_permlane32_swap(u, u, false, false); return __uint_as_float(r[0]) + __uint_as_float(r[1]); }
DI float xmax16(float x) { unsigned u = __float_as_uint(x); auto r = __builtin_amdgcn_permlane16_swap(u, u, false, false); return fmaxf(__uint_as_float(r[0]), __uint_as_float(r[1])); }
DI float xmax32(float x) { unsigned u = __float_as_uint(x); auto r = __builtin_amdgcn_permlane32_swap(u, u, false, false); return fmaxf(__uint_as_float(r[0]), __uint_as_float(r[1])); }
DI float ror4(float x) { return __uint_as_float(__builtin_amdgcn_update_dpp(0u, __float_as_uint(x), 0x124, 0xf, 0xf, false)); }
DI float ror8(float x) { return __uint_as_float(__builtin_amdgcn_update_dpp(0u, __float_as_uint(x), 0x128, 0xf, 0xf, false)); }
#define LBAR() asm volatile("s_waitcnt lgkmcnt(0)\n\ts_barrier" ::: "memory")
template <int N> DI void slp() { if constexpr (N > 0) { __builtin_amdgcn_s_sleep(N > 15 ? 15 : N); if constexpr (N > 15) slp<N - 15>(); } }

#define TS(i)
#define SPN(k)
constexpr unsigned SPIN_CAP = 1u << 17;
#define SLP_A 16
#define SLP_B 12
#define SLP_B3 4
#define SLP_C 11

DI void chain_role(const int cb, const u16* __restrict__ mem0, const u16* __restrict__ Wq, const u16* __restrict__ Wk,
                   const u16* __restrict__ Wv, const u16* __restrict__ Wo, char* __restrict__ ws, char* dyn_lds) {
  const int xcd = cb & 7, y = cb >> 3, h = xcd * 4 + (y >> 3), j = y & 7;
  const int tid = threadIdx.x, lane = tid & 63, wave = __builtin_amdgcn_readfirstlane(tid >> 6);
  const int ml = lane & 15, g = lane >> 4;
  const __amdgpu_buffer_rsrc_t rs = __builtin_amdgcn_make_buffer_rsrc(ws + OFF_P, 0, (int)(OFF_END - OFF_P), 0x00020000);
  constexpr unsigned R_P = 0, R_MEM = (unsigned)(OFF_MEM - OFF_P), R_QM = (unsigned)(OFF_QM - OFF_P);
  f32x4* X1 = (f32x4*)dyn_lds;
  char* KV = dyn_lds + 24576;
  char* WL = dyn_lds + 57344;
  const f32x4 z4 = {0, 0, 0, 0};
  const unsigned OOB = 0x7FFFFF00u;
  const unsigned lo_off = ml < 8 ? (unsigned)(g * 8 + ml) * 16u : OOB;
  unsigned spin_cap = SPIN_CAP;
  typedef short s16x8 __attribute__((ext_vector_type(8)));
  char* my = WL + (wave < 4 ? wave * 15360 : 61440 + (wave - 4) * 8192);
  const unsigned trb = (unsigned)(size_t)my + (8 * g + (ml >> 2)) * 32 + (lane & 3) * 8;
  auto trfrag = [&](int i) {
    s16x4 lo = __builtin_amdgcn_ds_read_tr16_b64_v4i16((LAS s16x4*)(trb + i * 1024));
    s16x4 hi = __builtin_amdgcn_ds_read_tr16_b64_v4i16((LAS s16x4*)(trb + i * 1024 + 128));
    s16x8 tt = {lo[0], lo[1], lo[2], lo[3], hi[0], hi[1], hi[2], hi[3]};
    return __builtin_bit_cast(b16x8, tt);
  };

  if (wave < 4) {
    b16x8 w1[3][5];
    {
      u32x4 t1[15];
#pragma unroll
      for (int t = 0; t < 3; ++t) {
        const u16* W = t == 0 ? Wq : (t == 1 ? Wk : Wv);
#pragma unroll
        for (int i = 0; i < 5; ++i) t1[t * 5 + i] = *(const u32x4*)(W + (size_t)(160 * wave + 32 * i + (lane >> 1)) * HE + h * E + 16 * j + (lane & 1) * 8);
      }
#pragma unroll
      for (int i = 0; i < 15; ++i) *(u32x4*)(my + i * 1024 + lane * 16) = t1[i];
      asm volatile("s_waitcnt lgkmcnt(0)" ::: "memory");
#pragma unroll
      for (int i = 0; i < 15; ++i) w1[i / 5][i % 5] = trfrag(i);
    }
    const char* kvsrc = ws + (wave == 2 ? OFF_KX : OFF_VX) + (size_t)h * 8192 + lane * 16;
    if (wave >= 2) {
#pragma unroll
      for (int sn = 0; sn < 2; ++sn) {
        u32x4 t[8];
#pragma unroll
        for (int i = 0; i < 8; ++i) t[i] = *(const u32x4*)(kvsrc + (size_t)sn * H * 8192 + i * 1024);
#pragma unroll
        for (int i = 0; i < 8; ++i) *(u32x4*)(KV + sn * 16384 + (wave - 2) * 8192 + i * 1024 + lane * 16) = t[i];
      }
    }
    LBAR();
    const int cc = lane & 7, hg = lane >> 3;
    for (int s = 0; s < NSEG; ++s) {
      TS(0);
      f32x4 ac[3];
      if (s == 0) {
#pragma unroll
        for (int u = 0; u < 3; ++u) ac[u] = z4;
#pragma unroll
        for (int kk = 0; kk < 5; ++kk) {
          u32x4 mf = *(const u32x4*)(mem0 + (ml & 7) * D + 32 * (5 * wave + kk) + 8 * g);
#pragma unroll
          for (int u = 0; u < 3; ++u) ac[u] = mfma16(w1[u][kk], asb(mf), ac[u]);
        }
      } else {
        const unsigned base = R_MEM + (unsigned)s * 10240u + (unsigned)wave * 2560u + lo_off;
        slp<SLP_A>();
        u32x4 mf[5];
        for (unsigned spin = 0;; ++spin) {
          asm volatile("" ::: "memory");
#pragma unroll
          for (int kk = 0; kk < 5; ++kk) mf[kk] = ld128(rs, base + kk * 512u);
          unsigned mx = 0;
#pragma unroll
          for (int kk = 0; kk < 5; ++kk) mx = umax3(mx, mf[kk][0], mf[kk][2]);
          if (__all(mx != 0xFFFFFFFFu) || spin >= spin_cap) { SPN(0); if (spin >= spin_cap) spin_cap = 0; break; }
        }
#pragma unroll
        for (int u = 0; u < 3; ++u) ac[u] = z4;
#pragma unroll
        for (int kk = 0; kk < 5; ++kk) {
#pragma unroll
          for (int u = 0; u < 3; ++u) ac[u] = mfma16(w1[u][kk], asb(mf[kk]), ac[u]);
        }
      }
      TS(1);
#pragma unroll
      for (int u = 0; u < 3; ++u) X1[u * 256 + tid] = ac[u];
      LBAR();
      if (wave < 2) {
        const int tl = 2 * wave + (ml >> 3);
        const f32x4* xp = X1 + (tl < 3 ? tl : 2) * 256 + g * 16 + (ml & 7);
        f32x4 a = (xp[0] + xp[64]) + (xp[128] + xp[192]);
        u32x2 v = {nosent(pack2(a[0], a[1])), nosent(pack2(a[2], a[3]))};
        unsigned off = R_QM + (unsigned)(s * H + h) * 6144u + (unsigned)tl * 2048u + (unsigned)(((j >> 1) * 4 + 2 * (j & 1) + (g >> 1)) * 8 + (ml & 7)) * 16u + (g & 1) * 8u;
        if (tl < 3) st64(rs, off, v);
      }
      TS(2);
      if (s == NSEG - 1) break;
      if (wave != 0) { LBAR(); LBAR(); } else {
        LBAR();
        const unsigned off = cc < 5 ? R_P + (unsigned)(s & 1) * (unsigned)SZ_P1 + (unsigned)hg * 81920u + (unsigned)(5 * cb + cc) * 16u : OOB;
        slp<SLP_C>();
        u32x4 v[4];
        for (unsigned spin = 0;; ++spin) {
          asm volatile("" ::: "memory");
#pragma unroll
          for (int i = 0; i < 4; ++i) v[i] = ld128(rs, off + i * 20480u);
          unsigned mx = 0;
#pragma unroll
          for (int i = 0; i < 4; ++i) { mx = umax3(mx, v[i][0], v[i][1]); mx = umax3(mx, v[i][2], v[i][3]); }
          if (__all(mx != 0xFFFFFFFFu) || spin >= spin_cap) { SPN(2); if (spin >= spin_cap) spin_cap = 0; break; }
        }
        TS(5);
        float f[4];
#pragma unroll
        for (int r = 0; r < 4; ++r) {
          float x = (__uint_as_float(v[0][r]) + __uint_as_float(v[1][r])) + (__uint_as_float(v[2][r]) + __uint_as_float(v[3][r]));
          x += ror8(x); x = xsum32(xsum16(x)); f[r] = x;
        }
        u32x2 w = {nosent(pack2(f[0], f[1])), nosent(pack2(f[2], f[3]))};
        if (lane < 5) st64(rs, R_MEM + (unsigned)(s + 1) * 10240u + (unsigned)(5 * cb + lane) * 8u, w);
        TS(6);
        LBAR();
        const u32x4 ones = {0xFFFFFFFFu, 0xFFFFFFFFu, 0xFFFFFFFFu, 0xFFFFFFFFu};
        if (cc < 5) {
#pragma unroll
          for (int i = 0; i < 4; ++i) st128(rs, off + i * 20480u, ones);
        }
      }
    }
    return;
  }
  const int w4 = wave - 4;
  const bool three = w4 < 1;
  b16x8 w2[2][4];
  {
    u32x4 t2[8];
#pragma unroll
    for (int t = 0; t < 2; ++t) {
      const int ct = (t < 1 || three) ? w4 + 4 * t : 0;
#pragma unroll
      for (int i = 0; i < 4; ++i) t2[t * 4 + i] = *(const u32x4*)(Wo + (size_t)(h * E + 32 * i + (lane >> 1)) * D + 80 * j + 16 * ct + (lane & 1) * 8);
    }
#pragma unroll
    for (int i = 0; i < 8; ++i) *(u32x4*)(my + i * 1024 + lane * 16) = t2[i];
    asm volatile("s_waitcnt lgkmcnt(0)" ::: "memory");
#pragma unroll
    for (int i = 0; i < 8; ++i) w2[i / 4][i % 4] = trfrag(i);
  }
  b16x8 kx[2][4]; u32x4 vwx[2];
  const b16x8 ones8 = asb((u32x4){0x3F803F80u, 0x3F803F80u, 0x3F803F80u, 0x3F803F80u});
  auto prep = [&](int sn) {
    const char* kb = KV + (sn & 1) * 16384 + lane * 16;
    b16x8 vf[2][4];
#pragma unroll
    for (int kt = 0; kt < 2; ++kt)
#pragma unroll
      for (int ks = 0; ks < 4; ++ks) { kx[kt][ks] = *(const b16x8*)(kb + (kt * 4 + ks) * 1024); vf[kt][ks] = *(const b16x8*)(kb + 8192 + (kt * 4 + ks) * 1024); }
#pragma unroll
    for (int t = 0; t < 2; ++t) {
      f32x4 d0 = z4, d1 = z4;
#pragma unroll
      for (int ks = 0; ks < 4; ++ks) { d0 = mfma16(vf[0][ks], w2[t][ks], d0); d1 = mfma16(vf[1][ks], w2[t][ks], d1); }
      vwx[t] = (u32x4){pack2(d0[0], d0[1]), pack2(d0[2], d0[3]), pack2(d1[0], d1[1]), pack2(d1[2], d1[3])};
    }
  };
  const char* kvs = ws + (w4 < 2 ? OFF_KX : OFF_VX) + (size_t)h * 8192 + (w4 & 1) * 4096 + lane * 16;
  u32x4 stg[4];
#pragma unroll
  for (int i = 0; i < 4; ++i) stg[i] = *(const u32x4*)(kvs + (size_t)2 * H * 8192 + i * 1024);
  LBAR();
  prep(0);
  for (int s = 0; s < NSEG - 1; ++s) {
    const unsigned hb = R_QM + (unsigned)(s * H + h) * 6144u + lo_off;
    LBAR();
    slp<SLP_B>();
    f32x4 a[2];
    u32x4 qf[4], kf[4], vf[4];
    for (unsigned spin = 0;; ++spin) {
      asm volatile("" ::: "memory");
#pragma unroll
      for (int ks = 0; ks < 4; ++ks) qf[ks] = ld128(rs, hb + ks * 512u);
#pragma unroll
      for (int ks = 0; ks < 4; ++ks) kf[ks] = ld128(rs, hb + 2048u + ks * 512u);
#pragma unroll
      for (int ks = 0; ks < 4; ++ks) vf[ks] = ld128(rs, hb + 4096u + ks * 512u);
      unsigned mx = 0;
#pragma unroll
      for (int ks = 0; ks < 4; ++ks) { mx = umax3(mx, qf[ks][0], qf[ks][2]); mx = umax3(mx, kf[ks][0], kf[ks][2]); mx = umax3(mx, vf[ks][0], vf[ks][2]); }
      if (__all(mx != 0xFFFFFFFFu) || spin >= spin_cap) { SPN(1); if (spin >= spin_cap) spin_cap = 0; break; }
    }
    {
#define SB() __builtin_amdgcn_sched_barrier(0)
      f32x4 s0 = z4, s1 = z4, sm = z4, dm[2] = {z4, z4};
#pragma unroll
      for (int ks = 0; ks < 4; ++ks) { s0 = mfma16(kx[0][ks], asb(qf[ks]), s0); s1 = mfma16(kx[1][ks], asb(qf[ks]), s1); SB(); }
#pragma unroll
      for (int ks = 0; ks < 4; ++ks) { sm = mfma16(asb(kf[ks]), asb(qf[ks]), sm); SB(); }
      TS(3);
      const float NEG = -INFINITY;
      const float c2 = SCALE * 1.4426950408889634f;
      float tv[12];
      dm[0] = mfma16(asb(vf[0]), w2[0][0], dm[0]);
#pragma unroll
      for (int r = 0; r < 4; ++r) tv[r] = s0[r] * c2;
      SB();
      dm[1] = mfma16(asb(vf[0]), w2[1][0], dm[1]);
#pragma unroll
      for (int r = 0; r < 4; ++r) tv[4 + r] = s1[r] * c2;
      SB();
      float m1 = fmaxf(fmaxf(tv[0], tv[1]), tv[2]), m2 = fmaxf(fmaxf(tv[3], tv[4]), tv[5]);
      m1 = fmaxf(fmaxf(m1, tv[6]), tv[7]);
      SB();
      dm[0] = mfma16(asb(vf[1]), w2[0][1], dm[0]);
#pragma unroll
      for (int r = 0; r < 4; ++r) tv[8 + r] = (g < 2) ? sm[r] * c2 : NEG;
      SB();
      dm[1] = mfma16(asb(vf[1]), w2[1][1], dm[1]);
      m2 = fmaxf(fmaxf(m2, tv[8]), tv[9]); m1 = fmaxf(fmaxf(m1, tv[10]), tv[11]);
      float mxv = fmaxf(m1, m2);
      { unsigned u = __float_as_uint(mxv); auto r = __builtin_amdgcn_permlane16_swap(u, u, false, false); mxv = fmaxf(__uint_as_float(r[0]) + 0.f, __uint_as_float(r[1]) + 0.f); }
      SB();
      { unsigned u = __float_as_uint(mxv); auto r = __builtin_amdgcn_permlane32_swap(u, u, false, false); mxv = fmaxf(__uint_as_float(r[0]) + 0.f, __uint_as_float(r[1]) + 0.f); }
      SB();
      dm[0] = mfma16(asb(vf[2]), w2[0][2], dm[0]);
#pragma unroll
      for (int r = 0; r < 2; ++r) tv[r] = __builtin_amdgcn_exp2f(tv[r] - mxv);
      SB();
      dm[1] = mfma16(asb(vf[2]), w2[1][2], dm[1]);
#pragma unroll
      for (int r = 2; r < 4; ++r) tv[r] = __builtin_amdgcn_exp2f(tv[r] - mxv);
      SB();
#pragma unroll
      for (int r = 4; r < 6; ++r) tv[r] = __builtin_amdgcn_exp2f(tv[r] - mxv);
      SB();
      dm[0] = mfma16(asb(vf[3]), w2[0][3], dm[0]);
#pragma unroll
      for (int r = 6; r < 8; ++r) tv[r] = __builtin_amdgcn_exp2f(tv[r] - mxv);
      SB();
      dm[1] = mfma16(asb(vf[3]), w2[1][3], dm[1]);
#pragma unroll
      for (int r = 8; r < 10; ++r) tv[r] = __builtin_amdgcn_exp2f(tv[r] - mxv);
      SB();
#pragma unroll
      for (int r = 10; r < 12; ++r) tv[r] = __builtin_amdgcn_exp2f(tv[r] - mxv);
      u32x4 px = {pack2(tv[0], tv[1]), pack2(tv[2], tv[3]), pack2(tv[4], tv[5]), pack2(tv[6], tv[7])};
      SB();
      f32x4 sx = mfma16(ones8, asb(px), z4);
      a[0] = mfma16(asb(vwx[0]), asb(px), z4);
      a[1] = mfma16(asb(vwx[1]), asb(px), z4);
      u32x4 pm = {pack2(tv[8], tv[9]), pack2(tv[10], tv[11]), 0u, 0u};
      SB();
      sx = mfma16(ones8, asb(pm), sx);
#pragma unroll
      for (int t = 0; t < 2; ++t) {
        u32x4 vwm = {pack2(dm[t][0], dm[t][1]), pack2(dm[t][2], dm[t][3]), 0u, 0u};
        a[t] = mfma16(asb(vwm), asb(pm), a[t]);
      }
      SB();
      const float inv = __builtin_amdgcn_rcpf(sx[0]);
      a[0] *= inv; a[1] *= inv;
    }
#pragma unroll
    for (int t = 0; t < 2; ++t) {
      if (t == 1 && !three) break;
      u32x4 v;
#pragma unroll
      for (int r = 0; r < 4; ++r) v[r] = min(__float_as_uint(a[t][r]), 0xFFFFFFFEu);
      const int T = 5 * j + w4 + 4 * t;
      unsigned off = R_P + (unsigned)(s & 1) * (unsigned)SZ_P1 + (unsigned)h * 20480u + (unsigned)(((T >> 1) * 4 + 2 * (T & 1) + (g >> 1)) * 8 + ml) * 32u + (unsigned)(g & 1) * 16u;
      if (ml < 8) st128(rs, off, v);
    }
    TS(4);
    LBAR();
    if (s + 1 < NSEG - 1) prep(s + 1);
    if (s + 2 < NSEG - 1) {
#pragma unroll
      for (int i = 0; i < 4; ++i) *(u32x4*)(KV + (s & 1) * 16384 + w4 * 4096 + i * 1024 + lane * 16) = stg[i];
    }
    LBAR();
    if (s + 3 < NSEG - 1) {
#pragma unroll
      for (int i = 0; i < 4; ++i) stg[i] = *(const u32x4*)(kvs + (size_t)(s + 3) * H * 8192 + i * 1024);
    }
  }
  LBAR();
}

__global__ __launch_bounds__(512, 1) void k_chain(const u16* __restrict__ mem0, const u16* __restrict__ Wq, const u16* __restrict__ Wk,
                                                   const u16* __restrict__ Wv, const u16* __restrict__ Wo, char* __restrict__ ws) {
  extern __shared__ __attribute__((aligned(16))) char dyn_lds[];
  chain_role(blockIdx.x, mem0, Wq, Wk, Wv, Wo, ws, dyn_lds);
}

DI void attn_unit(const int s, const int h, const b16x8 (&qf)[2][4], char* __restrict__ ws, char* V) {
  const int lane = threadIdx.x & 63, ml = lane & 15, g = lane >> 4;
  const char* KX = ws + OFF_KX + (size_t)(s * H + h) * 8192 + lane * 16;
  const char* VX = ws + OFF_VX + (size_t)(s * H + h) * 8192 + lane * 16;
  const char* KM = ws + OFF_QM + (size_t)(s * H + h) * 6144 + 2048 + (g * 8 + (ml & 7)) * 16;
  b16x8 kx[2][4], km[4];
#pragma unroll
  for (int ks = 0; ks < 4; ++ks) {
#pragma unroll
    for (int t = 0; t < 2; ++t) {
      kx[t][ks] = *(const b16x8*)(KX + (t * 4 + ks) * 1024);
      *(u32x4*)(V + (16 * t + ml) * 272 + (32 * ks + 8 * g) * 2) = *(const u32x4*)(VX + (t * 4 + ks) * 1024);
    }
    km[ks] = *(const b16x8*)(KM + ks * 512);
    u32x4 vmr = *(const u32x4*)(KM + 2048 + ks * 512);
    if (ml >= 8) vmr = (u32x4){0, 0, 0, 0};
    *(u32x4*)(V + (32 + ml) * 272 + (32 * ks + 8 * g) * 2) = vmr;
  }
  u32x4 px[2], pm[2];
#pragma unroll
  for (int qt = 0; qt < 2; ++qt) {
    f32x4 s0 = {0, 0, 0, 0}, s1 = {0, 0, 0, 0}, sm = {0, 0, 0, 0};
#pragma unroll
    for (int ks = 0; ks < 4; ++ks) {
      s0 = mfma16(kx[0][ks], qf[qt][ks], s0);
      s1 = mfma16(kx[1][ks], qf[qt][ks], s1);
      sm = mfma16(km[ks], qf[qt][ks], sm);
    }
    const int qi = 16 * qt + ml;
    float sv[12];
#pragma unroll
    for (int r = 0; r < 4; ++r) {
      sv[r] = (4 * g + r <= qi) ? rbf(rbf(s0[r]) * SCALE) : -INFINITY;
      sv[4 + r] = (16 + 4 * g + r <= qi) ? rbf(rbf(s1[r]) * SCALE) : -INFINITY;
      sv[8 + r] = (g < 2) ? rbf(rbf(sm[r]) * SCALE) : -INFINITY;
    }
    float mxv = sv[0];
#pragma unroll
    for (int r = 1; r < 12; ++r) mxv = fmaxf(mxv, sv[r]);
    mxv = xmax32(xmax16(mxv));
    float sum = 0.f;
#pragma unroll
    for (int r = 0; r < 12; ++r) { sv[r] = __expf(sv[r] - mxv); sum += sv[r]; }
    sum = xsum32(xsum16(sum));
    const float inv = 1.f / sum;
    px[qt] = (u32x4){pack2(sv[0] * inv, sv[1] * inv), pack2(sv[2] * inv, sv[3] * inv), pack2(sv[4] * inv, sv[5] * inv), pack2(sv[6] * inv, sv[7] * inv)};
    pm[qt] = (u32x4){pack2(sv[8] * inv, sv[9] * inv), pack2(sv[10] * inv, sv[11] * inv), 0u, 0u};
  }
  asm volatile("s_waitcnt lgkmcnt(0)" ::: "memory");
  const unsigned vb = (unsigned)(size_t)V + (4 * g + (ml >> 2)) * 272 + (lane & 3) * 8;
#pragma unroll
  for (int kk = 0; kk < 4; ++kk) {
    u32x4 of[2];
#pragma unroll
    for (int hf = 0; hf < 2; ++hf) {
      const int et = 2 * kk + hf;
      s16x4 v0 = __builtin_amdgcn_ds_read_tr16_b64_v4i16((LAS s16x4*)(vb + et * 32));
      s16x4 v1 = __builtin_amdgcn_ds_read_tr16_b64_v4i16((LAS s16x4*)(vb + 16 * 272 + et * 32));
      s16x4 v2 = __builtin_amdgcn_ds_read_tr16_b64_v4i16((LAS s16x4*)(vb + 32 * 272 + et * 32));
      typedef short s16x8 __attribute__((ext_vector_type(8)));
      s16x8 va = {v0[0], v0[1], v0[2], v0[3], v1[0], v1[1], v1[2], v1[3]};
      s16x8 vbm = {v2[0], v2[1], v2[2], v2[3], 0, 0, 0, 0};
#pragma unroll
      for (int qt = 0; qt < 2; ++qt) {
        f32x4 o = {0, 0, 0, 0};
        o = mfma16(__builtin_bit_cast(b16x8, va), asb(px[qt]), o);
        o = mfma16(__builtin_bit_cast(b16x8, vbm), asb(pm[qt]), o);
        of[qt][2 * hf] = pack2(o[0], o[1]); of[qt][2 * hf + 1] = pack2(o[2], o[3]);
      }
    }
#pragma unroll
    for (int qt = 0; qt < 2; ++qt) *(u32x4*)(ws + OFF_O + (size_t)((s * 2 + qt) * 128 + h * 4 + kk) * 1024 + lane * 16) = of[qt];
  }
  asm volatile("s_waitcnt lgkmcnt(0)" ::: "memory");
}

__global__ __launch_bounds__(512, 1) void k_post1(const u16* __restrict__ x, const u16* __restrict__ Wq, const u16* __restrict__ wg,
                                                  const u16* __restrict__ wu, char* __restrict__ ws) {
  extern __shared__ __attribute__((aligned(16))) char lds[];
  const int b = blockIdx.x, xc = b & 7, idx = b >> 3;
  f32x4 acc[8][4];
#pragma unroll
  for (int i = 0; i < 8; ++i)
#pragma unroll
    for (int j = 0; j < 4; ++j) acc[i][j] = (f32x4){0, 0, 0, 0};
  if (idx < 16) {
    const int nt = xc * 2 + (idx >> 3), mt = idx & 7, n0 = nt * 256, m0 = mt * 256;
    gemm256<0>(x, D, Wq, nullptr, HE, HE, D, m0, n0, lds, acc, [] {});
    int tz = threadIdx.x; asm volatile("" : "+v"(tz));
    const int wr = tz >> 8, wc = (tz >> 6) & 3, fr = tz & 15, g = (tz >> 4) & 3, lane = tz & 63, wave = __builtin_amdgcn_readfirstlane(tz >> 6);
#pragma unroll
    for (int i = 0; i < 8; ++i)
#pragma unroll
      for (int j = 0; j < 4; ++j) {
        u32x2 v = {pack2(acc[i][j][0], acc[i][j][1]), pack2(acc[i][j][2], acc[i][j][3])};
        const int tok = wr * 128 + i * 16 + fr, n = wc * 64 + j * 16 + 4 * g;
        const int seg = tok >> 5, kt = (tok >> 4) & 1, hh = n >> 7, e = n & 127;
        *(u32x2*)(lds + (seg * 2 + hh) * 8192 + (((kt * 4 + (e >> 5)) * 64 + (tok & 15) + 16 * ((e & 31) >> 3)) * 16 + (e & 4) * 2)) = v;
      }
    RBAR();
    b16x8 q0[2][4], q1[2][4];
#pragma unroll
    for (int t = 0; t < 2; ++t)
#pragma unroll
      for (int ks = 0; ks < 4; ++ks) {
        q0[t][ks] = *(const b16x8*)(lds + (wave * 2) * 8192 + (t * 4 + ks) * 1024 + lane * 16);
        q1[t][ks] = *(const b16x8*)(lds + (wave * 2 + 1) * 8192 + (t * 4 + ks) * 1024 + lane * 16);
      }
    RBAR();
    char* V = lds + wave * (48 * 272);
    attn_unit(mt * 8 + wave, nt * 2, q0, ws, V);
    attn_unit(mt * 8 + wave, nt * 2 + 1, q1, ws, V);
  } else {
    const int tile = xc * 14 + (idx - 16), ft = tile >> 3, mt = tile & 7, f0 = ft * 128, m0 = mt * 256;
    gemm256<1>(x, D, wg, wu, F, F, D, m0, f0, lds, acc, [] {});
    int tz = threadIdx.x; asm volatile("" : "+v"(tz));
    const int wr = tz >> 8, wc = (tz >> 6) & 3, ln = tz & 63;
    if (f0 + wc * 32 < F) {
      const int kk = (f0 + wc * 32) >> 5;
#pragma unroll
      for (int i = 0; i < 8; ++i) {
        u32x4 v;
#pragma unroll
        for (int j = 0; j < 2; ++j) {
          float hv[4];
#pragma unroll
          for (int r = 0; r < 4; ++r) {
            float gg = rbf(acc[i][j][r]), uu = rbf(acc[i][j + 2][r]);
            hv[r] = rbf(gelu_tanh(gg)) * uu;
          }
          v[2 * j] = pack2(hv[0], hv[1]); v[2 * j + 1] = pack2(hv[2], hv[3]);
        }
        const int t16 = (m0 + wr * 128 + i * 16) >> 4;
        *(u32x4*)(ws + OFF_HH + (size_t)(t16 * 54 + kk) * 1024 + ln * 16) = v;
      }
    }
  }
}

__global__ __launch_bounds__(512, 1) void k_post2(const char* __restrict__ ws, u16* __restrict__ out) {
  extern __shared__ __attribute__((aligned(16))) char lds[];
  const int tid = threadIdx.x, lane = tid & 63, wave = __builtin_amdgcn_readfirstlane(tid >> 6), ml = lane & 15, g = lane >> 4;
  const int b = blockIdx.x, xc = b & 7, idx = b >> 3, tt = xc * 4 + (idx >> 3), cgp = idx & 7;
  f32x4* X = (f32x4*)lds;
  f32x4 asum[2][2];
#pragma unroll
  for (int ph = 0; ph < 2; ++ph) {
    const int kt = ph == 0 ? 128 : 54, nk = ph == 0 ? 16 : (wave < 6 ? 7 : 6);
    const char* tb = ws + (ph ? OFF_HH : OFF_O) + (size_t)(tt * 4) * kt * 1024 + (size_t)wave * 1024 + lane * 16;
    const size_t tstr = (size_t)kt * 1024;
    const char* wb = ws + OFF_WF + ((size_t)(cgp * 5) * 182 + (ph ? 128 : 0) + wave) * 1024 + lane * 16;
    f32x4 acc[4][5];
#pragma unroll
    for (int i = 0; i < 4; ++i)
#pragma unroll
      for (int c = 0; c < 5; ++c) acc[i][c] = (f32x4){0, 0, 0, 0};
    b16x8 tf[4][4], wf[4][5];
    auto ldk = [&](int slot, int k) {
      const int kn = k < nk ? k : nk - 1;
#pragma unroll
      for (int i = 0; i < 4; ++i) tf[slot][i] = *(const b16x8*)(tb + i * tstr + (size_t)kn * 8192);
#pragma unroll
      for (int c = 0; c < 5; ++c) wf[slot][c] = *(const b16x8*)(wb + (size_t)c * 182 * 1024 + (size_t)kn * 8192);
    };
    ldk(0, 0); ldk(1, 1); ldk(2, 2);
    for (int k = 0; k < nk; k += 4) {
#pragma unroll
      for (int u = 0; u < 4; ++u) {
        ldk((u + 3) & 3, k + u + 3);
        if (k + u < nk) {
#pragma unroll
          for (int i = 0; i < 4; ++i)
#pragma unroll
            for (int c = 0; c < 5; ++c) acc[i][c] = mfma16(wf[u][c], tf[u][i], acc[i][c]);
        }
      }
    }
#pragma unroll
    for (int rd = 0; rd < 2; ++rd) {
#pragma unroll
      for (int t = 0; t < 10; ++t) { const int tl = rd * 10 + t; X[(wave * 10 + t) * 64 + lane] = acc[tl / 5][tl % 5]; }
      RBAR();
#pragma unroll
      for (int u = 0; u < 2; ++u) {
        const int t = wave + 8 * u;
        if (t < 10) {
          const f32x4* xp = X + t * 64 + lane;
          const f32x4 v = ((xp[0] + xp[640]) + (xp[1280] + xp[1920])) + ((xp[2560] + xp[3200]) + (xp[3840] + xp[4480]));
          if (ph == 0) asum[rd][u] = v;
          else {
            const int tl = rd * 10 + t, i = tl / 5, c = tl % 5;
            const int tok = tt * 64 + 16 * i + ml;
            const float gg = ((const float*)(ws + OFF_G))[tok], og = rbf(1.f - gg);
            float r[4];
#pragma unroll
            for (int q = 0; q < 4; ++q) { float ta = rbf(gg * rbf(asum[rd][u][q])), tb2 = rbf(og * rbf(v[q])); asm volatile("" : "+v"(ta), "+v"(tb2)); r[q] = ta + tb2; }
            *(u32x2*)(out + (size_t)tok * D + 80 * cgp + 16 * c + 4 * g) = (u32x2){pack2(r[0], r[1]), pack2(r[2], r[3])};
          }
        }
      }
      RBAR();
    }
  }
}

extern "C" void kernel_launch(void* const* d_in, const int* in_sizes, int n_in, void* d_out, int out_size, void* d_ws, size_t ws_size,
                              hipStream_t stream) {
  const u16* x = (const u16*)d_in[0];
  const u16* mem0 = (const u16*)d_in[1];
  const u16* Wq = (const u16*)d_in[2];
  const u16* Wk = (const u16*)d_in[3];
  const u16* Wv = (const u16*)d_in[4];
  const u16* Wo = (const u16*)d_in[5];
  const u16* wg = (const u16*)d_in[8];
  const u16* wu = (const u16*)d_in[9];
  const u16* wd = (const u16*)d_in[10];
  const u16* gw = (const u16*)d_in[11];
  char* ws = (char*)d_ws;
  hipFuncSetAttribute((const void*)k_pre, hipFuncAttributeMaxDynamicSharedMemorySize, 160 * 1024);
  k_pre<<<256, 512, 160 * 1024, stream>>>(x, Wk, Wv, Wo, wd, gw, ws);
  hipFuncSetAttribute((const void*)k_chain, hipFuncAttributeMaxDynamicSharedMemorySize, 156 * 1024);
  k_chain<<<256, 512, 156 * 1024, stream>>>(mem0, Wq, Wk, Wv, Wo, ws);
  hipFuncSetAttribute((const void*)k_post1, hipFuncAttributeMaxDynamicSharedMemorySize, 128 * 1024);
  k_post1<<<240, 512, 128 * 1024, stream>>>(x, Wq, wg, wu, ws);
  hipFuncSetAttribute((const void*)k_post2, hipFuncAttributeMaxDynamicSharedMemorySize, 80 * 1024);
  k_post2<<<256, 512, 80 * 1024, stream>>>(ws, (u16*)d_out);
}
```

```cpp
#include <hip/hip_runtime.h>
#include <hip/hip_bf16.h>
#include <stdint.h>
#include <stdio.h>

typedef unsigned short u16;
typedef __bf16 b16x8 __attribute__((ext_vector_type(8)));
typedef short s16x4 __attribute__((ext_vector_type(4)));
typedef float f32x4 __attribute__((ext_vector_type(4)));
typedef unsigned u32x4 __attribute__((ext_vector_type(4)));
typedef unsigned u32x2 __attribute__((ext_vector_type(2)));
#define LAS __attribute__((address_space(3)))
#define DI __device__ __forceinline__

constexpr int S = 2048, D = 640, H = 32, E = 128, HE = 4096, MM = 8, SEG = 32, NSEG = 64, F = 1728;
constexpr float SCALE = 0.08838834764831845f;

constexpr size_t OFF_KX = 0;
constexpr size_t OFF_VX = OFF_KX + (size_t)S * HE * 2;
constexpr size_t OFF_O  = OFF_VX + (size_t)S * HE * 2;
constexpr size_t OFF_HH = OFF_O + (size_t)S * HE * 2;
constexpr size_t OFF_WF = OFF_HH + (size_t)S * F * 2;
constexpr size_t OFF_G  = OFF_WF + (size_t)40 * 182 * 1024;
constexpr size_t OFF_P  = OFF_G + (size_t)S * 4;
constexpr size_t SZ_P1  = (size_t)H * 640 * 8 * 4;
constexpr size_t SZ_P   = 2 * SZ_P1;
constexpr size_t OFF_SENT = OFF_P;
constexpr size_t OFF_MEM = OFF_P + SZ_P;
constexpr size_t OFF_QM = OFF_MEM + (size_t)NSEG * 10240;
constexpr size_t OFF_END = OFF_QM + (size_t)NSEG * H * 6144;
constexpr size_t SZ_SENT = OFF_END - OFF_SENT;
constexpr size_t OFF_DBG = OFF_END;

DI float bf2f(u16 v) { return __builtin_bit_cast(float, (unsigned)v << 16); }
DI u16 f2bf(float f) { return __builtin_bit_cast(u16, (__bf16)f); }
DI float rbf(float f) { return (float)(__bf16)f; }
typedef float f32x2 __attribute__((ext_vector_type(2)));
typedef __bf16 b16x2 __attribute__((ext_vector_type(2)));
DI unsigned pack2(float lo, float hi) { f32x2 v = {lo, hi}; return __builtin_bit_cast(unsigned, __builtin_convertvector(v, b16x2)); }
DI f32x4 mfma16(b16x8 a, b16x8 b, f32x4 c) { return __builtin_amdgcn_mfma_f32_16x16x32_bf16(a, b, c, 0, 0, 0); }
DI b16x8 asb(u32x4 v) { return __builtin_bit_cast(b16x8, v); }

DI float gelu_tanh(float x) {
  float y = 0.7978845608028654f * (x + 0.044715f * x * x * x);
  float t = 1.f - 2.f / (__expf(2.f * y) + 1.f);
  return 0.5f * x * (1.f + t);
}

#define RBAR() asm volatile("s_waitcnt lgkmcnt(0)\n\ts_barrier" ::: "memory")
DI unsigned fB(unsigned row) { return (row & 3) | (((row >> 3) & 1) << 2); }
template <int BMODE>
DI void gemm256(const u16* __restrict__ A, int lda, const u16* __restrict__ B0, const u16* __restrict__ B1, int ldb, int ncols, int K,
                int m0, int n0, char* lds, f32x4 (&acc)[8][4]) {
  const int tid = threadIdx.x, lane = tid & 63, wave = __builtin_amdgcn_readfirstlane(tid >> 6), wr = wave >> 2, wc = wave & 3;
  const int fr = lane & 15, g = lane >> 4;
  unsigned offA[4], offB[2];
#pragma unroll
  for (int i = 0; i < 4; ++i) {
    const unsigned L = (unsigned)(i * 8 + wave) * 1024u + lane * 16u;
    const unsigned row = L >> 7, ch = (L >> 4) & 7; offA[i] = (m0 + row) * lda + ((ch ^ (row & 7)) * 8);
  }
#pragma unroll
  for (int i = 0; i < 2; ++i) {
    const unsigned L = (unsigned)(i * 8 + wave) * 1024u + lane * 16u;
    const unsigned row = L >> 8, chp = (L >> 4) & 15, ch = chp ^ (fB(row) << 1);
    int col = n0 + ch * 8; if (BMODE == 1) col = col < ncols ? col : ncols - 8;
    offB[i] = row * ldb + col;
  }
  const u16* Bs1 = BMODE == 0 ? B0 + 128 : B1;
  auto stage = [&](int buf, int k0) {
    const u16* Ak = A + k0; const u16* B0k = B0 + (size_t)k0 * ldb; const u16* B1k = Bs1 + (size_t)k0 * ldb;
#pragma unroll
    for (int i = 0; i < 4; ++i) {
      __builtin_amdgcn_global_load_lds((const unsigned*)(Ak + offA[i]), (LAS unsigned*)(lds + buf * 65536 + (i * 8 + wave) * 1024), 16, 0, 0);
      __builtin_amdgcn_global_load_lds((const unsigned*)((i < 2 ? B0k : B1k) + offB[i & 1]), (LAS unsigned*)(lds + buf * 65536 + 32768 + (i * 8 + wave) * 1024), 16, 0, 0);
    }
  };
  const unsigned ldsb = (unsigned)(size_t)lds;
  const unsigned q4 = fr >> 2, p4 = lane & 3;
  const int nt = K / 64;
  stage(0, 0);
  asm volatile("s_waitcnt vmcnt(0)" ::: "memory"); RBAR();
  if (wr == 1) RBAR();
  for (int t = 0; t < nt; ++t) {
    const int buf = t & 1;
    stage(buf ^ 1, t + 1 < nt ? (t + 1) * 64 : 0);
    const char* sA = lds + buf * 65536;
    const unsigned sB = ldsb + buf * 65536 + 32768;
#pragma unroll
    for (int ks = 0; ks < 2; ++ks) {
      b16x8 af[8], bfr[4];
#pragma unroll
      for (int jn = 0; jn < 4; ++jn) {
        const unsigned sub = BMODE == 0 ? (wc >> 1) : (jn >> 1), c = BMODE == 0 ? ((wc & 1) * 4 + jn) : (2 * wc + (jn & 1));
        const unsigned r0 = 32 * ks + 8 * g + q4, r1 = r0 + 4;
        s16x4 lo = __builtin_amdgcn_ds_read_tr16_b64_v4i16((LAS s16x4*)(sB + sub * 16384 + r0 * 256 + (((2 * c + (p4 >> 1)) ^ (fB(r0) << 1)) << 4) + 8 * (p4 & 1)));
        s16x4 hi = __builtin_amdgcn_ds_read_tr16_b64_v4i16((LAS s16x4*)(sB + sub * 16384 + r1 * 256 + (((2 * c + (p4 >> 1)) ^ (fB(r1) << 1)) << 4) + 8 * (p4 & 1)));
        typedef short s16x8 __attribute__((ext_vector_type(8)));
        s16x8 tt = {lo[0], lo[1], lo[2], lo[3], hi[0], hi[1], hi[2], hi[3]};
        bfr[jn] = __builtin_bit_cast(b16x8, tt);
      }
#pragma unroll
      for (int i = 0; i < 8; ++i) { const unsigned row = wr * 128 + i * 16 + fr; af[i] = *(const b16x8*)(sA + row * 128 + (((ks * 4 + g) ^ (row & 7)) << 4)); }
      if (ks == 1 && wr == 1) asm volatile("s_waitcnt vmcnt(0)" ::: "memory");
      RBAR();
      __builtin_amdgcn_s_setprio(1);
#pragma unroll
      for (int i = 0; i < 8; ++i)
#pragma unroll
        for (int jn = 0; jn < 4; ++jn) acc[i][jn] = mfma16(bfr[jn], af[i], acc[i][jn]);
      __builtin_amdgcn_s_setprio(0);
      if (ks == 1 && wr == 0) asm volatile("s_waitcnt vmcnt(0)" ::: "memory");
      RBAR();
    }
  }
  if (wr == 0) RBAR();
}

__global__ __launch_bounds__(512, 1) void k_pre(const u16* __restrict__ x, const u16* __restrict__ Wk, const u16* __restrict__ Wv,
                                                const u16* __restrict__ Wq, const u16* __restrict__ Wo, char* __restrict__ ws) {
  extern __shared__ __attribute__((aligned(16))) char lds[];
  const int b = blockIdx.x;
  const int nt = (b & 7) * 4 + (b >> 6), mt = (b >> 3) & 7, which = nt >> 4, n0 = (nt & 15) * 256, m0 = mt * 256;
  f32x4 acc[8][4];
#pragma unroll
  for (int i = 0; i < 8; ++i)
#pragma unroll
    for (int j = 0; j < 4; ++j) acc[i][j] = (f32x4){0, 0, 0, 0};
  gemm256<0>(x, D, which == 0 ? Wk : Wv, nullptr, HE, HE, D, m0, n0, lds, acc);
  char* out = ws + (which == 0 ? OFF_KX : OFF_VX);
  int tz = threadIdx.x; asm volatile("" : "+v"(tz));
  const int wr = tz >> 8, wc = (tz >> 6) & 3, fr = tz & 15, g = (tz >> 4) & 3;
#pragma unroll
  for (int i = 0; i < 8; ++i)
#pragma unroll
    for (int j = 0; j < 4; ++j) {
      u32x2 v = {pack2(acc[i][j][0], acc[i][j][1]), pack2(acc[i][j][2], acc[i][j][3])};
      const int tok = m0 + wr * 128 + i * 16 + fr, n = n0 + wc * 64 + j * 16 + 4 * g;
      const int seg = tok >> 5, kt = (tok >> 4) & 1, hh = n >> 7, e = n & 127;
      *(u32x2*)(out + (size_t)(seg * H + hh) * 8192 + (size_t)(((kt * 4 + (e >> 5)) * 64 + (tok & 15) + 16 * ((e & 31) >> 3)) * 16 + (e & 4) * 2)) = v;
    }
  const u32x4 ones = {0xFFFFFFFFu, 0xFFFFFFFFu, 0xFFFFFFFFu, 0xFFFFFFFFu};
  const __amdgpu_buffer_rsrc_t rsF = __builtin_amdgcn_make_buffer_rsrc(ws + OFF_SENT, 0, (int)SZ_SENT, 0x00020000);
#define FILL_AUX 16
#pragma unroll
  for (int t = 0; t < 7; ++t)
    __builtin_amdgcn_raw_buffer_store_b128(ones, rsF, (unsigned)(b * 512 + tz) * 16u + t * (256u * 512u * 16u), 0, FILL_AUX);
  static_assert(SZ_SENT <= (size_t)7 * 256 * 512 * 16, "fill");
}

#define SC1 16
DI u32x4 ld128(__amdgpu_buffer_rsrc_t rs, unsigned off) { return __builtin_amdgcn_raw_buffer_load_b128(rs, off, 0, SC1); }
DI u32x2 ld64(__amdgpu_buffer_rsrc_t rs, unsigned off) { return __builtin_amdgcn_raw_buffer_load_b64(rs, off, 0, SC1); }
DI void st128(__amdgpu_buffer_rsrc_t rs, unsigned off, u32x4 v) { __builtin_amdgcn_raw_buffer_store_b128(v, rs, off, 0, SC1); }
DI void st64(__amdgpu_buffer_rsrc_t rs, unsigned off, u32x2 v) { __builtin_amdgcn_raw_buffer_store_b64(v, rs, off, 0, SC1); }
DI unsigned nosent(unsigned v) { return v == 0xFFFFFFFFu ? 0xFFFEFFFFu : v; }
DI unsigned umax3(unsigned a, unsigned b, unsigned c) { return max(max(a, b), c); }
DI float xsum16(float x) { unsigned u = __float_as_uint(x); auto r = __builtin_amdgcn_permlane16_swap(u, u, false, false); return __uint_as_float(r[0]) + __uint_as_float(r[1]); }
DI float xsum32(float x) { unsigned u = __float_as_uint(x); auto r = __builtin_amdgcn_permlane32_swap(u, u, false, false); return __uint_as_float(r[0]) + __uint_as_float(r[1]); }
DI float xmax16(float x) { unsigned u = __float_as_uint(x); auto r = __builtin_amdgcn_permlane16_swap(u, u, false, false); return fmaxf(__uint_as_float(r[0]), __uint_as_float(r[1])); }
DI float xmax32(float x) { unsigned u = __float_as_uint(x); auto r = __builtin_amdgcn_permlane32_swap(u, u, false, false); return fmaxf(__uint_as_float(r[0]), __uint_as_float(r[1])); }
DI float ror4(float x) { return __uint_as_float(__builtin_amdgcn_update_dpp(0u, __float_as_uint(x), 0x124, 0xf, 0xf, false)); }
DI float ror8(float x) { return __uint_as_float(__builtin_amdgcn_update_dpp(0u, __float_as_uint(x), 0x128, 0xf, 0xf, false)); }
#define LBAR() asm volatile("s_waitcnt lgkmcnt(0)\n\ts_barrier" ::: "memory")
template <int N> DI void slp() { if constexpr (N > 0) { __builtin_amdgcn_s_sleep(N > 15 ? 15 : N); if constexpr (N > 15) slp<N - 15>(); } }

#define TS(i)
#define SPN(k)
constexpr unsigned SPIN_CAP = 1u << 17;
#define SLP_A 16
#define SLP_B 12
#define SLP_B3 4
#define SLP_C 11

DI void chain_role(const int cb, const u16* __restrict__ mem0, const u16* __restrict__ Wq, const u16* __restrict__ Wk,
                   const u16* __restrict__ Wv, const u16* __restrict__ Wo, char* __restrict__ ws, char* dyn_lds) {
  const int xcd = cb & 7, y = cb >> 3, h = xcd * 4 + (y >> 3), j = y & 7;
  const int tid = threadIdx.x, lane = tid & 63, wave = __builtin_amdgcn_readfirstlane(tid >> 6);
  const int ml = lane & 15, g = lane >> 4;
  const __amdgpu_buffer_rsrc_t rs = __builtin_amdgcn_make_buffer_rsrc(ws + OFF_P, 0, (int)(OFF_END - OFF_P), 0x00020000);
  constexpr unsigned R_P = 0, R_MEM = (unsigned)(OFF_MEM - OFF_P), R_QM = (unsigned)(OFF_QM - OFF_P);
  f32x4* X1 = (f32x4*)dyn_lds;
  char* KV = dyn_lds + 24576;
  char* WL = dyn_lds + 57344;
  const f32x4 z4 = {0, 0, 0, 0};
  const unsigned OOB = 0x7FFFFF00u;
  const unsigned lo_off = ml < 8 ? (unsigned)(g * 8 + ml) * 16u : OOB;
  unsigned spin_cap = SPIN_CAP;
  typedef short s16x8 __attribute__((ext_vector_type(8)));
  char* my = WL + (wave < 4 ? wave * 15360 : 61440 + (wave - 4) * 8192);
  const unsigned trb = (unsigned)(size_t)my + (8 * g + (ml >> 2)) * 32 + (lane & 3) * 8;
  auto trfrag = [&](int i) {
    s16x4 lo = __builtin_amdgcn_ds_read_tr16_b64_v4i16((LAS s16x4*)(trb + i * 1024));
    s16x4 hi = __builtin_amdgcn_ds_read_tr16_b64_v4i16((LAS s16x4*)(trb + i * 1024 + 128));
    s16x8 tt = {lo[0], lo[1], lo[2], lo[3], hi[0], hi[1], hi[2], hi[3]};
    return __builtin_bit_cast(b16x8, tt);
  };

  if (wave < 4) {
    b16x8 w1[3][5];
    {
      u32x4 t1[15];
#pragma unroll
      for (int t = 0; t < 3; ++t) {
        const u16* W = t == 0 ? Wq : (t == 1 ? Wk : Wv);
#pragma unroll
        for (int i = 0; i < 5; ++i) t1[t * 5 + i] = *(const u32x4*)(W + (size_t)(160 * wave + 32 * i + (lane >> 1)) * HE + h * E + 16 * j + (lane & 1) * 8);
      }
#pragma unroll
      for (int i = 0; i < 15; ++i) *(u32x4*)(my + i * 1024 + lane * 16) = t1[i];
      asm volatile("s_waitcnt lgkmcnt(0)" ::: "memory");
#pragma unroll
      for (int i = 0; i < 15; ++i) w1[i / 5][i % 5] = trfrag(i);
    }
    const char* kvsrc = ws + (wave == 2 ? OFF_KX : OFF_VX) + (size_t)h * 8192 + lane * 16;
    if (wave >= 2) {
#pragma unroll
      for (int sn = 0; sn < 2; ++sn) {
        u32x4 t[8];
#pragma unroll
        for (int i = 0; i < 8; ++i) t[i] = *(const u32x4*)(kvsrc + (size_t)sn * H * 8192 + i * 1024);
#pragma unroll
        for (int i = 0; i < 8; ++i) *(u32x4*)(KV + sn * 16384 + (wave - 2) * 8192 + i * 1024 + lane * 16) = t[i];
      }
    }
    LBAR();
    const int cc = lane & 7, hg = lane >> 3;
    for (int s = 0; s < NSEG; ++s) {
      TS(0);
      f32x4 ac[3];
      if (s == 0) {
#pragma unroll
        for (int u = 0; u < 3; ++u) ac[u] = z4;
#pragma unroll
        for (int kk = 0; kk < 5; ++kk) {
          u32x4 mf = *(const u32x4*)(mem0 + (ml & 7) * D + 32 * (5 * wave + kk) + 8 * g);
#pragma unroll
          for (int u = 0; u < 3; ++u) ac[u] = mfma16(w1[u][kk], asb(mf), ac[u]);
        }
      } else {
        const unsigned base = R_MEM + (unsigned)s * 10240u + (unsigned)wave * 2560u + lo_off;
        slp<SLP_A>();
        u32x4 mf[5];
        for (unsigned spin = 0;; ++spin) {
          asm volatile("" ::: "memory");
#pragma unroll
          for (int kk = 0; kk < 5; ++kk) mf[kk] = ld128(rs, base + kk * 512u);
          unsigned mx = 0;
#pragma unroll
          for (int kk = 0; kk < 5; ++kk) mx = umax3(mx, mf[kk][0], mf[kk][2]);
          if (__all(mx != 0xFFFFFFFFu) || spin >= spin_cap) { SPN(0); if (spin >= spin_cap) spin_cap = 0; break; }
        }
#pragma unroll
        for (int u = 0; u < 3; ++u) ac[u] = z4;
#pragma unroll
        for (int kk = 0; kk < 5; ++kk) {
#pragma unroll
          for (int u = 0; u < 3; ++u) ac[u] = mfma16(w1[u][kk], asb(mf[kk]), ac[u]);
        }
      }
      TS(1);
#pragma unroll
      for (int u = 0; u < 3; ++u) X1[u * 256 + tid] = ac[u];
      LBAR();
      if (wave < 2) {
        const int tl = 2 * wave + (ml >> 3);
        const f32x4* xp = X1 + (tl < 3 ? tl : 2) * 256 + g * 16 + (ml & 7);
        f32x4 a = (xp[0] + xp[64]) + (xp[128] + xp[192]);
        u32x2 v = {nosent(pack2(a[0], a[1])), nosent(pack2(a[2], a[3]))};
        unsigned off = R_QM + (unsigned)(s * H + h) * 6144u + (unsigned)tl * 2048u + (unsigned)(((j >> 1) * 4 + 2 * (j & 1) + (g >> 1)) * 8 + (ml & 7)) * 16u + (g & 1) * 8u;
        if (tl < 3) st64(rs, off, v);
      }
      TS(2);
      if (s == NSEG - 1) break;
      if (wave != 0) { LBAR(); LBAR(); } else {
        LBAR();
        const unsigned off = cc < 5 ? R_P + (unsigned)(s & 1) * (unsigned)SZ_P1 + (unsigned)hg * 81920u + (unsigned)(5 * cb + cc) * 16u : OOB;
        slp<SLP_C>();
        u32x4 v[4];
        for (unsigned spin = 0;; ++spin) {
          asm volatile("" ::: "memory");
#pragma unroll
          for (int i = 0; i < 4; ++i) v[i] = ld128(rs, off + i * 20480u);
          unsigned mx = 0;
#pragma unroll
          for (int i = 0; i < 4; ++i) { mx = umax3(mx, v[i][0], v[i][1]); mx = umax3(mx, v[i][2], v[i][3]); }
          if (__all(mx != 0xFFFFFFFFu) || spin >= spin_cap) { SPN(2); if (spin >= spin_cap) spin_cap = 0; break; }
        }
        TS(5);
        float f[4];
#pragma unroll
        for (int r = 0; r < 4; ++r) {
          float x = (__uint_as_float(v[0][r]) + __uint_as_float(v[1][r])) + (__uint_as_float(v[2][r]) + __uint_as_float(v[3][r]));
          x += ror8(x); x = xsum32(xsum16(x)); f[r] = x;
        }
        u32x2 w = {nosent(pack2(f[0], f[1])), nosent(pack2(f[2], f[3]))};
        if (lane < 5) st64(rs, R_MEM + (unsigned)(s + 1) * 10240u + (unsigned)(5 * cb + lane) * 8u, w);
        TS(6);
        LBAR();
        const u32x4 ones = {0xFFFFFFFFu, 0xFFFFFFFFu, 0xFFFFFFFFu, 0xFFFFFFFFu};
        if (cc < 5) {
#pragma unroll
          for (int i = 0; i < 4; ++i) st128(rs, off + i * 20480u, ones);
        }
      }
    }
    return;
  }
  const int w4 = wave - 4;
  const bool three = w4 < 1;
  b16x8 w2[2][4];
  {
    u32x4 t2[8];
#pragma unroll
    for (int t = 0; t < 2; ++t) {
      const int ct = (t < 1 || three) ? w4 + 4 * t : 0;
#pragma unroll
      for (int i = 0; i < 4; ++i) t2[t * 4 + i] = *(const u32x4*)(Wo + (size_t)(h * E + 32 * i + (lane >> 1)) * D + 80 * j + 16 * ct + (lane & 1) * 8);
    }
#pragma unroll
    for (int i = 0; i < 8; ++i) *(u32x4*)(my + i * 1024 + lane * 16) = t2[i];
    asm volatile("s_waitcnt lgkmcnt(0)" ::: "memory");
#pragma unroll
    for (int i = 0; i < 8; ++i) w2[i / 4][i % 4] = trfrag(i);
  }
  b16x8 kx[2][4]; u32x4 vwx[2];
  const b16x8 ones8 = asb((u32x4){0x3F803F80u, 0x3F803F80u, 0x3F803F80u, 0x3F803F80u});
  auto prep = [&](int sn) {
    const char* kb = KV + (sn & 1) * 16384 + lane * 16;
    b16x8 vf[2][4];
#pragma unroll
    for (int kt = 0; kt < 2; ++kt)
#pragma unroll
      for (int ks = 0; ks < 4; ++ks) { kx[kt][ks] = *(const b16x8*)(kb + (kt * 4 + ks) * 1024); vf[kt][ks] = *(const b16x8*)(kb + 8192 + (kt * 4 + ks) * 1024); }
#pragma unroll
    for (int t = 0; t < 2; ++t) {
      f32x4 d0 = z4, d1 = z4;
#pragma unroll
      for (int ks = 0; ks < 4; ++ks) { d0 = mfma16(vf[0][ks], w2[t][ks], d0); d1 = mfma16(vf[1][ks], w2[t][ks], d1); }
      vwx[t] = (u32x4){pack2(d0[0], d0[1]), pack2(d0[2], d0[3]), pack2(d1[0], d1[1]), pack2(d1[2], d1[3])};
    }
  };
  const char* kvs = ws + (w4 < 2 ? OFF_KX : OFF_VX) + (size_t)h * 8192 + (w4 & 1) * 4096 + lane * 16;
  u32x4 stg[4];
#pragma unroll
  for (int i = 0; i < 4; ++i) stg[i] = *(const u32x4*)(kvs + (size_t)2 * H * 8192 + i * 1024);
  LBAR();
  prep(0);
  for (int s = 0; s < NSEG - 1; ++s) {
    const unsigned hb = R_QM + (unsigned)(s * H + h) * 6144u + lo_off;
    LBAR();
    slp<SLP_B>();
    f32x4 a[2];
    u32x4 qf[4], kf[4], vf[4];
    for (unsigned spin = 0;; ++spin) {
      asm volatile("" ::: "memory");
#pragma unroll
      for (int ks = 0; ks < 4; ++ks) qf[ks] = ld128(rs, hb + ks * 512u);
#pragma unroll
      for (int ks = 0; ks < 4; ++ks) kf[ks] = ld128(rs, hb + 2048u + ks * 512u);
#pragma unroll
      for (int ks = 0; ks < 4; ++ks) vf[ks] = ld128(rs, hb + 4096u + ks * 512u);
      unsigned mx = 0;
#pragma unroll
      for (int ks = 0; ks < 4; ++ks) { mx = umax3(mx, qf[ks][0], qf[ks][2]); mx = umax3(mx, kf[ks][0], kf[ks][2]); mx = umax3(mx, vf[ks][0], vf[ks][2]); }
      if (__all(mx != 0xFFFFFFFFu) || spin >= spin_cap) { SPN(1); if (spin >= spin_cap) spin_cap = 0; break; }
    }
    {
#define SB() __builtin_amdgcn_sched_barrier(0)
      f32x4 s0 = z4, s1 = z4, sm = z4, dm[2] = {z4, z4};
#pragma unroll
      for (int ks = 0; ks < 4; ++ks) { s0 = mfma16(kx[0][ks], asb(qf[ks]), s0); s1 = mfma16(kx[1][ks], asb(qf[ks]), s1); SB(); }
#pragma unroll
      for (int ks = 0; ks < 4; ++ks) { sm = mfma16(asb(kf[ks]), asb(qf[ks]), sm); SB(); }
      TS(3);
      const float NEG = -INFINITY;
      const float c2 = SCALE * 1.4426950408889634f;
      float tv[12];
      dm[0] = mfma16(asb(vf[0]), w2[0][0], dm[0]);
#pragma unroll
      for (int r = 0; r < 4; ++r) tv[r] = s0[r] * c2;
      SB();
      dm[1] = mfma16(asb(vf[0]), w2[1][0], dm[1]);
#pragma unroll
      for (int r = 0; r < 4; ++r) tv[4 + r] = s1[r] * c2;
      SB();
      float m1 = fmaxf(fmaxf(tv[0], tv[1]), tv[2]), m2 = fmaxf(fmaxf(tv[3], tv[4]), tv[5]);
      m1 = fmaxf(fmaxf(m1, tv[6]), tv[7]);
      SB();
      dm[0] = mfma16(asb(vf[1]), w2[0][1], dm[0]);
#pragma unroll
      for (int r = 0; r < 4; ++r) tv[8 + r] = (g < 2) ? sm[r] * c2 : NEG;
      SB();
      dm[1] = mfma16(asb(vf[1]), w2[1][1], dm[1]);
      m2 = fmaxf(fmaxf(m2, tv[8]), tv[9]); m1 = fmaxf(fmaxf(m1, tv[10]), tv[11]);
      float mxv = fmaxf(m1, m2);
      { unsigned u = __float_as_uint(mxv); auto r = __builtin_amdgcn_permlane16_swap(u, u, false, false); mxv = fmaxf(__uint_as_float(r[0]) + 0.f, __uint_as_float(r[1]) + 0.f); }
      SB();
      { unsigned u = __float_as_uint(mxv); auto r = __builtin_amdgcn_permlane32_swap(u, u, false, false); mxv = fmaxf(__uint_as_float(r[0]) + 0.f, __uint_as_float(r[1]) + 0.f); }
      SB();
      dm[0] = mfma16(asb(vf[2]), w2[0][2], dm[0]);
#pragma unroll
      for (int r = 0; r < 2; ++r) tv[r] = __builtin_amdgcn_exp2f(tv[r] - mxv);
      SB();
      dm[1] = mfma16(asb(vf[2]), w2[1][2], dm[1]);
#pragma unroll
      for (int r = 2; r < 4; ++r) tv[r] = __builtin_amdgcn_exp2f(tv[r] - mxv);
      SB();
#pragma unroll
      for (int r = 4; r < 6; ++r) tv[r] = __builtin_amdgcn_exp2f(tv[r] - mxv);
      SB();
      dm[0] = mfma16(asb(vf[3]), w2[0][3], dm[0]);
#pragma unroll
      for (int r = 6; r < 8; ++r) tv[r] = __builtin_amdgcn_exp2f(tv[r] - mxv);
      SB();
      dm[1] = mfma16(asb(vf[3]), w2[1][3], dm[1]);
#pragma unroll
      for (int r = 8; r < 10; ++r) tv[r] = __builtin_amdgcn_exp2f(tv[r] - mxv);
      SB();
#pragma unroll
      for (int r = 10; r < 12; ++r) tv[r] = __builtin_amdgcn_exp2f(tv[r] - mxv);
      u32x4 px = {pack2(tv[0], tv[1]), pack2(tv[2], tv[3]), pack2(tv[4], tv[5]), pack2(tv[6], tv[7])};
      SB();
      f32x4 sx = mfma16(ones8, asb(px), z4);
      a[0] = mfma16(asb(vwx[0]), asb(px), z4);
      a[1] = mfma16(asb(vwx[1]), asb(px), z4);
      u32x4 pm = {pack2(tv[8], tv[9]), pack2(tv[10], tv[11]), 0u, 0u};
      SB();
      sx = mfma16(ones8, asb(pm), sx);
#pragma unroll
      for (int t = 0; t < 2; ++t) {
        u32x4 vwm = {pack2(dm[t][0], dm[t][1]), pack2(dm[t][2], dm[t][3]), 0u, 0u};
        a[t] = mfma16(asb(vwm), asb(pm), a[t]);
      }
      SB();
      const float inv = __builtin_amdgcn_rcpf(sx[0]);
      a[0] *= inv; a[1] *= inv;
    }
#pragma unroll
    for (int t = 0; t < 2; ++t) {
      if (t == 1 && !three) break;
      u32x4 v;
#pragma unroll
      for (int r = 0; r < 4; ++r) v[r] = min(__float_as_uint(a[t][r]), 0xFFFFFFFEu);
      const int T = 5 * j + w4 + 4 * t;
      unsigned off = R_P + (unsigned)(s & 1) * (unsigned)SZ_P1 + (unsigned)h * 20480u + (unsigned)(((T >> 1) * 4 + 2 * (T & 1) + (g >> 1)) * 8 + ml) * 32u + (unsigned)(g & 1) * 16u;
      if (ml < 8) st128(rs, off, v);
    }
    TS(4);
    LBAR();
    if (s + 1 < NSEG - 1) prep(s + 1);
    if (s + 2 < NSEG - 1) {
#pragma unroll
      for (int i = 0; i < 4; ++i) *(u32x4*)(KV + (s & 1) * 16384 + w4 * 4096 + i * 1024 + lane * 16) = stg[i];
    }
    LBAR();
    if (s + 3 < NSEG - 1) {
#pragma unroll
      for (int i = 0; i < 4; ++i) stg[i] = *(const u32x4*)(kvs + (size_t)(s + 3) * H * 8192 + i * 1024);
    }
  }
  LBAR();
}

__global__ __launch_bounds__(512, 1) void k_chain(const u16* __restrict__ mem0, const u16* __restrict__ Wq, const u16* __restrict__ Wk,
                                                   const u16* __restrict__ Wv, const u16* __restrict__ Wo, char* __restrict__ ws) {
  extern __shared__ __attribute__((aligned(16))) char dyn_lds[];
  chain_role(blockIdx.x, mem0, Wq, Wk, Wv, Wo, ws, dyn_lds);
}

DI void attn_unit(const int s, const int h, const b16x8 (&qf)[2][4], char* __restrict__ ws, char* V) {
  const int lane = threadIdx.x & 63, ml = lane & 15, g = lane >> 4;
  const char* KX = ws + OFF_KX + (size_t)(s * H + h) * 8192 + lane * 16;
  const char* VX = ws + OFF_VX + (size_t)(s * H + h) * 8192 + lane * 16;
  const char* KM = ws + OFF_QM + (size_t)(s * H + h) * 6144 + 2048 + (g * 8 + (ml & 7)) * 16;
  b16x8 kx[2][4], km[4];
#pragma unroll
  for (int ks = 0; ks < 4; ++ks) {
#pragma unroll
    for (int t = 0; t < 2; ++t) {
      kx[t][ks] = *(const b16x8*)(KX + (t * 4 + ks) * 1024);
      *(u32x4*)(V + (16 * t + ml) * 272 + (32 * ks + 8 * g) * 2) = *(const u32x4*)(VX + (t * 4 + ks) * 1024);
    }
    km[ks] = *(const b16x8*)(KM + ks * 512);
    u32x4 vmr = *(const u32x4*)(KM + 2048 + ks * 512);
    if (ml >= 8) vmr = (u32x4){0, 0, 0, 0};
    *(u32x4*)(V + (32 + ml) * 272 + (32 * ks + 8 * g) * 2) = vmr;
  }
  u32x4 px[2], pm[2];
#pragma unroll
  for (int qt = 0; qt < 2; ++qt) {
    f32x4 s0 = {0, 0, 0, 0}, s1 = {0, 0, 0, 0}, sm = {0, 0, 0, 0};
#pragma unroll
    for (int ks = 0; ks < 4; ++ks) {
      s0 = mfma16(kx[0][ks], qf[qt][ks], s0);
      s1 = mfma16(kx[1][ks], qf[qt][ks], s1);
      sm = mfma16(km[ks], qf[qt][ks], sm);
    }
    const int qi = 16 * qt + ml;
    float sv[12];
#pragma unroll
    for (int r = 0; r < 4; ++r) {
      sv[r] = (4 * g + r <= qi) ? rbf(rbf(s0[r]) * SCALE) : -INFINITY;
      sv[4 + r] = (16 + 4 * g + r <= qi) ? rbf(rbf(s1[r]) * SCALE) : -INFINITY;
      sv[8 + r] = (g < 2) ? rbf(rbf(sm[r]) * SCALE) : -INFINITY;
    }
    float mxv = sv[0];
#pragma unroll
    for (int r = 1; r < 12; ++r) mxv = fmaxf(mxv, sv[r]);
    mxv = xmax32(xmax16(mxv));
    float sum = 0.f;
#pragma unroll
    for (int r = 0; r < 12; ++r) { sv[r] = __expf(sv[r] - mxv); sum += sv[r]; }
    sum = xsum32(xsum16(sum));
    const float inv = 1.f / sum;
    px[qt] = (u32x4){pack2(sv[0] * inv, sv[1] * inv), pack2(sv[2] * inv, sv[3] * inv), pack2(sv[4] * inv, sv[5] * inv), pack2(sv[6] * inv, sv[7] * inv)};
    pm[qt] = (u32x4){pack2(sv[8] * inv, sv[9] * inv), pack2(sv[10] * inv, sv[11] * inv), 0u, 0u};
  }
  asm volatile("s_waitcnt lgkmcnt(0)" ::: "memory");
  const unsigned vb = (unsigned)(size_t)V + (4 * g + (ml >> 2)) * 272 + (lane & 3) * 8;
#pragma unroll
  for (int kk = 0; kk < 4; ++kk) {
    u32x4 of[2];
#pragma unroll
    for (int hf = 0; hf < 2; ++hf) {
      const int et = 2 * kk + hf;
      s16x4 v0 = __builtin_amdgcn_ds_read_tr16_b64_v4i16((LAS s16x4*)(vb + et * 32));
      s16x4 v1 = __builtin_amdgcn_ds_read_tr16_b64_v4i16((LAS s16x4*)(vb + 16 * 272 + et * 32));
      s16x4 v2 = __builtin_amdgcn_ds_read_tr16_b64_v4i16((LAS s16x4*)(vb + 32 * 272 + et * 32));
      typedef short s16x8 __attribute__((ext_vector_type(8)));
      s16x8 va = {v0[0], v0[1], v0[2], v0[3], v1[0], v1[1], v1[2], v1[3]};
      s16x8 vbm = {v2[0], v2[1], v2[2], v2[3], 0, 0, 0, 0};
#pragma unroll
      for (int qt = 0; qt < 2; ++qt) {
        f32x4 o = {0, 0, 0, 0};
        o = mfma16(__builtin_bit_cast(b16x8, va), asb(px[qt]), o);
        o = mfma16(__builtin_bit_cast(b16x8, vbm), asb(pm[qt]), o);
        of[qt][2 * hf] = pack2(o[0], o[1]); of[qt][2 * hf + 1] = pack2(o[2], o[3]);
      }
    }
#pragma unroll
    for (int qt = 0; qt < 2; ++qt) *(u32x4*)(ws + OFF_O + (size_t)((s * 2 + qt) * 128 + h * 4 + kk) * 1024 + lane * 16) = of[qt];
  }
  asm volatile("s_waitcnt lgkmcnt(0)" ::: "memory");
}

__global__ __launch_bounds__(512, 1) void k_post1(const u16* __restrict__ x, const u16* __restrict__ Wq, const u16* __restrict__ wg,
                                                  const u16* __restrict__ wu, const u16* __restrict__ Wo, const u16* __restrict__ wd,
                                                  const u16* __restrict__ gate_w, char* __restrict__ ws) {
  extern __shared__ __attribute__((aligned(16))) char lds[];
  const int b = blockIdx.x, xc = b & 7, idx = b >> 3;
  if (idx >= 30) {
    const int lane = threadIdx.x & 63, wave = threadIdx.x >> 6, ml = lane & 15, g = lane >> 4;
    const int gw = (b - 240) * 8 + wave;
    for (int t = gw; t < S; t += 128) {
      float s = 0.f;
#pragma unroll
      for (int i = 0; i < 5; ++i) {
        const unsigned xv = *(const unsigned*)(x + (size_t)t * D + i * 128 + lane * 2), gv = *(const unsigned*)(gate_w + i * 128 + lane * 2);
        s += rbf(__uint_as_float(xv << 16) * __uint_as_float(gv << 16)) + rbf(__uint_as_float(xv & 0xFFFF0000u) * __uint_as_float(gv & 0xFFFF0000u));
      }
#pragma unroll
      for (int o = 32; o >= 1; o >>= 1) s += __shfl_xor(s, o);
      s = rbf(s);
      float gg = rbf(1.f / (1.f + __expf(-s)));
      if (lane == 0) ((float*)(ws + OFF_G))[t] = gg;
    }
    char* my = lds + wave * 8192;
    const unsigned trb = (unsigned)(size_t)my + (4 * g + (ml >> 2)) * 32 + (lane & 3) * 8;
    for (int u = gw; u < 182 * 5; u += 128) {
      const int kk = u / 5, c0 = (u % 5) * 8;
      const u16* W = kk < 128 ? Wo + (size_t)32 * kk * D : wd + (size_t)32 * (kk - 128) * D;
      u32x4 t8[8];
#pragma unroll
      for (int i = 0; i < 8; ++i) t8[i] = *(const u32x4*)(W + (size_t)(4 * i + (lane >> 4)) * D + 16 * c0 + (lane & 15) * 8);
#pragma unroll
      for (int i = 0; i < 8; ++i) *(u32x4*)(my + ((lane & 15) >> 1) * 1024 + (4 * i + (lane >> 4)) * 32 + (lane & 1) * 16) = t8[i];
      asm volatile("s_waitcnt lgkmcnt(0)" ::: "memory");
#pragma unroll
      for (int i = 0; i < 8; ++i) {
        s16x4 lo = __builtin_amdgcn_ds_read_tr16_b64_v4i16((LAS s16x4*)(trb + i * 1024));
        s16x4 hi = __builtin_amdgcn_ds_read_tr16_b64_v4i16((LAS s16x4*)(trb + i * 1024 + 512));
        typedef short s16x8 __attribute__((ext_vector_type(8)));
        s16x8 tt = {lo[0], lo[1], lo[2], lo[3], hi[0], hi[1], hi[2], hi[3]};
        *(s16x8*)(ws + OFF_WF + ((size_t)(c0 + i) * 182 + kk) * 1024 + lane * 16) = tt;
      }
      asm volatile("s_waitcnt lgkmcnt(0)" ::: "memory");
    }
    return;
  }
  f32x4 acc[8][4];
#pragma unroll
  for (int i = 0; i < 8; ++i)
#pragma unroll
    for (int j = 0; j < 4; ++j) acc[i][j] = (f32x4){0, 0, 0, 0};
  if (idx < 16) {
    const int nt = xc * 2 + (idx >> 3), mt = idx & 7, n0 = nt * 256, m0 = mt * 256;
    gemm256<0>(x, D, Wq, nullptr, HE, HE, D, m0, n0, lds, acc);
    int tz = threadIdx.x; asm volatile("" : "+v"(tz));
    const int wr = tz >> 8, wc = (tz >> 6) & 3, fr = tz & 15, g = (tz >> 4) & 3, lane = tz & 63, wave = __builtin_amdgcn_readfirstlane(tz >> 6);
#pragma unroll
    for (int i = 0; i < 8; ++i)
#pragma unroll
      for (int j = 0; j < 4; ++j) {
        u32x2 v = {pack2(acc[i][j][0], acc[i][j][1]), pack2(acc[i][j][2], acc[i][j][3])};
        const int tok = wr * 128 + i * 16 + fr, n = wc * 64 + j * 16 + 4 * g;
        const int seg = tok >> 5, kt = (tok >> 4) & 1, hh = n >> 7, e = n & 127;
        *(u32x2*)(lds + (seg * 2 + hh) * 8192 + (((kt * 4 + (e >> 5)) * 64 + (tok & 15) + 16 * ((e & 31) >> 3)) * 16 + (e & 4) * 2)) = v;
      }
    RBAR();
    b16x8 q0[2][4], q1[2][4];
#pragma unroll
    for (int t = 0; t < 2; ++t)
#pragma unroll
      for (int ks = 0; ks < 4; ++ks) {
        q0[t][ks] = *(const b16x8*)(lds + (wave * 2) * 8192 + (t * 4 + ks) * 1024 + lane * 16);
        q1[t][ks] = *(const b16x8*)(lds + (wave * 2 + 1) * 8192 + (t * 4 + ks) * 1024 + lane * 16);
      }
    RBAR();
    char* V = lds + wave * (48 * 272);
    attn_unit(mt * 8 + wave, nt * 2, q0, ws, V);
    attn_unit(mt * 8 + wave, nt * 2 + 1, q1, ws, V);
  } else {
    const int tile = xc * 14 + (idx - 16), ft = tile >> 3, mt = tile & 7, f0 = ft * 128, m0 = mt * 256;
    gemm256<1>(x, D, wg, wu, F, F, D, m0, f0, lds, acc);
    int tz = threadIdx.x; asm volatile("" : "+v"(tz));
    const int wr = tz >> 8, wc = (tz >> 6) & 3, ln = tz & 63;
    if (f0 + wc * 32 < F) {
      const int kk = (f0 + wc * 32) >> 5;
#pragma unroll
      for (int i = 0; i < 8; ++i) {
        u32x4 v;
#pragma unroll
        for (int j = 0; j < 2; ++j) {
          float hv[4];
#pragma unroll
          for (int r = 0; r < 4; ++r) {
            float gg = rbf(acc[i][j][r]), uu = rbf(acc[i][j + 2][r]);
            hv[r] = rbf(gelu_tanh(gg)) * uu;
          }
          v[2 * j] = pack2(hv[0], hv[1]); v[2 * j + 1] = pack2(hv[2], hv[3]);
        }
        const int t16 = (m0 + wr * 128 + i * 16) >> 4;
        *(u32x4*)(ws + OFF_HH + (size_t)(t16 * 54 + kk) * 1024 + ln * 16) = v;
      }
    }
  }
}

__global__ __launch_bounds__(512, 1) void k_post2(const char* __restrict__ ws, u16* __restrict__ out) {
  extern __shared__ __attribute__((aligned(16))) char lds[];
  const int tid = threadIdx.x, lane = tid & 63, wave = __builtin_amdgcn_readfirstlane(tid >> 6), ml = lane & 15, g = lane >> 4;
  const int b = blockIdx.x, xc = b & 7, idx = b >> 3, tt = xc * 4 + (idx >> 3), cgp = idx & 7;
  f32x4* X = (f32x4*)lds;
  f32x4 asum[2][2];
#pragma unroll
  for (int ph = 0; ph < 2; ++ph) {
    const int kt = ph == 0 ? 128 : 54, nk = ph == 0 ? 16 : (wave < 6 ? 7 : 6);
    const char* tb = ws + (ph ? OFF_HH : OFF_O) + (size_t)(tt * 4) * kt * 1024 + (size_t)wave * 1024 + lane * 16;
    const size_t tstr = (size_t)kt * 1024;
    const char* wb = ws + OFF_WF + ((size_t)(cgp * 5) * 182 + (ph ? 128 : 0) + wave) * 1024 + lane * 16;
    f32x4 acc[4][5];
#pragma unroll
    for (int i = 0; i < 4; ++i)
#pragma unroll
      for (int c = 0; c < 5; ++c) acc[i][c] = (f32x4){0, 0, 0, 0};
    b16x8 tf[4][4], wf[4][5];
    auto ldk = [&](int slot, int k) {
      const int kn = k < nk ? k : nk - 1;
#pragma unroll
      for (int i = 0; i < 4; ++i) tf[slot][i] = *(const b16x8*)(tb + i * tstr + (size_t)kn * 8192);
#pragma unroll
      for (int c = 0; c < 5; ++c) wf[slot][c] = *(const b16x8*)(wb + (size_t)c * 182 * 1024 + (size_t)kn * 8192);
    };
    ldk(0, 0); ldk(1, 1); ldk(2, 2);
    for (int k = 0; k < nk; k += 4) {
#pragma unroll
      for (int u = 0; u < 4; ++u) {
        ldk((u + 3) & 3, k + u + 3);
        if (k + u < nk) {
#pragma unroll
          for (int i = 0; i < 4; ++i)
#pragma unroll
            for (int c = 0; c < 5; ++c) acc[i][c] = mfma16(wf[u][c], tf[u][i], acc[i][c]);
        }
      }
    }
#pragma unroll
    for (int rd = 0; rd < 2; ++rd) {
#pragma unroll
      for (int t = 0; t < 10; ++t) { const int tl = rd * 10 + t; X[(wave * 10 + t) * 64 + lane] = acc[tl / 5][tl % 5]; }
      RBAR();
#pragma unroll
      for (int u = 0; u < 2; ++u) {
        const int t = wave + 8 * u;
        if (t < 10) {
          const f32x4* xp = X + t * 64 + lane;
          const f32x4 v = ((xp[0] + xp[640]) + (xp[1280] + xp[1920])) + ((xp[2560] + xp[3200]) + (xp[3840] + xp[4480]));
          if (ph == 0) asum[rd][u] = v;
          else {
            const int tl = rd * 10 + t, i = tl / 5, c = tl % 5;
            const int tok = tt * 64 + 16 * i + ml;
            const float gg = ((const float*)(ws + OFF_G))[tok], og = rbf(1.f - gg);
            float r[4];
#pragma unroll
            for (int q = 0; q < 4; ++q) { float ta = rbf(gg * rbf(asum[rd][u][q])), tb2 = rbf(og * rbf(v[q])); asm volatile("" : "+v"(ta), "+v"(tb2)); r[q] = ta + tb2; }
            *(u32x2*)(out + (size_t)tok * D + 80 * cgp + 16 * c + 4 * g) = (u32x2){pack2(r[0], r[1]), pack2(r[2], r[3])};
          }
        }
      }
      RBAR();
    }
  }
}

extern "C" void kernel_launch(void* const* d_in, const int* in_sizes, int n_in, void* d_out, int out_size, void* d_ws, size_t ws_size,
                              hipStream_t stream) {
  const u16* x = (const u16*)d_in[0];
  const u16* mem0 = (const u16*)d_in[1];
  const u16* Wq = (const u16*)d_in[2];
  const u16* Wk = (const u16*)d_in[3];
  const u16* Wv = (const u16*)d_in[4];
  const u16* Wo = (const u16*)d_in[5];
  const u16* wg = (const u16*)d_in[8];
  const u16* wu = (const u16*)d_in[9];
  const u16* wd = (const u16*)d_in[10];
  const u16* gw = (const u16*)d_in[11];
  char* ws = (char*)d_ws;
  hipFuncSetAttribute((const void*)k_pre, hipFuncAttributeMaxDynamicSharedMemorySize, 128 * 1024);
  k_pre<<<256, 512, 128 * 1024, stream>>>(x, Wk, Wv, Wq, Wo, ws);
  hipFuncSetAttribute((const void*)k_chain, hipFuncAttributeMaxDynamicSharedMemorySize, 156 * 1024);
  k_chain<<<256, 512, 156 * 1024, stream>>>(mem0, Wq, Wk, Wv, Wo, ws);
  hipFuncSetAttribute((const void*)k_post1, hipFuncAttributeMaxDynamicSharedMemorySize, 128 * 1024);
  k_post1<<<256, 512, 128 * 1024, stream>>>(x, Wq, wg, wu, Wo, wd, gw, ws);
  hipFuncSetAttribute((const void*)k_post2, hipFuncAttributeMaxDynamicSharedMemorySize, 80 * 1024);
  k_post2<<<256, 512, 80 * 1024, stream>>>(ws, (u16*)d_out);
}
```

```cpp
#include <hip/hip_runtime.h>
#include <hip/hip_bf16.h>
#include <stdint.h>
#include <stdio.h>

typedef unsigned short u16;
typedef __bf16 b16x8 __attribute__((ext_vector_type(8)));
typedef short s16x4 __attribute__((ext_vector_type(4)));
typedef float f32x4 __attribute__((ext_vector_type(4)));
typedef unsigned u32x4 __attribute__((ext_vector_type(4)));
typedef unsigned u32x2 __attribute__((ext_vector_type(2)));
#define LAS __attribute__((address_space(3)))
#define DI __device__ __forceinline__

constexpr int S = 2048, D = 640, H = 32, E = 128, HE = 4096, MM = 8, SEG = 32, NSEG = 64, F = 1728;
constexpr float SCALE = 0.08838834764831845f;

constexpr size_t OFF_KX = 0;
constexpr size_t OFF_VX = OFF_KX + (size_t)S * HE * 2;
constexpr size_t OFF_O  = OFF_VX + (size_t)S * HE * 2;
constexpr size_t OFF_HH = OFF_O + (size_t)S * HE * 2;
constexpr size_t OFF_WF = OFF_HH + (size_t)S * F * 2;
constexpr size_t OFF_G  = OFF_WF + (size_t)40 * 182 * 1024;
constexpr size_t OFF_QM = OFF_G + (size_t)S * 4;
constexpr size_t OFF_COMM = OFF_QM + (size_t)NSEG * H * 6144;
constexpr size_t OFF_END = OFF_COMM + (size_t)8 * 1048576;
constexpr size_t OFF_DBG = OFF_END;
constexpr unsigned PAGE = 2097152u, C_MEM = 65536u, C_QKV = 196608u, C_P = 65536u;
DI char* comm_base(char* ws) { return (char*)(((size_t)(ws + OFF_COMM) + (PAGE - 1)) & ~(size_t)(PAGE - 1)); }

DI float bf2f(u16 v) { return __builtin_bit_cast(float, (unsigned)v << 16); }
DI u16 f2bf(float f) { return __builtin_bit_cast(u16, (__bf16)f); }
DI float rbf(float f) { return (float)(__bf16)f; }
typedef float f32x2 __attribute__((ext_vector_type(2)));
typedef __bf16 b16x2 __attribute__((ext_vector_type(2)));
DI unsigned pack2(float lo, float hi) { f32x2 v = {lo, hi}; return __builtin_bit_cast(unsigned, __builtin_convertvector(v, b16x2)); }
DI f32x4 mfma16(b16x8 a, b16x8 b, f32x4 c) { return __builtin_amdgcn_mfma_f32_16x16x32_bf16(a, b, c, 0, 0, 0); }
DI b16x8 asb(u32x4 v) { return __builtin_bit_cast(b16x8, v); }

DI float gelu_tanh(float x) {
  float y = 0.7978845608028654f * (x + 0.044715f * x * x * x);
  float t = 1.f - 2.f / (__expf(2.f * y) + 1.f);
  return 0.5f * x * (1.f + t);
}

#define RBAR() asm volatile("s_waitcnt lgkmcnt(0)\n\ts_barrier" ::: "memory")
DI unsigned fB(unsigned row) { return (row & 3) | (((row >> 3) & 1) << 2); }
template <int BMODE>
DI void gemm256(const u16* __restrict__ A, int lda, const u16* __restrict__ B0, const u16* __restrict__ B1, int ldb, int ncols, int K,
                int m0, int n0, char* lds, f32x4 (&acc)[8][4]) {
  const int tid = threadIdx.x, lane = tid & 63, wave = __builtin_amdgcn_readfirstlane(tid >> 6), wr = wave >> 2, wc = wave & 3;
  const int fr = lane & 15, g = lane >> 4;
  unsigned offA[4], offB[2];
#pragma unroll
  for (int i = 0; i < 4; ++i) {
    const unsigned L = (unsigned)(i * 8 + wave) * 1024u + lane * 16u;
    const unsigned row = L >> 7, ch = (L >> 4) & 7; offA[i] = (m0 + row) * lda + ((ch ^ (row & 7)) * 8);
  }
#pragma unroll
  for (int i = 0; i < 2; ++i) {
    const unsigned L = (unsigned)(i * 8 + wave) * 1024u + lane * 16u;
    const unsigned row = L >> 8, chp = (L >> 4) & 15, ch = chp ^ (fB(row) << 1);
    int col = n0 + ch * 8; if (BMODE == 1) col = col < ncols ? col : ncols - 8;
    offB[i] = row * ldb + col;
  }
  const u16* Bs1 = BMODE == 0 ? B0 + 128 : B1;
  auto stage = [&](int buf, int k0) {
    const u16* Ak = A + k0; const u16* B0k = B0 + (size_t)k0 * ldb; const u16* B1k = Bs1 + (size_t)k0 * ldb;
#pragma unroll
    for (int i = 0; i < 4; ++i) {
      __builtin_amdgcn_global_load_lds((const unsigned*)(Ak + offA[i]), (LAS unsigned*)(lds + buf * 65536 + (i * 8 + wave) * 1024), 16, 0, 0);
      __builtin_amdgcn_global_load_lds((const unsigned*)((i < 2 ? B0k : B1k) + offB[i & 1]), (LAS unsigned*)(lds + buf * 65536 + 32768 + (i * 8 + wave) * 1024), 16, 0, 0);
    }
  };
  const unsigned ldsb = (unsigned)(size_t)lds;
  const unsigned q4 = fr >> 2, p4 = lane & 3;
  const int nt = K / 64;
  stage(0, 0);
  asm volatile("s_waitcnt vmcnt(0)" ::: "memory"); RBAR();
  if (wr == 1) RBAR();
  for (int t = 0; t < nt; ++t) {
    const int buf = t & 1;
    stage(buf ^ 1, t + 1 < nt ? (t + 1) * 64 : 0);
    const char* sA = lds + buf * 65536;
    const unsigned sB = ldsb + buf * 65536 + 32768;
#pragma unroll
    for (int ks = 0; ks < 2; ++ks) {
      b16x8 af[8], bfr[4];
#pragma unroll
      for (int jn = 0; jn < 4; ++jn) {
        const unsigned sub = BMODE == 0 ? (wc >> 1) : (jn >> 1), c = BMODE == 0 ? ((wc & 1) * 4 + jn) : (2 * wc + (jn & 1));
        const unsigned r0 = 32 * ks + 8 * g + q4, r1 = r0 + 4;
        s16x4 lo = __builtin_amdgcn_ds_read_tr16_b64_v4i16((LAS s16x4*)(sB + sub * 16384 + r0 * 256 + (((2 * c + (p4 >> 1)) ^ (fB(r0) << 1)) << 4) + 8 * (p4 & 1)));
        s16x4 hi = __builtin_amdgcn_ds_read_tr16_b64_v4i16((LAS s16x4*)(sB + sub * 16384 + r1 * 256 + (((2 * c + (p4 >> 1)) ^ (fB(r1) << 1)) << 4) + 8 * (p4 & 1)));
        typedef short s16x8 __attribute__((ext_vector_type(8)));
        s16x8 tt = {lo[0], lo[1], lo[2], lo[3], hi[0], hi[1], hi[2], hi[3]};
        bfr[jn] = __builtin_bit_cast(b16x8, tt);
      }
#pragma unroll
      for (int i = 0; i < 8; ++i) { const unsigned row = wr * 128 + i * 16 + fr; af[i] = *(const b16x8*)(sA + row * 128 + (((ks * 4 + g) ^ (row & 7)) << 4)); }
      if (ks == 1 && wr == 1) asm volatile("s_waitcnt vmcnt(0)" ::: "memory");
      RBAR();
      __builtin_amdgcn_s_setprio(1);
#pragma unroll
      for (int i = 0; i < 8; ++i)
#pragma unroll
        for (int jn = 0; jn < 4; ++jn) acc[i][jn] = mfma16(bfr[jn], af[i], acc[i][jn]);
      __builtin_amdgcn_s_setprio(0);
      if (ks == 1 && wr == 0) asm volatile("s_waitcnt vmcnt(0)" ::: "memory");
      RBAR();
    }
  }
  if (wr == 0) RBAR();
}

__global__ __launch_bounds__(512, 1) void k_pre(const u16* __restrict__ x, const u16* __restrict__ Wk, const u16* __restrict__ Wv,
                                                const u16* __restrict__ Wq, const u16* __restrict__ Wo, char* __restrict__ ws) {
  extern __shared__ __attribute__((aligned(16))) char lds[];
  const int b = blockIdx.x;
  const int nt = (b & 7) * 4 + (b >> 6), mt = (b >> 3) & 7, which = nt >> 4, n0 = (nt & 15) * 256, m0 = mt * 256;
  f32x4 acc[8][4];
#pragma unroll
  for (int i = 0; i < 8; ++i)
#pragma unroll
    for (int j = 0; j < 4; ++j) acc[i][j] = (f32x4){0, 0, 0, 0};
  gemm256<0>(x, D, which == 0 ? Wk : Wv, nullptr, HE, HE, D, m0, n0, lds, acc);
  char* out = ws + (which == 0 ? OFF_KX : OFF_VX);
  int tz = threadIdx.x; asm volatile("" : "+v"(tz));
  const int wr = tz >> 8, wc = (tz >> 6) & 3, fr = tz & 15, g = (tz >> 4) & 3;
#pragma unroll
  for (int i = 0; i < 8; ++i)
#pragma unroll
    for (int j = 0; j < 4; ++j) {
      u32x2 v = {pack2(acc[i][j][0], acc[i][j][1]), pack2(acc[i][j][2], acc[i][j][3])};
      const int tok = m0 + wr * 128 + i * 16 + fr, n = n0 + wc * 64 + j * 16 + 4 * g;
      const int seg = tok >> 5, kt = (tok >> 4) & 1, hh = n >> 7, e = n & 127;
      *(u32x2*)(out + (size_t)(seg * H + hh) * 8192 + (size_t)(((kt * 4 + (e >> 5)) * 64 + (tok & 15) + 16 * ((e & 31) >> 3)) * 16 + (e & 4) * 2)) = v;
    }
  const u32x4 ones = {0xFFFFFFFFu, 0xFFFFFFFFu, 0xFFFFFFFFu, 0xFFFFFFFFu};
  char* cbp = comm_base(ws);
#pragma unroll
  for (int t = 0; t < 3; ++t) *(u32x4*)(cbp + (size_t)t * PAGE + (size_t)(b * 512 + tz) * 16) = ones;
}

#define SC1 16
DI u32x4 ld128(__amdgpu_buffer_rsrc_t rs, unsigned off) { return __builtin_amdgcn_raw_buffer_load_b128(rs, off, 0, SC1); }
DI u32x2 ld64(__amdgpu_buffer_rsrc_t rs, unsigned off) { return __builtin_amdgcn_raw_buffer_load_b64(rs, off, 0, SC1); }
DI void st128(__amdgpu_buffer_rsrc_t rs, unsigned off, u32x4 v) { __builtin_amdgcn_raw_buffer_store_b128(v, rs, off, 0, SC1); }
DI void st64(__amdgpu_buffer_rsrc_t rs, unsigned off, u32x2 v) { __builtin_amdgcn_raw_buffer_store_b64(v, rs, off, 0, SC1); }
DI unsigned nosent(unsigned v) { return v == 0xFFFFFFFFu ? 0xFFFEFFFFu : v; }
DI unsigned umax3(unsigned a, unsigned b, unsigned c) { return max(max(a, b), c); }
DI float xsum16(float x) { unsigned u = __float_as_uint(x); auto r = __builtin_amdgcn_permlane16_swap(u, u, false, false); return __uint_as_float(r[0]) + __uint_as_float(r[1]); }
DI float xsum32(float x) { unsigned u = __float_as_uint(x); auto r = __builtin_amdgcn_permlane32_swap(u, u, false, false); return __uint_as_float(r[0]) + __uint_as_float(r[1]); }
DI float xmax16(float x) { unsigned u = __float_as_uint(x); auto r = __builtin_amdgcn_permlane16_swap(u, u, false, false); return fmaxf(__uint_as_float(r[0]), __uint_as_float(r[1])); }
DI float xmax32(float x) { unsigned u = __float_as_uint(x); auto r = __builtin_amdgcn_permlane32_swap(u, u, false, false); return fmaxf(__uint_as_float(r[0]), __uint_as_float(r[1])); }
DI float ror4(float x) { return __uint_as_float(__builtin_amdgcn_update_dpp(0u, __float_as_uint(x), 0x124, 0xf, 0xf, false)); }
DI float ror8(float x) { return __uint_as_float(__builtin_amdgcn_update_dpp(0u, __float_as_uint(x), 0x128, 0xf, 0xf, false)); }
#define LBAR() asm volatile("s_waitcnt lgkmcnt(0)\n\ts_barrier" ::: "memory")
template <int N> DI void slp() { if constexpr (N > 0) { __builtin_amdgcn_s_sleep(N > 15 ? 15 : N); if constexpr (N > 15) slp<N - 15>(); } }

#define TS(i)
#define SPN(k)
constexpr unsigned SPIN_CAP = 1u << 17;
#define SLP_A 15
#define SLP_B 12
#define SLP_B3 4
#define SLP_C 12

DI void chain_role(const int cb, const u16* __restrict__ mem0, const u16* __restrict__ Wq, const u16* __restrict__ Wk,
                   const u16* __restrict__ Wv, const u16* __restrict__ Wo, char* __restrict__ ws, char* dyn_lds) {
  const int xcd = cb & 7, y = cb >> 3, h = xcd * 4 + (y >> 3), j = y & 7;
  const int tid = threadIdx.x, lane = tid & 63, wave = __builtin_amdgcn_readfirstlane(tid >> 6);
  const int ml = lane & 15, g = lane >> 4;
  const __amdgpu_buffer_rsrc_t rs = __builtin_amdgcn_make_buffer_rsrc(comm_base(ws), 0, (int)(3 * PAGE), 0x00020000);
  unsigned selA, selP0, selP1;
  {
    unsigned* pl = (unsigned*)(dyn_lds + 156 * 1024 - 64);
    if (wave == 0) {
#pragma unroll 1
      for (int p = 0; p < 6; ++p) {
        const unsigned po = (unsigned)(p >> 1) * PAGE + (unsigned)(cb >> 6) * 16384u + (unsigned)(p & 1) * 8192u + (unsigned)(cb & 63) * 128u;
        asm volatile("s_waitcnt vmcnt(0) lgkmcnt(0)" ::: "memory");
        const unsigned long long t0 = __builtin_amdgcn_s_memtime();
        asm volatile("s_waitcnt lgkmcnt(0)" ::: "memory");
        const unsigned v = __builtin_amdgcn_raw_buffer_load_b32(rs, po, 0, SC1);
        asm volatile("s_waitcnt vmcnt(0)" :: "v"(v) : "memory");
        const unsigned long long t1 = __builtin_amdgcn_s_memtime();
        if (lane == 0) pl[p] = (unsigned)(t1 - t0);
      }
    }
    LBAR();
    selA = __builtin_amdgcn_readfirstlane(pl[1] < pl[0] ? 8192u : 0u);
    selP0 = __builtin_amdgcn_readfirstlane(pl[3] < pl[2] ? 8192u : 0u);
    selP1 = __builtin_amdgcn_readfirstlane(pl[5] < pl[4] ? 8192u : 0u);
  }
  const u32x4 ones4 = {0xFFFFFFFFu, 0xFFFFFFFFu, 0xFFFFFFFFu, 0xFFFFFFFFu};
  const u32x2 ones2 = {0xFFFFFFFFu, 0xFFFFFFFFu};
  f32x4* X1 = (f32x4*)dyn_lds;
  char* KV = dyn_lds + 24576;
  char* WL = dyn_lds + 57344;
  const f32x4 z4 = {0, 0, 0, 0};
  const unsigned OOB = 0x7FFFFF00u;
  const unsigned lo_off = ml < 8 ? (unsigned)(g * 8 + ml) * 16u : OOB;
  unsigned spin_cap = SPIN_CAP;
  typedef short s16x8 __attribute__((ext_vector_type(8)));
  char* my = WL + (wave < 4 ? wave * 15360 : 61440 + (wave - 4) * 8192);
  const unsigned trb = (unsigned)(size_t)my + (8 * g + (ml >> 2)) * 32 + (lane & 3) * 8;
  auto trfrag = [&](int i) {
    s16x4 lo = __builtin_amdgcn_ds_read_tr16_b64_v4i16((LAS s16x4*)(trb + i * 1024));
    s16x4 hi = __builtin_amdgcn_ds_read_tr16_b64_v4i16((LAS s16x4*)(trb + i * 1024 + 128));
    s16x8 tt = {lo[0], lo[1], lo[2], lo[3], hi[0], hi[1], hi[2], hi[3]};
    return __builtin_bit_cast(b16x8, tt);
  };

  if (wave < 4) {
    b16x8 w1[3][5];
    {
      u32x4 t1[15];
#pragma unroll
      for (int t = 0; t < 3; ++t) {
        const u16* W = t == 0 ? Wq : (t == 1 ? Wk : Wv);
#pragma unroll
        for (int i = 0; i < 5; ++i) t1[t * 5 + i] = *(const u32x4*)(W + (size_t)(160 * wave + 32 * i + (lane >> 1)) * HE + h * E + 16 * j + (lane & 1) * 8);
      }
#pragma unroll
      for (int i = 0; i < 15; ++i) *(u32x4*)(my + i * 1024 + lane * 16) = t1[i];
      asm volatile("s_waitcnt lgkmcnt(0)" ::: "memory");
#pragma unroll
      for (int i = 0; i < 15; ++i) w1[i / 5][i % 5] = trfrag(i);
    }
    const char* kvsrc = ws + (wave == 2 ? OFF_KX : OFF_VX) + (size_t)h * 8192 + lane * 16;
    if (wave >= 2) {
#pragma unroll
      for (int sn = 0; sn < 2; ++sn) {
        u32x4 t[8];
#pragma unroll
        for (int i = 0; i < 8; ++i) t[i] = *(const u32x4*)(kvsrc + (size_t)sn * H * 8192 + i * 1024);
#pragma unroll
        for (int i = 0; i < 8; ++i) *(u32x4*)(KV + sn * 16384 + (wave - 2) * 8192 + i * 1024 + lane * 16) = t[i];
      }
    }
    LBAR();
    const int cc = lane & 7, hg = lane >> 3;
    int r3 = 0;
    for (int s = 0; s < NSEG; ++s, r3 = r3 == 2 ? 0 : r3 + 1) {
      TS(0);
      const int r3n = r3 == 2 ? 0 : r3 + 1, r3p = r3 == 0 ? 2 : r3 - 1;
      f32x4 ac[3];
      if (s == 0) {
#pragma unroll
        for (int u = 0; u < 3; ++u) ac[u] = z4;
#pragma unroll
        for (int kk = 0; kk < 5; ++kk) {
          u32x4 mf = *(const u32x4*)(mem0 + (ml & 7) * D + 32 * (5 * wave + kk) + 8 * g);
#pragma unroll
          for (int u = 0; u < 3; ++u) ac[u] = mfma16(w1[u][kk], asb(mf), ac[u]);
        }
      } else {
        const unsigned base = C_MEM + (unsigned)r3 * 32768u + (unsigned)(wave >> 1) * 16384u + (unsigned)(wave & 1) * 2560u + selA + lo_off;
        slp<SLP_A>();
        u32x4 mf[5];
        for (unsigned spin = 0;; ++spin) {
          asm volatile("" ::: "memory");
#pragma unroll
          for (int kk = 0; kk < 5; ++kk) mf[kk] = ld128(rs, base + kk * 512u);
          unsigned mx = 0;
#pragma unroll
          for (int kk = 0; kk < 5; ++kk) mx = umax3(mx, mf[kk][0], mf[kk][2]);
          if (__all(mx != 0xFFFFFFFFu) || spin >= spin_cap) { SPN(0); if (spin >= spin_cap) spin_cap = 0; break; }
        }
#pragma unroll
        for (int u = 0; u < 3; ++u) ac[u] = z4;
#pragma unroll
        for (int kk = 0; kk < 5; ++kk) {
#pragma unroll
          for (int u = 0; u < 3; ++u) ac[u] = mfma16(w1[u][kk], asb(mf[kk]), ac[u]);
        }
      }
      TS(1);
#pragma unroll
      for (int u = 0; u < 3; ++u) X1[u * 256 + tid] = ac[u];
      LBAR();
      if (wave < 2) {
        const int tl = 2 * wave + (ml >> 3);
        const f32x4* xp = X1 + (tl < 3 ? tl : 2) * 256 + g * 16 + (ml & 7);
        f32x4 a = (xp[0] + xp[64]) + (xp[128] + xp[192]);
        u32x2 v = {nosent(pack2(a[0], a[1])), nosent(pack2(a[2], a[3]))};
        const unsigned in = (unsigned)tl * 2048u + (unsigned)(((j >> 1) * 4 + 2 * (j & 1) + (g >> 1)) * 8 + (ml & 7)) * 16u + (g & 1) * 8u;
        if (tl < 3) {
          const unsigned off = C_QKV + (unsigned)(r3 * H + h) * 16384u + in;
          st64(rs, off + selA, v); st64(rs, off + (8192u - selA), v);
          if (tl > 0) *(u32x2*)(ws + OFF_QM + (size_t)(s * H + h) * 6144 + in) = v;
          const unsigned offp = C_QKV + (unsigned)(r3p * H + h) * 16384u + in;
          st64(rs, offp, ones2); st64(rs, offp + 8192u, ones2);
        }
      }
      TS(2);
      if (s == NSEG - 1) break;
      if (wave != 0) { LBAR(); LBAR(); } else {
        LBAR();
        const unsigned offw = (unsigned)(1 + (s & 1)) * PAGE + C_P + (unsigned)(cb / 3) * 16384u + (unsigned)(cb % 3) * 2560u + (unsigned)hg * 320u + (unsigned)cc * 16u;
        const unsigned off = cc < 5 ? offw + ((s & 1) ? selP1 : selP0) : OOB;
        slp<SLP_C>();
        u32x4 v[4];
        for (unsigned spin = 0;; ++spin) {
          asm volatile("" ::: "memory");
#pragma unroll
          for (int i = 0; i < 4; ++i) v[i] = ld128(rs, off + i * 80u);
          unsigned mx = 0;
#pragma unroll
          for (int i = 0; i < 4; ++i) { mx = umax3(mx, v[i][0], v[i][1]); mx = umax3(mx, v[i][2], v[i][3]); }
          if (__all(mx != 0xFFFFFFFFu) || spin >= spin_cap) { SPN(2); if (spin >= spin_cap) spin_cap = 0; break; }
        }
        TS(5);
        float f[4];
#pragma unroll
        for (int r = 0; r < 4; ++r) {
          float x = (__uint_as_float(v[0][r]) + __uint_as_float(v[1][r])) + (__uint_as_float(v[2][r]) + __uint_as_float(v[3][r]));
          x += ror8(x); x = xsum32(xsum16(x)); f[r] = x;
        }
        u32x2 w = {nosent(pack2(f[0], f[1])), nosent(pack2(f[2], f[3]))};
        const unsigned mq = (unsigned)cb >> 6, min_ = (mq >> 1) * 16384u + (mq & 1) * 2560u + (unsigned)(5 * (cb & 63) + lane) * 8u;
        if (lane < 5) { st64(rs, C_MEM + (unsigned)r3n * 32768u + min_, w); st64(rs, C_MEM + (unsigned)r3n * 32768u + 8192u + min_, w); }
        TS(6);
        LBAR();
        if (cc < 5) {
#pragma unroll
          for (int i = 0; i < 4; ++i) { st128(rs, offw + i * 80u, ones4); st128(rs, offw + 8192u + i * 80u, ones4); }
        }
        if (lane < 5) { st64(rs, C_MEM + (unsigned)r3 * 32768u + min_, ones2); st64(rs, C_MEM + (unsigned)r3 * 32768u + 8192u + min_, ones2); }
      }
    }
    return;
  }
  const int w4 = wave - 4;
  const bool three = w4 < 1;
  b16x8 w2[2][4];
  {
    u32x4 t2[8];
#pragma unroll
    for (int t = 0; t < 2; ++t) {
      const int ct = (t < 1 || three) ? w4 + 4 * t : 0;
#pragma unroll
      for (int i = 0; i < 4; ++i) t2[t * 4 + i] = *(const u32x4*)(Wo + (size_t)(h * E + 32 * i + (lane >> 1)) * D + 80 * j + 16 * ct + (lane & 1) * 8);
    }
#pragma unroll
    for (int i = 0; i < 8; ++i) *(u32x4*)(my + i * 1024 + lane * 16) = t2[i];
    asm volatile("s_waitcnt lgkmcnt(0)" ::: "memory");
#pragma unroll
    for (int i = 0; i < 8; ++i) w2[i / 4][i % 4] = trfrag(i);
  }
  b16x8 kx[2][4]; u32x4 vwx[2];
  const b16x8 ones8 = asb((u32x4){0x3F803F80u, 0x3F803F80u, 0x3F803F80u, 0x3F803F80u});
  auto prep = [&](int sn) {
    const char* kb = KV + (sn & 1) * 16384 + lane * 16;
    b16x8 vf[2][4];
#pragma unroll
    for (int kt = 0; kt < 2; ++kt)
#pragma unroll
      for (int ks = 0; ks < 4; ++ks) { kx[kt][ks] = *(const b16x8*)(kb + (kt * 4 + ks) * 1024); vf[kt][ks] = *(const b16x8*)(kb + 8192 + (kt * 4 + ks) * 1024); }
#pragma unroll
    for (int t = 0; t < 2; ++t) {
      f32x4 d0 = z4, d1 = z4;
#pragma unroll
      for (int ks = 0; ks < 4; ++ks) { d0 = mfma16(vf[0][ks], w2[t][ks], d0); d1 = mfma16(vf[1][ks], w2[t][ks], d1); }
      vwx[t] = (u32x4){pack2(d0[0], d0[1]), pack2(d0[2], d0[3]), pack2(d1[0], d1[1]), pack2(d1[2], d1[3])};
    }
  };
  const char* kvs = ws + (w4 < 2 ? OFF_KX : OFF_VX) + (size_t)h * 8192 + (w4 & 1) * 4096 + lane * 16;
  u32x4 stg[4];
#pragma unroll
  for (int i = 0; i < 4; ++i) stg[i] = *(const u32x4*)(kvs + (size_t)2 * H * 8192 + i * 1024);
  LBAR();
  prep(0);
  unsigned poff[2];
#pragma unroll
  for (int t = 0; t < 2; ++t) {
    const int T = 5 * j + w4 + 4 * t;
    const unsigned c16 = (unsigned)(((T >> 1) * 4 + 2 * (T & 1) + (g >> 1)) * 8 + (ml & 7)) * 2u + (unsigned)(g & 1), rb = c16 / 5u;
    poff[t] = C_P + (rb / 3u) * 16384u + (rb % 3u) * 2560u + (unsigned)h * 80u + (c16 % 5u) * 16u;
  }
  int r3 = 0;
  for (int s = 0; s < NSEG - 1; ++s, r3 = r3 == 2 ? 0 : r3 + 1) {
    const unsigned hb = C_QKV + (unsigned)(r3 * H + h) * 16384u + selA + lo_off;
    LBAR();
    slp<SLP_B>();
    f32x4 a[2];
    u32x4 qf[4], kf[4], vf[4];
    for (unsigned spin = 0;; ++spin) {
      asm volatile("" ::: "memory");
#pragma unroll
      for (int ks = 0; ks < 4; ++ks) qf[ks] = ld128(rs, hb + ks * 512u);
#pragma unroll
      for (int ks = 0; ks < 4; ++ks) kf[ks] = ld128(rs, hb + 2048u + ks * 512u);
#pragma unroll
      for (int ks = 0; ks < 4; ++ks) vf[ks] = ld128(rs, hb + 4096u + ks * 512u);
      unsigned mx = 0;
#pragma unroll
      for (int ks = 0; ks < 4; ++ks) { mx = umax3(mx, qf[ks][0], qf[ks][2]); mx = umax3(mx, kf[ks][0], kf[ks][2]); mx = umax3(mx, vf[ks][0], vf[ks][2]); }
      if (__all(mx != 0xFFFFFFFFu) || spin >= spin_cap) { SPN(1); if (spin >= spin_cap) spin_cap = 0; break; }
    }
    {
#define SB() __builtin_amdgcn_sched_barrier(0)
      f32x4 s0 = z4, s1 = z4, sm = z4, dm[2] = {z4, z4};
#pragma unroll
      for (int ks = 0; ks < 4; ++ks) { s0 = mfma16(kx[0][ks], asb(qf[ks]), s0); s1 = mfma16(kx[1][ks], asb(qf[ks]), s1); SB(); }
#pragma unroll
      for (int ks = 0; ks < 4; ++ks) { sm = mfma16(asb(kf[ks]), asb(qf[ks]), sm); SB(); }
      TS(3);
      const float NEG = -INFINITY;
      const float c2 = SCALE * 1.4426950408889634f;
      float tv[12];
      dm[0] = mfma16(asb(vf[0]), w2[0][0], dm[0]);
#pragma unroll
      for (int r = 0; r < 4; ++r) tv[r] = s0[r] * c2;
      SB();
      dm[1] = mfma16(asb(vf[0]), w2[1][0], dm[1]);
#pragma unroll
      for (int r = 0; r < 4; ++r) tv[4 + r] = s1[r] * c2;
      SB();
      float m1 = fmaxf(fmaxf(tv[0], tv[1]), tv[2]), m2 = fmaxf(fmaxf(tv[3], tv[4]), tv[5]);
      m1 = fmaxf(fmaxf(m1, tv[6]), tv[7]);
      SB();
      dm[0] = mfma16(asb(vf[1]), w2[0][1], dm[0]);
#pragma unroll
      for (int r = 0; r < 4; ++r) tv[8 + r] = (g < 2) ? sm[r] * c2 : NEG;
      SB();
      dm[1] = mfma16(asb(vf[1]), w2[1][1], dm[1]);
      m2 = fmaxf(fmaxf(m2, tv[8]), tv[9]); m1 = fmaxf(fmaxf(m1, tv[10]), tv[11]);
      float mxv = fmaxf(m1, m2);
      { unsigned u = __float_as_uint(mxv); auto r = __builtin_amdgcn_permlane16_swap(u, u, false, false); mxv = fmaxf(__uint_as_float(r[0]) + 0.f, __uint_as_float(r[1]) + 0.f); }
      SB();
      { unsigned u = __float_as_uint(mxv); auto r = __builtin_amdgcn_permlane32_swap(u, u, false, false); mxv = fmaxf(__uint_as_float(r[0]) + 0.f, __uint_as_float(r[1]) + 0.f); }
      SB();
      dm[0] = mfma16(asb(vf[2]), w2[0][2], dm[0]);
#pragma unroll
      for (int r = 0; r < 2; ++r) tv[r] = __builtin_amdgcn_exp2f(tv[r] - mxv);
      SB();
      dm[1] = mfma16(asb(vf[2]), w2[1][2], dm[1]);
#pragma unroll
      for (int r = 2; r < 4; ++r) tv[r] = __builtin_amdgcn_exp2f(tv[r] - mxv);
      SB();
#pragma unroll
      for (int r = 4; r < 6; ++r) tv[r] = __builtin_amdgcn_exp2f(tv[r] - mxv);
      SB();
      dm[0] = mfma16(asb(vf[3]), w2[0][3], dm[0]);
#pragma unroll
      for (int r = 6; r < 8; ++r) tv[r] = __builtin_amdgcn_exp2f(tv[r] - mxv);
      SB();
      dm[1] = mfma16(asb(vf[3]), w2[1][3], dm[1]);
#pragma unroll
      for (int r = 8; r < 10; ++r) tv[r] = __builtin_amdgcn_exp2f(tv[r] - mxv);
      SB();
#pragma unroll
      for (int r = 10; r < 12; ++r) tv[r] = __builtin_amdgcn_exp2f(tv[r] - mxv);
      u32x4 px = {pack2(tv[0], tv[1]), pack2(tv[2], tv[3]), pack2(tv[4], tv[5]), pack2(tv[6], tv[7])};
      SB();
      f32x4 sx = mfma16(ones8, asb(px), z4);
      a[0] = mfma16(asb(vwx[0]), asb(px), z4);
      a[1] = mfma16(asb(vwx[1]), asb(px), z4);
      u32x4 pm = {pack2(tv[8], tv[9]), pack2(tv[10], tv[11]), 0u, 0u};
      SB();
      sx = mfma16(ones8, asb(pm), sx);
#pragma unroll
      for (int t = 0; t < 2; ++t) {
        u32x4 vwm = {pack2(dm[t][0], dm[t][1]), pack2(dm[t][2], dm[t][3]), 0u, 0u};
        a[t] = mfma16(asb(vwm), asb(pm), a[t]);
      }
      SB();
      const float inv = __builtin_amdgcn_rcpf(sx[0]);
      a[0] *= inv; a[1] *= inv;
    }
#pragma unroll
    for (int t = 0; t < 2; ++t) {
      if (t == 1 && !three) break;
      u32x4 v;
#pragma unroll
      for (int r = 0; r < 4; ++r) v[r] = min(__float_as_uint(a[t][r]), 0xFFFFFFFEu);
      const unsigned off = (unsigned)(1 + (s & 1)) * PAGE + poff[t];
      if (ml < 8) { st128(rs, off, v); st128(rs, off + 8192u, v); }
    }
    TS(4);
    LBAR();
    if (s + 1 < NSEG - 1) prep(s + 1);
    if (s + 2 < NSEG - 1) {
#pragma unroll
      for (int i = 0; i < 4; ++i) *(u32x4*)(KV + (s & 1) * 16384 + w4 * 4096 + i * 1024 + lane * 16) = stg[i];
    }
    LBAR();
    if (s + 3 < NSEG - 1) {
#pragma unroll
      for (int i = 0; i < 4; ++i) stg[i] = *(const u32x4*)(kvs + (size_t)(s + 3) * H * 8192 + i * 1024);
    }
  }
  LBAR();
}

__global__ __launch_bounds__(512, 1) void k_chain(const u16* __restrict__ mem0, const u16* __restrict__ Wq, const u16* __restrict__ Wk,
                                                   const u16* __restrict__ Wv, const u16* __restrict__ Wo, char* __restrict__ ws) {
  extern __shared__ __attribute__((aligned(16))) char dyn_lds[];
  chain_role(blockIdx.x, mem0, Wq, Wk, Wv, Wo, ws, dyn_lds);
}

DI void attn_unit(const int s, const int h, const b16x8 (&qf)[2][4], char* __restrict__ ws, char* V) {
  const int lane = threadIdx.x & 63, ml = lane & 15, g = lane >> 4;
  const char* KX = ws + OFF_KX + (size_t)(s * H + h) * 8192 + lane * 16;
  const char* VX = ws + OFF_VX + (size_t)(s * H + h) * 8192 + lane * 16;
  const char* KM = ws + OFF_QM + (size_t)(s * H + h) * 6144 + 2048 + (g * 8 + (ml & 7)) * 16;
  b16x8 kx[2][4], km[4];
#pragma unroll
  for (int ks = 0; ks < 4; ++ks) {
#pragma unroll
    for (int t = 0; t < 2; ++t) {
      kx[t][ks] = *(const b16x8*)(KX + (t * 4 + ks) * 1024);
      *(u32x4*)(V + (16 * t + ml) * 272 + (32 * ks + 8 * g) * 2) = *(const u32x4*)(VX + (t * 4 + ks) * 1024);
    }
    km[ks] = *(const b16x8*)(KM + ks * 512);
    u32x4 vmr = *(const u32x4*)(KM + 2048 + ks * 512);
    if (ml >= 8) vmr = (u32x4){0, 0, 0, 0};
    *(u32x4*)(V + (32 + ml) * 272 + (32 * ks + 8 * g) * 2) = vmr;
  }
  u32x4 px[2], pm[2];
#pragma unroll
  for (int qt = 0; qt < 2; ++qt) {
    f32x4 s0 = {0, 0, 0, 0}, s1 = {0, 0, 0, 0}, sm = {0, 0, 0, 0};
#pragma unroll
    for (int ks = 0; ks < 4; ++ks) {
      s0 = mfma16(kx[0][ks], qf[qt][ks], s0);
      s1 = mfma16(kx[1][ks], qf[qt][ks], s1);
      sm = mfma16(km[ks], qf[qt][ks], sm);
    }
    const int qi = 16 * qt + ml;
    float sv[12];
#pragma unroll
    for (int r = 0; r < 4; ++r) {
      sv[r] = (4 * g + r <= qi) ? rbf(rbf(s0[r]) * SCALE) : -INFINITY;
      sv[4 + r] = (16 + 4 * g + r <= qi) ? rbf(rbf(s1[r]) * SCALE) : -INFINITY;
      sv[8 + r] = (g < 2) ? rbf(rbf(sm[r]) * SCALE) : -INFINITY;
    }
    float mxv = sv[0];
#pragma unroll
    for (int r = 1; r < 12; ++r) mxv = fmaxf(mxv, sv[r]);
    mxv = xmax32(xmax16(mxv));
    float sum = 0.f;
#pragma unroll
    for (int r = 0; r < 12; ++r) { sv[r] = __expf(sv[r] - mxv); sum += sv[r]; }
    sum = xsum32(xsum16(sum));
    const float inv = 1.f / sum;
    px[qt] = (u32x4){pack2(sv[0] * inv, sv[1] * inv), pack2(sv[2] * inv, sv[3] * inv), pack2(sv[4] * inv, sv[5] * inv), pack2(sv[6] * inv, sv[7] * inv)};
    pm[qt] = (u32x4){pack2(sv[8] * inv, sv[9] * inv), pack2(sv[10] * inv, sv[11] * inv), 0u, 0u};
  }
  asm volatile("s_waitcnt lgkmcnt(0)" ::: "memory");
  const unsigned vb = (unsigned)(size_t)V + (4 * g + (ml >> 2)) * 272 + (lane & 3) * 8;
#pragma unroll
  for (int kk = 0; kk < 4; ++kk) {
    u32x4 of[2];
#pragma unroll
    for (int hf = 0; hf < 2; ++hf) {
      const int et = 2 * kk + hf;
      s16x4 v0 = __builtin_amdgcn_ds_read_tr16_b64_v4i16((LAS s16x4*)(vb + et * 32));
      s16x4 v1 = __builtin_amdgcn_ds_read_tr16_b64_v4i16((LAS s16x4*)(vb + 16 * 272 + et * 32));
      s16x4 v2 = __builtin_amdgcn_ds_read_tr16_b64_v4i16((LAS s16x4*)(vb + 32 * 272 + et * 32));
      typedef short s16x8 __attribute__((ext_vector_type(8)));
      s16x8 va = {v0[0], v0[1], v0[2], v0[3], v1[0], v1[1], v1[2], v1[3]};
      s16x8 vbm = {v2[0], v2[1], v2[2], v2[3], 0, 0, 0, 0};
#pragma unroll
      for (int qt = 0; qt < 2; ++qt) {
        f32x4 o = {0, 0, 0, 0};
        o = mfma16(__builtin_bit_cast(b16x8, va), asb(px[qt]), o);
        o = mfma16(__builtin_bit_cast(b16x8, vbm), asb(pm[qt]), o);
        of[qt][2 * hf] = pack2(o[0], o[1]); of[qt][2 * hf + 1] = pack2(o[2], o[3]);
      }
    }
#pragma unroll
    for (int qt = 0; qt < 2; ++qt) *(u32x4*)(ws + OFF_O + (size_t)((s * 2 + qt) * 128 + h * 4 + kk) * 1024 + lane * 16) = of[qt];
  }
  asm volatile("s_waitcnt lgkmcnt(0)" ::: "memory");
}

__global__ __launch_bounds__(512, 1) void k_post1(const u16* __restrict__ x, const u16* __restrict__ Wq, const u16* __restrict__ wg,
                                                  const u16* __restrict__ wu, const u16* __restrict__ Wo, const u16* __restrict__ wd,
                                                  const u16* __restrict__ gate_w, char* __restrict__ ws) {
  extern __shared__ __attribute__((aligned(16))) char lds[];
  const int b = blockIdx.x, xc = b & 7, idx = b >> 3;
  if (idx >= 30) {
    const int lane = threadIdx.x & 63, wave = threadIdx.x >> 6, ml = lane & 15, g = lane >> 4;
    const int gw = (b - 240) * 8 + wave;
    for (int t = gw; t < S; t += 128) {
      float s = 0.f;
#pragma unroll
      for (int i = 0; i < 5; ++i) {
        const unsigned xv = *(const unsigned*)(x + (size_t)t * D + i * 128 + lane * 2), gv = *(const unsigned*)(gate_w + i * 128 + lane * 2);
        s += rbf(__uint_as_float(xv << 16) * __uint_as_float(gv << 16)) + rbf(__uint_as_float(xv & 0xFFFF0000u) * __uint_as_float(gv & 0xFFFF0000u));
      }
#pragma unroll
      for (int o = 32; o >= 1; o >>= 1) s += __shfl_xor(s, o);
      s = rbf(s);
      float gg = rbf(1.f / (1.f + __expf(-s)));
      if (lane == 0) ((float*)(ws + OFF_G))[t] = gg;
    }
    char* my = lds + wave * 8192;
    const unsigned trb = (unsigned)(size_t)my + (4 * g + (ml >> 2)) * 32 + (lane & 3) * 8;
    for (int u = gw; u < 182 * 5; u += 128) {
      const int kk = u / 5, c0 = (u % 5) * 8;
      const u16* W = kk < 128 ? Wo + (size_t)32 * kk * D : wd + (size_t)32 * (kk - 128) * D;
      u32x4 t8[8];
#pragma unroll
      for (int i = 0; i < 8; ++i) t8[i] = *(const u32x4*)(W + (size_t)(4 * i + (lane >> 4)) * D + 16 * c0 + (lane & 15) * 8);
#pragma unroll
      for (int i = 0; i < 8; ++i) *(u32x4*)(my + ((lane & 15) >> 1) * 1024 + (4 * i + (lane >> 4)) * 32 + (lane & 1) * 16) = t8[i];
      asm volatile("s_waitcnt lgkmcnt(0)" ::: "memory");
#pragma unroll
      for (int i = 0; i < 8; ++i) {
        s16x4 lo = __builtin_amdgcn_ds_read_tr16_b64_v4i16((LAS s16x4*)(trb + i * 1024));
        s16x4 hi = __builtin_amdgcn_ds_read_tr16_b64_v4i16((LAS s16x4*)(trb + i * 1024 + 512));
        typedef short s16x8 __attribute__((ext_vector_type(8)));
        s16x8 tt = {lo[0], lo[1], lo[2], lo[3], hi[0], hi[1], hi[2], hi[3]};
        *(s16x8*)(ws + OFF_WF + ((size_t)(c0 + i) * 182 + kk) * 1024 + lane * 16) = tt;
      }
      asm volatile("s_waitcnt lgkmcnt(0)" ::: "memory");
    }
    return;
  }
  f32x4 acc[8][4];
#pragma unroll
  for (int i = 0; i < 8; ++i)
#pragma unroll
    for (int j = 0; j < 4; ++j) acc[i][j] = (f32x4){0, 0, 0, 0};
  if (idx < 16) {
    const int nt = xc * 2 + (idx >> 3), mt = idx & 7, n0 = nt * 256, m0 = mt * 256;
    gemm256<0>(x, D, Wq, nullptr, HE, HE, D, m0, n0, lds, acc);
    int tz = threadIdx.x; asm volatile("" : "+v"(tz));
    const int wr = tz >> 8, wc = (tz >> 6) & 3, fr = tz & 15, g = (tz >> 4) & 3, lane = tz & 63, wave = __builtin_amdgcn_readfirstlane(tz >> 6);
#pragma unroll
    for (int i = 0; i < 8; ++i)
#pragma unroll
      for (int j = 0; j < 4; ++j) {
        u32x2 v = {pack2(acc[i][j][0], acc[i][j][1]), pack2(acc[i][j][2], acc[i][j][3])};
        const int tok = wr * 128 + i * 16 + fr, n = wc * 64 + j * 16 + 4 * g;
        const int seg = tok >> 5, kt = (tok >> 4) & 1, hh = n >> 7, e = n & 127;
        *(u32x2*)(lds + (seg * 2 + hh) * 8192 + (((kt * 4 + (e >> 5)) * 64 + (tok & 15) + 16 * ((e & 31) >> 3)) * 16 + (e & 4) * 2)) = v;
      }
    RBAR();
    b16x8 q0[2][4], q1[2][4];
#pragma unroll
    for (int t = 0; t < 2; ++t)
#pragma unroll
      for (int ks = 0; ks < 4; ++ks) {
        q0[t][ks] = *(const b16x8*)(lds + (wave * 2) * 8192 + (t * 4 + ks) * 1024 + lane * 16);
        q1[t][ks] = *(const b16x8*)(lds + (wave * 2 + 1) * 8192 + (t * 4 + ks) * 1024 + lane * 16);
      }
    RBAR();
    char* V = lds + wave * (48 * 272);
    attn_unit(mt * 8 + wave, nt * 2, q0, ws, V);
    attn_unit(mt * 8 + wave, nt * 2 + 1, q1, ws, V);
  } else {
    const int tile = xc * 14 + (idx - 16), ft = tile >> 3, mt = tile & 7, f0 = ft * 128, m0 = mt * 256;
    gemm256<1>(x, D, wg, wu, F, F, D, m0, f0, lds, acc);
    int tz = threadIdx.x; asm volatile("" : "+v"(tz));
    const int wr = tz >> 8, wc = (tz >> 6) & 3, ln = tz & 63;
    if (f0 + wc * 32 < F) {
      const int kk = (f0 + wc * 32) >> 5;
#pragma unroll
      for (int i = 0; i < 8; ++i) {
        u32x4 v;
#pragma unroll
        for (int j = 0; j < 2; ++j) {
          float hv[4];
#pragma unroll
          for (int r = 0; r < 4; ++r) {
            float gg = rbf(acc[i][j][r]), uu = rbf(acc[i][j + 2][r]);
            hv[r] = rbf(gelu_tanh(gg)) * uu;
          }
          v[2 * j] = pack2(hv[0], hv[1]); v[2 * j + 1] = pack2(hv[2], hv[3]);
        }
        const int t16 = (m0 + wr * 128 + i * 16) >> 4;
        *(u32x4*)(ws + OFF_HH + (size_t)(t16 * 54 + kk) * 1024 + ln * 16) = v;
      }
    }
  }
}

__global__ __launch_bounds__(512, 1) void k_post2(const char* __restrict__ ws, u16* __restrict__ out) {
  extern __shared__ __attribute__((aligned(16))) char lds[];
  const int tid = threadIdx.x, lane = tid & 63, wave = __builtin_amdgcn_readfirstlane(tid >> 6), ml = lane & 15, g = lane >> 4;
  const int b = blockIdx.x, xc = b & 7, idx = b >> 3, tt = xc * 4 + (idx >> 3), cgp = idx & 7;
  f32x4* X = (f32x4*)lds;
  f32x4 asum[2][2];
#pragma unroll
  for (int ph = 0; ph < 2; ++ph) {
    const int kt = ph == 0 ? 128 : 54, nk = ph == 0 ? 16 : (wave < 6 ? 7 : 6);
    const char* tb = ws + (ph ? OFF_HH : OFF_O) + (size_t)(tt * 4) * kt * 1024 + (size_t)wave * 1024 + lane * 16;
    const size_t tstr = (size_t)kt * 1024;
    const char* wb = ws + OFF_WF + ((size_t)(cgp * 5) * 182 + (ph ? 128 : 0) + wave) * 1024 + lane * 16;
    f32x4 acc[4][5];
#pragma unroll
    for (int i = 0; i < 4; ++i)
#pragma unroll
      for (int c = 0; c < 5; ++c) acc[i][c] = (f32x4){0, 0, 0, 0};
    b16x8 tf[4][4], wf[4][5];
    auto ldk = [&](int slot, int k) {
      const int kn = k < nk ? k : nk - 1;
#pragma unroll
      for (int i = 0; i < 4; ++i) tf[slot][i] = *(const b16x8*)(tb + i * tstr + (size_t)kn * 8192);
#pragma unroll
      for (int c = 0; c < 5; ++c) wf[slot][c] = *(const b16x8*)(wb + (size_t)c * 182 * 1024 + (size_t)kn * 8192);
    };
    ldk(0, 0); ldk(1, 1); ldk(2, 2);
    for (int k = 0; k < nk; k += 4) {
#pragma unroll
      for (int u = 0; u < 4; ++u) {
        ldk((u + 3) & 3, k + u + 3);
        if (k + u < nk) {
#pragma unroll
          for (int i = 0; i < 4; ++i)
#pragma unroll
            for (int c = 0; c < 5; ++c) acc[i][c] = mfma16(wf[u][c], tf[u][i], acc[i][c]);
        }
      }
    }
#pragma unroll
    for (int rd = 0; rd < 2; ++rd) {
#pragma unroll
      for (int t = 0; t < 10; ++t) { const int tl = rd * 10 + t; X[(wave * 10 + t) * 64 + lane] = acc[tl / 5][tl % 5]; }
      RBAR();
#pragma unroll
      for (int u = 0; u < 2; ++u) {
        const int t = wave + 8 * u;
        if (t < 10) {
          const f32x4* xp = X + t * 64 + lane;
          const f32x4 v = ((xp[0] + xp[640]) + (xp[1280] + xp[1920])) + ((xp[2560] + xp[3200]) + (xp[3840] + xp[4480]));
          if (ph == 0) asum[rd][u] = v;
          else {
            const int tl = rd * 10 + t, i = tl / 5, c = tl % 5;
            const int tok = tt * 64 + 16 * i + ml;
            const float gg = ((const float*)(ws + OFF_G))[tok], og = rbf(1.f - gg);
            float r[4];
#pragma unroll
            for (int q = 0; q < 4; ++q) { float ta = rbf(gg * rbf(asum[rd][u][q])), tb2 = rbf(og * rbf(v[q])); asm volatile("" : "+v"(ta), "+v"(tb2)); r[q] = ta + tb2; }
            *(u32x2*)(out + (size_t)tok * D + 80 * cgp + 16 * c + 4 * g) = (u32x2){pack2(r[0], r[1]), pack2(r[2], r[3])};
          }
        }
      }
      RBAR();
    }
  }
}

extern "C" void kernel_launch(void* const* d_in, const int* in_sizes, int n_in, void* d_out, int out_size, void* d_ws, size_t ws_size,
                              hipStream_t stream) {
  const u16* x = (const u16*)d_in[0];
  const u16* mem0 = (const u16*)d_in[1];
  const u16* Wq = (const u16*)d_in[2];
  const u16* Wk = (const u16*)d_in[3];
  const u16* Wv = (const u16*)d_in[4];
  const u16* Wo = (const u16*)d_in[5];
  const u16* wg = (const u16*)d_in[8];
  const u16* wu = (const u16*)d_in[9];
  const u16* wd = (const u16*)d_in[10];
  const u16* gw = (const u16*)d_in[11];
  char* ws = (char*)d_ws;
  hipFuncSetAttribute((const void*)k_pre, hipFuncAttributeMaxDynamicSharedMemorySize, 128 * 1024);
  k_pre<<<256, 512, 128 * 1024, stream>>>(x, Wk, Wv, Wq, Wo, ws);
  hipFuncSetAttribute((const void*)k_chain, hipFuncAttributeMaxDynamicSharedMemorySize, 156 * 1024);
  k_chain<<<256, 512, 156 * 1024, stream>>>(mem0, Wq, Wk, Wv, Wo, ws);
  hipFuncSetAttribute((const void*)k_post1, hipFuncAttributeMaxDynamicSharedMemorySize, 128 * 1024);
  k_post1<<<256, 512, 128 * 1024, stream>>>(x, Wq, wg, wu, Wo, wd, gw, ws);
  hipFuncSetAttribute((const void*)k_post2, hipFuncAttributeMaxDynamicSharedMemorySize, 80 * 1024);
  k_post2<<<256, 512, 80 * 1024, stream>>>(ws, (u16*)d_out);
}
```

```cpp
#include <hip/hip_runtime.h>
#include <hip/hip_bf16.h>
#include <stdint.h>
#include <stdio.h>

typedef unsigned short u16;
typedef __bf16 b16x8 __attribute__((ext_vector_type(8)));
typedef short s16x4 __attribute__((ext_vector_type(4)));
typedef float f32x4 __attribute__((ext_vector_type(4)));
typedef unsigned u32x4 __attribute__((ext_vector_type(4)));
typedef unsigned u32x2 __attribute__((ext_vector_type(2)));
#define LAS __attribute__((address_space(3)))
#define DI __device__ __forceinline__

constexpr int S = 2048, D = 640, H = 32, E = 128, HE = 4096, MM = 8, SEG = 32, NSEG = 64, F = 1728;
constexpr float SCALE = 0.08838834764831845f;

constexpr size_t OFF_KX = 0;
constexpr size_t OFF_VX = OFF_KX + (size_t)S * HE * 2;
constexpr size_t OFF_QX = OFF_VX + (size_t)S * HE * 2;
constexpr size_t OFF_O  = OFF_QX + (size_t)S * HE * 2;
constexpr size_t OFF_HH = OFF_O + (size_t)S * HE * 2;
constexpr size_t OFF_WF = OFF_HH + (size_t)S * F * 2;
constexpr size_t OFF_XF = OFF_WF + (size_t)40 * 182 * 1024;
constexpr size_t OFF_G  = OFF_XF + (size_t)S * D * 2;
constexpr size_t OFF_QM = OFF_G + (size_t)S * 4;
constexpr size_t OFF_COMM = OFF_QM + (size_t)NSEG * H * 6144;
constexpr size_t OFF_END = OFF_COMM + (size_t)8 * 1048576;
constexpr size_t OFF_DBG = OFF_END;
constexpr unsigned PAGE = 2097152u, C_MEM = 65536u, C_TOK = 180224u, C_QKV = 196608u, C_P = 65536u;
__host__ __device__ inline char* comm_base(char* ws) { return (char*)(((size_t)(ws + OFF_COMM) + (PAGE - 1)) & ~(size_t)(PAGE - 1)); }

DI float bf2f(u16 v) { return __builtin_bit_cast(float, (unsigned)v << 16); }
DI u16 f2bf(float f) { return __builtin_bit_cast(u16, (__bf16)f); }
DI float rbf(float f) { return (float)(__bf16)f; }
typedef float f32x2 __attribute__((ext_vector_type(2)));
typedef __bf16 b16x2 __attribute__((ext_vector_type(2)));
DI unsigned pack2(float lo, float hi) { f32x2 v = {lo, hi}; return __builtin_bit_cast(unsigned, __builtin_convertvector(v, b16x2)); }
DI f32x4 mfma16(b16x8 a, b16x8 b, f32x4 c) { return __builtin_amdgcn_mfma_f32_16x16x32_bf16(a, b, c, 0, 0, 0); }
DI b16x8 asb(u32x4 v) { return __builtin_bit_cast(b16x8, v); }

DI float gelu_tanh(float x) {
  float y = 0.7978845608028654f * (x + 0.044715f * x * x * x);
  float t = 1.f - 2.f / (__expf(2.f * y) + 1.f);
  return 0.5f * x * (1.f + t);
}

#define RBAR() asm volatile("s_waitcnt lgkmcnt(0)\n\ts_barrier" ::: "memory")

__global__ __launch_bounds__(256) void k_init(const u16* __restrict__ x, char* __restrict__ ws) {
  const unsigned i = blockIdx.x * 256 + threadIdx.x;
  constexpr unsigned NX = S * D / 8, NF = (2 * PAGE + C_P + 86 * 16384) / 16;
  if (i < NX) {
    const unsigned lane = i & 63, f = i >> 6, kk = f % 20, t16 = f / 20;
    *(u32x4*)(ws + OFF_XF + (size_t)i * 16) = *(const u32x4*)(x + (size_t)(16 * t16 + (lane & 15)) * D + 32 * kk + 8 * (lane >> 4));
  } else if (i - NX < NF) *(u32x4*)(comm_base(ws) + (size_t)(i - NX) * 16) = (u32x4){0xFFFFFFFFu, 0xFFFFFFFFu, 0xFFFFFFFFu, 0xFFFFFFFFu};
}

#define SC1 16
DI u32x4 ld128(__amdgpu_buffer_rsrc_t rs, unsigned off) { return __builtin_amdgcn_raw_buffer_load_b128(rs, off, 0, SC1); }
DI u32x2 ld64(__amdgpu_buffer_rsrc_t rs, unsigned off) { return __builtin_amdgcn_raw_buffer_load_b64(rs, off, 0, SC1); }
DI void st128(__amdgpu_buffer_rsrc_t rs, unsigned off, u32x4 v) { __builtin_amdgcn_raw_buffer_store_b128(v, rs, off, 0, SC1); }
DI void st64(__amdgpu_buffer_rsrc_t rs, unsigned off, u32x2 v) { __builtin_amdgcn_raw_buffer_store_b64(v, rs, off, 0, SC1); }
DI unsigned nosent(unsigned v) { return v == 0xFFFFFFFFu ? 0xFFFEFFFFu : v; }
DI unsigned umax3(unsigned a, unsigned b, unsigned c) { return max(max(a, b), c); }
DI float xsum16(float x) { unsigned u = __float_as_uint(x); auto r = __builtin_amdgcn_permlane16_swap(u, u, false, false); return __uint_as_float(r[0]) + __uint_as_float(r[1]); }
DI float xsum32(float x) { unsigned u = __float_as_uint(x); auto r = __builtin_amdgcn_permlane32_swap(u, u, false, false); return __uint_as_float(r[0]) + __uint_as_float(r[1]); }
DI float xmax16(float x) { unsigned u = __float_as_uint(x); auto r = __builtin_amdgcn_permlane16_swap(u, u, false, false); return fmaxf(__uint_as_float(r[0]), __uint_as_float(r[1])); }
DI float xmax32(float x) { unsigned u = __float_as_uint(x); auto r = __builtin_amdgcn_permlane32_swap(u, u, false, false); return fmaxf(__uint_as_float(r[0]), __uint_as_float(r[1])); }
DI float ror4(float x) { return __uint_as_float(__builtin_amdgcn_update_dpp(0u, __float_as_uint(x), 0x124, 0xf, 0xf, false)); }
DI float ror8(float x) { return __uint_as_float(__builtin_amdgcn_update_dpp(0u, __float_as_uint(x), 0x128, 0xf, 0xf, false)); }
DI u32x4 ror8x4(u32x4 v) { u32x4 r; for (int i = 0; i < 4; ++i) r[i] = __builtin_amdgcn_update_dpp(0u, v[i], 0x128, 0xf, 0xf, false); return r; }
template <int N> struct IC { static constexpr int value = N; };
#define LBAR() asm volatile("s_waitcnt lgkmcnt(0)\n\ts_barrier" ::: "memory")
template <int N> DI void slp() { if constexpr (N > 0) { __builtin_amdgcn_s_sleep(N > 15 ? 15 : N); if constexpr (N > 15) slp<N - 15>(); } }

#define SG(i)
#define TS(i)
#define SPN(k)
constexpr unsigned SPIN_CAP = 1u << 17;
#define SLP_A 15
#define SLP_B 13
#define SLP_B3 4
#define XK1 5
#define XK2 5
#define SLP_X 20
#define SLP_C 10

DI void chain_role(const int cb, const u16* __restrict__ mem0, const u16* __restrict__ Wq, const u16* __restrict__ Wk,
                   const u16* __restrict__ Wv, const u16* __restrict__ Wo, const u16* __restrict__ wg, const u16* __restrict__ wu,
                   char* __restrict__ ws, char* dyn_lds) {
  const int xcd = cb & 7, y = cb >> 3, h = xcd * 4 + (y >> 3), j = y & 7;
  const int tid = threadIdx.x, lane = tid & 63, wave = __builtin_amdgcn_readfirstlane(tid >> 6);
  const int ml = lane & 15, g = lane >> 4;
  const __amdgpu_buffer_rsrc_t rs = __builtin_amdgcn_make_buffer_rsrc(comm_base(ws), 0, (int)(3 * PAGE), 0x00020000);
  unsigned selA, selP0, selP1;
  {
    unsigned* pl = (unsigned*)(dyn_lds + 156 * 1024 - 64);
    if (wave == 0) {
#pragma unroll 1
      for (int p = 0; p < 4; ++p) {
        const unsigned po = (unsigned)(p < 2 ? 0 : p - 1) * PAGE + (unsigned)(cb >> 6) * 16384u + (unsigned)(p == 1) * 8192u + (unsigned)(cb & 63) * 128u;
        asm volatile("s_waitcnt vmcnt(0) lgkmcnt(0)" ::: "memory");
        const unsigned long long t0 = __builtin_amdgcn_s_memtime();
        asm volatile("s_waitcnt lgkmcnt(0)" ::: "memory");
        const unsigned v = __builtin_amdgcn_raw_buffer_load_b32(rs, po, 0, SC1);
        asm volatile("s_waitcnt vmcnt(0)" :: "v"(v) : "memory");
        const unsigned long long t1 = __builtin_amdgcn_s_memtime();
        if (lane == 0) pl[p] = (unsigned)(t1 - t0);
      }
    }
    LBAR();
    selA = __builtin_amdgcn_readfirstlane(pl[1] < pl[0] ? 8192u : 0u);
    selP0 = __builtin_amdgcn_readfirstlane(2 * pl[2] > pl[0] + pl[1] ? 8192u : 0u);
    selP1 = __builtin_amdgcn_readfirstlane(2 * pl[3] > pl[0] + pl[1] ? 8192u : 0u);
  }
  const u32x4 ones4 = {0xFFFFFFFFu, 0xFFFFFFFFu, 0xFFFFFFFFu, 0xFFFFFFFFu};
  const u32x2 ones2 = {0xFFFFFFFFu, 0xFFFFFFFFu};
  f32x4* X1 = (f32x4*)dyn_lds;
  f32x4* X2 = (f32x4*)(dyn_lds + 57344);
  const __amdgpu_buffer_rsrc_t rw = __builtin_amdgcn_make_buffer_rsrc(ws, 0, (int)OFF_O, 0x00020000);
  char* KV = dyn_lds + 24576;
  char* WL = dyn_lds + 57344;
  const f32x4 z4 = {0, 0, 0, 0};
  const unsigned OOB = 0x7FFFFF00u;
  const unsigned lo_off = ml < 8 ? (unsigned)(g * 8 + ml) * 16u : OOB;
  unsigned spin_cap = SPIN_CAP;
  typedef short s16x8 __attribute__((ext_vector_type(8)));
  char* my = WL + (wave < 4 ? wave * 15360 : 61440 + (wave - 4) * 8192);
  const unsigned trb = (unsigned)(size_t)my + (8 * g + (ml >> 2)) * 32 + (lane & 3) * 8;
  auto trfrag = [&](int i) {
    s16x4 lo = __builtin_amdgcn_ds_read_tr16_b64_v4i16((LAS s16x4*)(trb + i * 1024));
    s16x4 hi = __builtin_amdgcn_ds_read_tr16_b64_v4i16((LAS s16x4*)(trb + i * 1024 + 128));
    s16x8 tt = {lo[0], lo[1], lo[2], lo[3], hi[0], hi[1], hi[2], hi[3]};
    return __builtin_bit_cast(b16x8, tt);
  };

  if (wave < 4) {
    b16x8 w1[3][5], wgl[2][5];
    const bool glu = cb < 108;
    u32x4 mf0[5];
    u32x4 xf[2][5];
    auto xloadp = [&](int t, auto k0c, auto k1c) {
      constexpr int k0 = decltype(k0c)::value, k1 = decltype(k1c)::value;
#pragma unroll
      for (int qt = 0; qt < 2; ++qt)
#pragma unroll
        for (int kk = k0; kk < k1; ++kk) xf[qt][kk] = *(const u32x4*)(ws + OFF_XF + (size_t)((2 * t + qt) * 20 + 5 * wave + kk) * 1024 + lane * 16);
    };
    auto xload = [&](int t) { xloadp(t, IC<0>{}, IC<5>{}); };
    auto xmma = [&](int pb) {
#pragma unroll
      for (int u = 0; u < 3; ++u)
#pragma unroll
        for (int qt = 0; qt < 2; ++qt) {
          f32x4 a = z4;
#pragma unroll
          for (int kk = 0; kk < 5; ++kk) a = mfma16(w1[u][kk], asb(xf[qt][kk]), a);
          X2[pb * 2560 + (u * 2 + qt) * 256 + tid] = a;
        }
      if (glu) {
#pragma unroll
        for (int u = 0; u < 2; ++u)
#pragma unroll
          for (int qt = 0; qt < 2; ++qt) {
            f32x4 a = z4;
#pragma unroll
            for (int kk = 0; kk < 5; ++kk) a = mfma16(wgl[u][kk], asb(xf[qt][kk]), a);
            X2[pb * 2560 + (6 + u * 2 + qt) * 256 + tid] = a;
          }
      }
    };
    auto xstore = [&](int t, int pb) {
      if (wave < 3) {
        const unsigned ob = (wave == 0 ? (unsigned)OFF_QX : (wave == 1 ? (unsigned)OFF_KX : (unsigned)OFF_VX)) + (unsigned)(t * H + h) * 8192u;
#pragma unroll
        for (int qt = 0; qt < 2; ++qt) {
          const f32x4* xp = X2 + pb * 2560 + (wave * 2 + qt) * 256 + lane;
          const f32x4 a = (xp[0] + xp[64]) + (xp[128] + xp[192]);
          const unsigned oo = ob + (unsigned)(((qt * 4 + (j >> 1)) * 64 + ml + 16 * (2 * (j & 1) + (g >> 1))) * 16 + (g & 1) * 8);
          const u32x2 vv = {pack2(a[0], a[1]), pack2(a[2], a[3])};
          st64(rw, oo, vv);
        }
      }
      if (glu && wave < 2) {
        const f32x4* xp = X2 + pb * 2560 + (6 + wave) * 256 + lane;
        const f32x4 ga = (xp[0] + xp[64]) + (xp[128] + xp[192]), ua = (xp[512] + xp[576]) + (xp[640] + xp[704]);
        float hv[4];
#pragma unroll
        for (int r = 0; r < 4; ++r) hv[r] = rbf(gelu_tanh(rbf(ga[r]))) * rbf(ua[r]);
        *(u32x2*)(ws + OFF_HH + (size_t)((2 * t + wave) * 54 + (cb >> 1)) * 1024 + lane * 16 + (cb & 1) * 8) = (u32x2){pack2(hv[0], hv[1]), pack2(hv[2], hv[3])};
      }
    };
    {
      u32x4 t1[15], tg[10];
#pragma unroll
      for (int i = 0; i < 10; ++i) tg[i] = *(const u32x4*)((i < 5 ? wg : wu) + (size_t)(160 * wave + 32 * (i % 5) + (lane >> 1)) * F + 16 * (glu ? cb : 0) + (lane & 1) * 8);
#pragma unroll
      for (int tt = 0; tt < 3; ++tt) {
        const u16* W = tt == 0 ? Wq : (tt == 1 ? Wk : Wv);
#pragma unroll
        for (int i = 0; i < 5; ++i) t1[tt * 5 + i] = *(const u32x4*)(W + (size_t)(160 * wave + 32 * i + (lane >> 1)) * HE + h * E + 16 * j + (lane & 1) * 8);
      }
#pragma unroll
      for (int kk = 0; kk < 5; ++kk) mf0[kk] = *(const u32x4*)(mem0 + (ml & 7) * D + 32 * (5 * wave + kk) + 8 * g);
      xload(0);
#pragma unroll
      for (int i = 0; i < 15; ++i) *(u32x4*)(my + i * 1024 + lane * 16) = t1[i];
      asm volatile("s_waitcnt lgkmcnt(0)" ::: "memory");
#pragma unroll
      for (int i = 0; i < 15; ++i) w1[i / 5][i % 5] = trfrag(i);
      asm volatile("s_waitcnt lgkmcnt(0)" ::: "memory");
#pragma unroll
      for (int i = 0; i < 10; ++i) *(u32x4*)(my + i * 1024 + lane * 16) = tg[i];
      asm volatile("s_waitcnt lgkmcnt(0)" ::: "memory");
#pragma unroll
      for (int i = 0; i < 10; ++i) wgl[i / 5][i % 5] = trfrag(i);
    }
    LBAR();
#pragma unroll 1
    for (int t = 0; t < 4; ++t) {
      xmma(0);
      xload(t + 1);
      LBAR();
      xstore(t, 0);
      LBAR();
    }
    xmma(1);
    xload(5);
    asm volatile("s_waitcnt vmcnt(0)" ::: "memory");
    LBAR();
    if (tid == 0) st128(rs, C_TOK + (unsigned)h * 128u + (unsigned)j * 16u, (u32x4){1u, 1u, 1u, 1u});
    LBAR();
    const unsigned m_off = (unsigned)(ml >> 3) * 512u + (unsigned)(g * 8 + (ml & 7)) * 16u;
    int r3 = 0;
    for (int s = 0; s < NSEG; ++s, r3 = r3 == 2 ? 0 : r3 + 1) {
      TS(0);
      const int r3p = r3 == 0 ? 2 : r3 - 1;
      f32x4 ac[3];
      if (s == 0) {
#pragma unroll
        for (int u = 0; u < 3; ++u) ac[u] = z4;
#pragma unroll
        for (int kk = 0; kk < 5; ++kk) {
#pragma unroll
          for (int u = 0; u < 3; ++u) ac[u] = mfma16(w1[u][kk], asb(mf0[kk]), ac[u]);
        }
      } else {
        const unsigned base = C_MEM + (unsigned)r3 * 32768u + (unsigned)(wave >> 1) * 16384u + (unsigned)(wave & 1) * 2560u + selA;
        SG(9); slp<SLP_A>(); SG(0);
        u32x4 mf[5];
        for (unsigned spin = 0;; ++spin) {
          asm volatile("" ::: "memory");
          mf[0] = ld128(rs, base + m_off); mf[2] = ld128(rs, base + m_off + 1024u); mf[4] = ld128(rs, base + 2048u + lo_off);
          const unsigned mx = umax3(umax3(mf[0][0], mf[0][2], mf[2][0]), mf[2][2], max(mf[4][0], mf[4][2]));
          if (__all(mx != 0xFFFFFFFFu) || spin >= spin_cap) { SPN(0); if (spin >= spin_cap) spin_cap = 0; break; }
        }
        mf[1] = ror8x4(mf[0]); mf[3] = ror8x4(mf[2]);
        SG(1);
#pragma unroll
        for (int u = 0; u < 3; ++u) ac[u] = z4;
#pragma unroll
        for (int kk = 0; kk < 5; ++kk) {
#pragma unroll
          for (int u = 0; u < 3; ++u) ac[u] = mfma16(w1[u][kk], asb(mf[kk]), ac[u]);
        }
      }
      TS(1);
#pragma unroll
      for (int u = 0; u < 3; ++u) X1[u * 256 + tid] = ac[u];
      LBAR();
      SG(2);
      if (wave < 2) {
        const int tl = 2 * wave + (ml >> 3);
        const f32x4* xp = X1 + (tl < 3 ? tl : 2) * 256 + g * 16 + (ml & 7);
        f32x4 a = (xp[0] + xp[64]) + (xp[128] + xp[192]);
        u32x2 v = {nosent(pack2(a[0], a[1])), nosent(pack2(a[2], a[3]))};
        const unsigned in = (unsigned)tl * 2048u + (unsigned)(((j >> 1) * 4 + 2 * (j & 1) + (g >> 1)) * 8 + (ml & 7)) * 16u + (g & 1) * 8u;
        if (tl < 3) {
          const unsigned off = C_QKV + (unsigned)(r3 * H + h) * 16384u + in;
          st64(rs, off + selA, v); SG(3); st64(rs, off + (8192u - selA), v);
          if (tl > 0) *(u32x2*)(ws + OFF_QM + (size_t)(s * H + h) * 6144 + in) = v;
          const unsigned offp = C_QKV + (unsigned)(r3p * H + h) * 16384u + in;
          st64(rs, offp, ones2); st64(rs, offp + 8192u, ones2);
        }
      }
      TS(2);
      if (s == NSEG - 1) break;
#define XSPOS 1
      if (s + 5 < NSEG) xmma(s & 1);
      if (XSPOS == 0 && s + 4 < NSEG) xstore(s + 4, (s + 1) & 1);
      if (s + 6 < NSEG) { if (glu) slp<SLP_X - 5>(); else slp<SLP_X>(); xload(s + 6); }
      LBAR();
      if (XSPOS == 1 && s + 4 < NSEG) xstore(s + 4, (s + 1) & 1);
      LBAR();
      if (XSPOS == 2 && s + 4 < NSEG) xstore(s + 4, (s + 1) & 1);
      SG(8);
    }
    return;
  }
  const int w4 = wave - 4;
  const bool three = w4 < 1;
  b16x8 w2[2][4];
  {
    u32x4 t2[8];
#pragma unroll
    for (int t = 0; t < 2; ++t) {
      const int ct = (t < 1 || three) ? w4 + 4 * t : 0;
#pragma unroll
      for (int i = 0; i < 4; ++i) t2[t * 4 + i] = *(const u32x4*)(Wo + (size_t)(h * E + 32 * i + (lane >> 1)) * D + 80 * j + 16 * ct + (lane & 1) * 8);
    }
#pragma unroll
    for (int i = 0; i < 8; ++i) *(u32x4*)(my + i * 1024 + lane * 16) = t2[i];
    asm volatile("s_waitcnt lgkmcnt(0)" ::: "memory");
#pragma unroll
    for (int i = 0; i < 8; ++i) w2[i / 4][i % 4] = trfrag(i);
  }
  b16x8 kx[2][4]; u32x4 vwx[2];
  const b16x8 ones8 = asb((u32x4){0x3F803F80u, 0x3F803F80u, 0x3F803F80u, 0x3F803F80u});
  auto prep = [&](int sn) {
    const char* kb = KV + (sn & 1) * 16384 + lane * 16;
    b16x8 vf[2][4];
#pragma unroll
    for (int kt = 0; kt < 2; ++kt)
#pragma unroll
      for (int ks = 0; ks < 4; ++ks) { kx[kt][ks] = *(const b16x8*)(kb + (kt * 4 + ks) * 1024); vf[kt][ks] = *(const b16x8*)(kb + 8192 + (kt * 4 + ks) * 1024); }
#pragma unroll
    for (int t = 0; t < 2; ++t) {
      f32x4 d0 = z4, d1 = z4;
#pragma unroll
      for (int ks = 0; ks < 4; ++ks) { d0 = mfma16(vf[0][ks], w2[t][ks], d0); d1 = mfma16(vf[1][ks], w2[t][ks], d1); }
      vwx[t] = (u32x4){pack2(d0[0], d0[1]), pack2(d0[2], d0[3]), pack2(d1[0], d1[1]), pack2(d1[2], d1[3])};
    }
  };
  const unsigned kvs = (w4 < 2 ? (unsigned)OFF_KX : (unsigned)OFF_VX) + (unsigned)h * 8192u + (unsigned)(w4 & 1) * 4096u + (unsigned)lane * 16u;
  u32x4 stg[4];
  LBAR();
#pragma unroll 1
  for (int t = 0; t < 4; ++t) { LBAR(); LBAR(); }
  LBAR();
  for (unsigned spin = 0;; ++spin) {
    asm volatile("" ::: "memory");
    const u32x4 tk = ld128(rs, C_TOK + (unsigned)h * 128u + (unsigned)(lane & 7) * 16u);
    if (__all(tk[0] != 0xFFFFFFFFu) || spin >= spin_cap) { if (spin >= spin_cap) spin_cap = 0; break; }
    __builtin_amdgcn_s_sleep(4);
  }
  {
    u32x4 t8[8];
#pragma unroll
    for (int i = 0; i < 8; ++i) t8[i] = ld128(rw, kvs + (unsigned)(i >> 2) * (unsigned)(H * 8192) + (unsigned)(i & 3) * 1024u);
#pragma unroll
    for (int i = 0; i < 4; ++i) stg[i] = ld128(rw, kvs + 2u * (unsigned)(H * 8192) + (unsigned)i * 1024u);
#pragma unroll
    for (int i = 0; i < 8; ++i) *(u32x4*)(KV + (i >> 2) * 16384 + w4 * 4096 + (i & 3) * 1024 + lane * 16) = t8[i];
  }
  LBAR();
  prep(0);
  const int cc = lane & 7, hg = lane >> 3;
  unsigned poff[2];
#pragma unroll
  for (int t = 0; t < 2; ++t) {
    const int T = 5 * j + w4 + 4 * t;
    const unsigned c16 = (unsigned)(((T >> 1) * 4 + 2 * (T & 1) + (g >> 1)) * 8 + (ml & 7)) * 2u + (unsigned)(g & 1), rb = c16 / 5u;
    poff[t] = C_P + (rb / 3u) * 16384u + (rb % 3u) * 2560u + (unsigned)h * 80u + (c16 % 5u) * 16u;
  }
  const unsigned q_off = (unsigned)(ml >> 3) * 512u + (unsigned)(g * 8 + (ml & 7)) * 16u, kv_off = 2048u + (unsigned)(ml >> 3) * 2048u + (unsigned)(g * 8 + (ml & 7)) * 16u;
  int r3 = 0;
  for (int s = 0; s < NSEG - 1; ++s, r3 = r3 == 2 ? 0 : r3 + 1) {
    const unsigned hb = C_QKV + (unsigned)(r3 * H + h) * 16384u + selA;
    SG(9);
    LBAR();
    SG(0);
    slp<SLP_B>(); SG(1);
    f32x4 a[2];
    u32x4 qf[4], kv[4];
    for (unsigned spin = 0;; ++spin) {
      asm volatile("" ::: "memory");
      qf[0] = ld128(rs, hb + q_off); qf[2] = ld128(rs, hb + q_off + 1024u);
#pragma unroll
      for (int ks = 0; ks < 4; ++ks) kv[ks] = ld128(rs, hb + kv_off + ks * 512u);
      unsigned mx = umax3(umax3(qf[0][0], qf[0][2], qf[2][0]), qf[2][2], 0u);
#pragma unroll
      for (int ks = 0; ks < 4; ++ks) mx = umax3(mx, kv[ks][0], kv[ks][2]);
      if (__all(mx != 0xFFFFFFFFu) || spin >= spin_cap) { SPN(1); if (spin >= spin_cap) spin_cap = 0; break; }
    }
    qf[1] = ror8x4(qf[0]); qf[3] = ror8x4(qf[2]);
    SG(2);
    {
#define SB() __builtin_amdgcn_sched_barrier(0)
      f32x4 s0 = z4, s1 = z4, sm = z4, dm[2] = {z4, z4};
#pragma unroll
      for (int ks = 0; ks < 4; ++ks) { s0 = mfma16(kx[0][ks], asb(qf[ks]), s0); s1 = mfma16(kx[1][ks], asb(qf[ks]), s1); SB(); }
#pragma unroll
      for (int ks = 0; ks < 4; ++ks) { sm = mfma16(asb(kv[ks]), asb(qf[ks]), sm); SB(); }
      TS(3);
      const float NEG = -INFINITY;
      const float c2 = SCALE * 1.4426950408889634f;
      float tv[12];
      dm[0] = mfma16(asb(kv[0]), w2[0][0], dm[0]);
#pragma unroll
      for (int r = 0; r < 4; ++r) tv[r] = s0[r] * c2;
      SB();
      dm[1] = mfma16(asb(kv[0]), w2[1][0], dm[1]);
#pragma unroll
      for (int r = 0; r < 4; ++r) tv[4 + r] = s1[r] * c2;
      SB();
      float m1 = fmaxf(fmaxf(tv[0], tv[1]), tv[2]), m2 = fmaxf(fmaxf(tv[3], tv[4]), tv[5]);
      m1 = fmaxf(fmaxf(m1, tv[6]), tv[7]);
      SB();
      dm[0] = mfma16(asb(kv[1]), w2[0][1], dm[0]);
#pragma unroll
      for (int r = 0; r < 4; ++r) tv[8 + r] = (g < 2) ? sm[r] * c2 : NEG;
      SB();
      dm[1] = mfma16(asb(kv[1]), w2[1][1], dm[1]);
      m2 = fmaxf(fmaxf(m2, tv[8]), tv[9]); m1 = fmaxf(fmaxf(m1, tv[10]), tv[11]);
      float mxv = fmaxf(m1, m2);
      { unsigned u = __float_as_uint(mxv); auto r = __builtin_amdgcn_permlane16_swap(u, u, false, false); mxv = fmaxf(__uint_as_float(r[0]) + 0.f, __uint_as_float(r[1]) + 0.f); }
      SB();
      { unsigned u = __float_as_uint(mxv); auto r = __builtin_amdgcn_permlane32_swap(u, u, false, false); mxv = fmaxf(__uint_as_float(r[0]) + 0.f, __uint_as_float(r[1]) + 0.f); }
      SB();
      dm[0] = mfma16(asb(kv[2]), w2[0][2], dm[0]);
#pragma unroll
      for (int r = 0; r < 2; ++r) tv[r] = __builtin_amdgcn_exp2f(tv[r] - mxv);
      SB();
      dm[1] = mfma16(asb(kv[2]), w2[1][2], dm[1]);
#pragma unroll
      for (int r = 2; r < 4; ++r) tv[r] = __builtin_amdgcn_exp2f(tv[r] - mxv);
      SB();
#pragma unroll
      for (int r = 4; r < 6; ++r) tv[r] = __builtin_amdgcn_exp2f(tv[r] - mxv);
      SB();
      dm[0] = mfma16(asb(kv[3]), w2[0][3], dm[0]);
#pragma unroll
      for (int r = 6; r < 8; ++r) tv[r] = __builtin_amdgcn_exp2f(tv[r] - mxv);
      SB();
      dm[1] = mfma16(asb(kv[3]), w2[1][3], dm[1]);
#pragma unroll
      for (int r = 8; r < 10; ++r) tv[r] = __builtin_amdgcn_exp2f(tv[r] - mxv);
      SB();
#pragma unroll
      for (int r = 10; r < 12; ++r) tv[r] = __builtin_amdgcn_exp2f(tv[r] - mxv);
      u32x4 px = {pack2(tv[0], tv[1]), pack2(tv[2], tv[3]), pack2(tv[4], tv[5]), pack2(tv[6], tv[7])};
      SB();
      f32x4 sx = mfma16(ones8, asb(px), z4);
      a[0] = mfma16(asb(vwx[0]), asb(px), z4);
      a[1] = mfma16(asb(vwx[1]), asb(px), z4);
      u32x4 pm;
      { const unsigned p0 = pack2(tv[8], tv[9]), p1 = pack2(tv[10], tv[11]);
        auto r0 = __builtin_amdgcn_permlane32_swap(0u, p0, false, false); auto r1 = __builtin_amdgcn_permlane32_swap(0u, p1, false, false);
        pm = (u32x4){r0[0], r1[0], 0u, 0u}; }
      SB();
      sx = mfma16(ones8, asb(pm), sx);
#pragma unroll
      for (int t = 0; t < 2; ++t) {
        u32x4 vwm = {g >= 2 ? pack2(dm[t][0], dm[t][1]) : 0u, g >= 2 ? pack2(dm[t][2], dm[t][3]) : 0u, 0u, 0u};
        a[t] = mfma16(asb(vwm), asb(pm), a[t]);
      }
      SB();
      const float inv = __builtin_amdgcn_rcpf(sx[0]);
      a[0] *= inv; a[1] *= inv;
    }
#pragma unroll
    for (int t = 0; t < 2; ++t) {
      if (t == 1 && !three) break;
      u32x4 v;
#pragma unroll
      for (int r = 0; r < 4; ++r) v[r] = min(__float_as_uint(a[t][r]), 0xFFFFFFFEu);
      const unsigned off = (unsigned)(1 + (s & 1)) * PAGE + poff[t];
      if (ml < 8) { st128(rs, off, v); st128(rs, off + 8192u, v); }
    }
    SG(3);
    TS(4);
    LBAR();
    SG(4);
    auto kvwrite = [&] {
      if (s + 2 < NSEG - 1) {
#pragma unroll
        for (int i = 0; i < 4; ++i) *(u32x4*)(KV + (s & 1) * 16384 + w4 * 4096 + i * 1024 + lane * 16) = stg[i];
      }
    };
    if (w4 == 3) {
      const int r3n = r3 == 2 ? 0 : r3 + 1;
      kvwrite();
        const unsigned offw = (unsigned)(1 + (s & 1)) * PAGE + C_P + (unsigned)(cb / 3) * 16384u + (unsigned)(cb % 3) * 2560u + (unsigned)hg * 320u + (unsigned)cc * 16u;
        const unsigned off = cc < 5 ? offw + ((s & 1) ? selP1 : selP0) : OOB;
        slp<SLP_C>(); SG(5);
        u32x4 v[4];
        for (unsigned spin = 0;; ++spin) {
          asm volatile("" ::: "memory");
#pragma unroll
          for (int i = 0; i < 4; ++i) v[i] = ld128(rs, off + i * 80u);
          unsigned mx = 0;
#pragma unroll
          for (int i = 0; i < 4; ++i) { mx = umax3(mx, v[i][0], v[i][1]); mx = umax3(mx, v[i][2], v[i][3]); }
          if (__all(mx != 0xFFFFFFFFu) || spin >= spin_cap) { SPN(2); if (spin >= spin_cap) spin_cap = 0; break; }
        }
        SG(6);
        TS(5);
        float f[4];
#pragma unroll
        for (int r = 0; r < 4; ++r) {
          float x = (__uint_as_float(v[0][r]) + __uint_as_float(v[1][r])) + (__uint_as_float(v[2][r]) + __uint_as_float(v[3][r]));
          x += ror8(x); x = xsum32(xsum16(x)); f[r] = x;
        }
        u32x2 w = {nosent(pack2(f[0], f[1])), nosent(pack2(f[2], f[3]))};
        const unsigned mq = (unsigned)cb >> 6, min_ = (mq >> 1) * 16384u + (mq & 1) * 2560u + (unsigned)(5 * (cb & 63) + lane) * 8u;
        if (lane < 5) { st64(rs, C_MEM + (unsigned)r3n * 32768u + min_, w); st64(rs, C_MEM + (unsigned)r3n * 32768u + 8192u + min_, w); }
        SG(7);
        TS(6);
        LBAR();
        if (cc < 5) {
#pragma unroll
          for (int i = 0; i < 4; ++i) { st128(rs, offw + i * 80u, ones4); st128(rs, offw + 8192u + i * 80u, ones4); }
        }
        if (lane < 5) { st64(rs, C_MEM + (unsigned)r3 * 32768u + min_, ones2); st64(rs, C_MEM + (unsigned)r3 * 32768u + 8192u + min_, ones2); }
      if (s + 1 < NSEG - 1) prep(s + 1);
    } else {
      if (s + 1 < NSEG - 1) prep(s + 1);
      kvwrite();
      LBAR();
    }
    if (s + 3 < NSEG - 1) {
#pragma unroll
      for (int i = 0; i < 4; ++i) stg[i] = ld128(rw, kvs + (unsigned)(s + 3) * (unsigned)(H * 8192) + (unsigned)i * 1024u);
    }
  }
  LBAR();
}

__global__ __launch_bounds__(512, 1) void k_chain(const u16* __restrict__ mem0, const u16* __restrict__ Wq, const u16* __restrict__ Wk,
                                                   const u16* __restrict__ Wv, const u16* __restrict__ Wo, const u16* __restrict__ wg,
                                                   const u16* __restrict__ wu, char* __restrict__ ws) {
  extern __shared__ __attribute__((aligned(16))) char dyn_lds[];
  chain_role(blockIdx.x, mem0, Wq, Wk, Wv, Wo, wg, wu, ws, dyn_lds);
}

DI void attn_unit(const int s, const int h, const b16x8 (&qf)[2][4], char* __restrict__ ws, char* V) {
  const int lane = threadIdx.x & 63, ml = lane & 15, g = lane >> 4;
  const char* KX = ws + OFF_KX + (size_t)(s * H + h) * 8192 + lane * 16;
  const char* VX = ws + OFF_VX + (size_t)(s * H + h) * 8192 + lane * 16;
  const char* KM = ws + OFF_QM + (size_t)(s * H + h) * 6144 + 2048 + (g * 8 + (ml & 7)) * 16;
  b16x8 kx[2][4], km[4];
#pragma unroll
  for (int ks = 0; ks < 4; ++ks) {
#pragma unroll
    for (int t = 0; t < 2; ++t) {
      kx[t][ks] = *(const b16x8*)(KX + (t * 4 + ks) * 1024);
      *(u32x4*)(V + (16 * t + ml) * 272 + (32 * ks + 8 * g) * 2) = *(const u32x4*)(VX + (t * 4 + ks) * 1024);
    }
    km[ks] = *(const b16x8*)(KM + ks * 512);
    u32x4 vmr = *(const u32x4*)(KM + 2048 + ks * 512);
    if (ml >= 8) vmr = (u32x4){0, 0, 0, 0};
    *(u32x4*)(V + (32 + ml) * 272 + (32 * ks + 8 * g) * 2) = vmr;
  }
  u32x4 px[2], pm[2];
#pragma unroll
  for (int qt = 0; qt < 2; ++qt) {
    f32x4 s0 = {0, 0, 0, 0}, s1 = {0, 0, 0, 0}, sm = {0, 0, 0, 0};
#pragma unroll
    for (int ks = 0; ks < 4; ++ks) {
      s0 = mfma16(kx[0][ks], qf[qt][ks], s0);
      s1 = mfma16(kx[1][ks], qf[qt][ks], s1);
      sm = mfma16(km[ks], qf[qt][ks], sm);
    }
    const int qi = 16 * qt + ml;
    float sv[12];
#pragma unroll
    for (int r = 0; r < 4; ++r) {
      sv[r] = (4 * g + r <= qi) ? rbf(rbf(s0[r]) * SCALE) : -INFINITY;
      sv[4 + r] = (16 + 4 * g + r <= qi) ? rbf(rbf(s1[r]) * SCALE) : -INFINITY;
      sv[8 + r] = (g < 2) ? rbf(rbf(sm[r]) * SCALE) : -INFINITY;
    }
    float mxv = sv[0];
#pragma unroll
    for (int r = 1; r < 12; ++r) mxv = fmaxf(mxv, sv[r]);
    mxv = xmax32(xmax16(mxv));
    float sum = 0.f;
#pragma unroll
    for (int r = 0; r < 12; ++r) { sv[r] = __expf(sv[r] - mxv); sum += sv[r]; }
    sum = xsum32(xsum16(sum));
    const float inv = 1.f / sum;
    px[qt] = (u32x4){pack2(sv[0] * inv, sv[1] * inv), pack2(sv[2] * inv, sv[3] * inv), pack2(sv[4] * inv, sv[5] * inv), pack2(sv[6] * inv, sv[7] * inv)};
    pm[qt] = (u32x4){pack2(sv[8] * inv, sv[9] * inv), pack2(sv[10] * inv, sv[11] * inv), 0u, 0u};
  }
  asm volatile("s_waitcnt lgkmcnt(0)" ::: "memory");
  const unsigned vb = (unsigned)(size_t)V + (4 * g + (ml >> 2)) * 272 + (lane & 3) * 8;
#pragma unroll
  for (int kk = 0; kk < 4; ++kk) {
    u32x4 of[2];
#pragma unroll
    for (int hf = 0; hf < 2; ++hf) {
      const int et = 2 * kk + hf;
      s16x4 v0 = __builtin_amdgcn_ds_read_tr16_b64_v4i16((LAS s16x4*)(vb + et * 32));
      s16x4 v1 = __builtin_amdgcn_ds_read_tr16_b64_v4i16((LAS s16x4*)(vb + 16 * 272 + et * 32));
      s16x4 v2 = __builtin_amdgcn_ds_read_tr16_b64_v4i16((LAS s16x4*)(vb + 32 * 272 + et * 32));
      typedef short s16x8 __attribute__((ext_vector_type(8)));
      s16x8 va = {v0[0], v0[1], v0[2], v0[3], v1[0], v1[1], v1[2], v1[3]};
      s16x8 vbm = {v2[0], v2[1], v2[2], v2[3], 0, 0, 0, 0};
#pragma unroll
      for (int qt = 0; qt < 2; ++qt) {
        f32x4 o = {0, 0, 0, 0};
        o = mfma16(__builtin_bit_cast(b16x8, va), asb(px[qt]), o);
        o = mfma16(__builtin_bit_cast(b16x8, vbm), asb(pm[qt]), o);
        of[qt][2 * hf] = pack2(o[0], o[1]); of[qt][2 * hf + 1] = pack2(o[2], o[3]);
      }
    }
#pragma unroll
    for (int qt = 0; qt < 2; ++qt) *(u32x4*)(ws + OFF_O + (size_t)((s * 2 + qt) * 128 + h * 4 + kk) * 1024 + lane * 16) = of[qt];
  }
  asm volatile("s_waitcnt lgkmcnt(0)" ::: "memory");
}

__global__ __launch_bounds__(512, 1) void k_post1(const u16* __restrict__ x, const u16* __restrict__ Wo, const u16* __restrict__ wd,
                                                  const u16* __restrict__ gate_w, char* __restrict__ ws) {
  extern __shared__ __attribute__((aligned(16))) char lds[];
  const int lane = threadIdx.x & 63, wave = __builtin_amdgcn_readfirstlane(threadIdx.x >> 6);
  const int gw = blockIdx.x * 8 + wave;
  char* V = lds + wave * (48 * 272);
  {
    b16x8 q0[2][4];
#pragma unroll
    for (int t = 0; t < 2; ++t)
#pragma unroll
      for (int ks = 0; ks < 4; ++ks) q0[t][ks] = *(const b16x8*)(ws + OFF_QX + (size_t)gw * 8192 + (t * 4 + ks) * 1024 + lane * 16);
    attn_unit(gw >> 5, gw & 31, q0, ws, V);
  }
  {
    const int ml = lane & 15, g = lane >> 4;
    for (int t = gw; t < S; t += 2048) {
      float s = 0.f;
#pragma unroll
      for (int i = 0; i < 5; ++i) {
        const unsigned xv = *(const unsigned*)(x + (size_t)t * D + i * 128 + lane * 2), gv = *(const unsigned*)(gate_w + i * 128 + lane * 2);
        s += rbf(__uint_as_float(xv << 16) * __uint_as_float(gv << 16)) + rbf(__uint_as_float(xv & 0xFFFF0000u) * __uint_as_float(gv & 0xFFFF0000u));
      }
#pragma unroll
      for (int o = 32; o >= 1; o >>= 1) s += __shfl_xor(s, o);
      s = rbf(s);
      float gg = rbf(1.f / (1.f + __expf(-s)));
      if (lane == 0) ((float*)(ws + OFF_G))[t] = gg;
    }
    char* my = V;
    const unsigned trb = (unsigned)(size_t)my + (4 * g + (ml >> 2)) * 32 + (lane & 3) * 8;
    for (int u = gw; u < 182 * 5; u += 2048) {
      const int kk = u / 5, c0 = (u % 5) * 8;
      const u16* W = kk < 128 ? Wo + (size_t)32 * kk * D : wd + (size_t)32 * (kk - 128) * D;
      u32x4 t8[8];
#pragma unroll
      for (int i = 0; i < 8; ++i) t8[i] = *(const u32x4*)(W + (size_t)(4 * i + (lane >> 4)) * D + 16 * c0 + (lane & 15) * 8);
#pragma unroll
      for (int i = 0; i < 8; ++i) *(u32x4*)(my + ((lane & 15) >> 1) * 1024 + (4 * i + (lane >> 4)) * 32 + (lane & 1) * 16) = t8[i];
      asm volatile("s_waitcnt lgkmcnt(0)" ::: "memory");
#pragma unroll
      for (int i = 0; i < 8; ++i) {
        s16x4 lo = __builtin_amdgcn_ds_read_tr16_b64_v4i16((LAS s16x4*)(trb + i * 1024));
        s16x4 hi = __builtin_amdgcn_ds_read_tr16_b64_v4i16((LAS s16x4*)(trb + i * 1024 + 512));
        typedef short s16x8 __attribute__((ext_vector_type(8)));
        s16x8 tt = {lo[0], lo[1], lo[2], lo[3], hi[0], hi[1], hi[2], hi[3]};
        *(s16x8*)(ws + OFF_WF + ((size_t)(c0 + i) * 182 + kk) * 1024 + lane * 16) = tt;
      }
      asm volatile("s_waitcnt lgkmcnt(0)" ::: "memory");
    }
  }
}

__global__ __launch_bounds__(512, 1) void k_post2(const char* __restrict__ ws, u16* __restrict__ out) {
  extern __shared__ __attribute__((aligned(16))) char lds[];
  const int tid = threadIdx.x, lane = tid & 63, wave = __builtin_amdgcn_readfirstlane(tid >> 6), ml = lane & 15, g = lane >> 4;
  const int b = blockIdx.x, xc = b & 7, idx = b >> 3, tt = xc * 4 + (idx >> 3), cgp = idx & 7;
  f32x4* X = (f32x4*)lds;
  f32x4 asum[2][2];
#pragma unroll
  for (int ph = 0; ph < 2; ++ph) {
    const int kt = ph == 0 ? 128 : 54, nk = ph == 0 ? 16 : (wave < 6 ? 7 : 6);
    const char* tb = ws + (ph ? OFF_HH : OFF_O) + (size_t)(tt * 4) * kt * 1024 + (size_t)wave * 1024 + lane * 16;
    const size_t tstr = (size_t)kt * 1024;
    const char* wb = ws + OFF_WF + ((size_t)(cgp * 5) * 182 + (ph ? 128 : 0) + wave) * 1024 + lane * 16;
    f32x4 acc[4][5];
#pragma unroll
    for (int i = 0; i < 4; ++i)
#pragma unroll
      for (int c = 0; c < 5; ++c) acc[i][c] = (f32x4){0, 0, 0, 0};
    b16x8 tf[4][4], wf[4][5];
    auto ldk = [&](int slot, int k) {
      const int kn = k < nk ? k : nk - 1;
#pragma unroll
      for (int i = 0; i < 4; ++i) tf[slot][i] = *(const b16x8*)(tb + i * tstr + (size_t)kn * 8192);
#pragma unroll
      for (int c = 0; c < 5; ++c) wf[slot][c] = *(const b16x8*)(wb + (size_t)c * 182 * 1024 + (size_t)kn * 8192);
    };
    ldk(0, 0); ldk(1, 1); ldk(2, 2);
    for (int k = 0; k < nk; k += 4) {
#pragma unroll
      for (int u = 0; u < 4; ++u) {
        ldk((u + 3) & 3, k + u + 3);
        if (k + u < nk) {
#pragma unroll
          for (int i = 0; i < 4; ++i)
#pragma unroll
            for (int c = 0; c < 5; ++c) acc[i][c] = mfma16(wf[u][c], tf[u][i], acc[i][c]);
        }
      }
    }
#pragma unroll
    for (int rd = 0; rd < 2; ++rd) {
#pragma unroll
      for (int t = 0; t < 10; ++t) { const int tl = rd * 10 + t; X[(wave * 10 + t) * 64 + lane] = acc[tl / 5][tl % 5]; }
      RBAR();
#pragma unroll
      for (int u = 0; u < 2; ++u) {
        const int t = wave + 8 * u;
        if (t < 10) {
          const f32x4* xp = X + t * 64 + lane;
          const f32x4 v = ((xp[0] + xp[640]) + (xp[1280] + xp[1920])) + ((xp[2560] + xp[3200]) + (xp[3840] + xp[4480]));
          if (ph == 0) asum[rd][u] = v;
          else {
            const int tl = rd * 10 + t, i = tl / 5, c = tl % 5;
            const int tok = tt * 64 + 16 * i + ml;
            const float gg = ((const float*)(ws + OFF_G))[tok], og = rbf(1.f - gg);
            float r[4];
#pragma unroll
            for (int q = 0; q < 4; ++q) { float ta = rbf(gg * rbf(asum[rd][u][q])), tb2 = rbf(og * rbf(v[q])); asm volatile("" : "+v"(ta), "+v"(tb2)); r[q] = ta + tb2; }
            *(u32x2*)(out + (size_t)tok * D + 80 * cgp + 16 * c + 4 * g) = (u32x2){pack2(r[0], r[1]), pack2(r[2], r[3])};
          }
        }
      }
      RBAR();
    }
  }
}

extern "C" void kernel_launch(void* const* d_in, const int* in_sizes, int n_in, void* d_out, int out_size, void* d_ws, size_t ws_size,
                              hipStream_t stream) {
  const u16* x = (const u16*)d_in[0];
  const u16* mem0 = (const u16*)d_in[1];
  const u16* Wq = (const u16*)d_in[2];
  const u16* Wk = (const u16*)d_in[3];
  const u16* Wv = (const u16*)d_in[4];
  const u16* Wo = (const u16*)d_in[5];
  const u16* wg = (const u16*)d_in[8];
  const u16* wu = (const u16*)d_in[9];
  const u16* wd = (const u16*)d_in[10];
  const u16* gw = (const u16*)d_in[11];
  char* ws = (char*)d_ws;
  k_init<<<(S * D / 8 + (2 * PAGE + C_P + 86 * 16384) / 16 + 255) / 256, 256, 0, stream>>>(x, ws);
  hipFuncSetAttribute((const void*)k_chain, hipFuncAttributeMaxDynamicSharedMemorySize, 156 * 1024);
  k_chain<<<256, 512, 156 * 1024, stream>>>(mem0, Wq, Wk, Wv, Wo, wg, wu, ws);
  hipFuncSetAttribute((const void*)k_post1, hipFuncAttributeMaxDynamicSharedMemorySize, 128 * 1024);
  k_post1<<<256, 512, 104 * 1024 + 512, stream>>>(x, Wo, wd, gw, ws);
  hipFuncSetAttribute((const void*)k_post2, hipFuncAttributeMaxDynamicSharedMemorySize, 80 * 1024);
  k_post2<<<256, 512, 80 * 1024, stream>>>(ws, (u16*)d_out);
}
```

```cpp
#include <hip/hip_runtime.h>
#include <hip/hip_bf16.h>
#include <stdint.h>
#include <stdio.h>

typedef unsigned short u16;
typedef __bf16 b16x8 __attribute__((ext_vector_type(8)));
typedef short s16x4 __attribute__((ext_vector_type(4)));
typedef float f32x4 __attribute__((ext_vector_type(4)));
typedef unsigned u32x4 __attribute__((ext_vector_type(4)));
typedef unsigned u32x2 __attribute__((ext_vector_type(2)));
#define LAS __attribute__((address_space(3)))
#define DI __device__ __forceinline__

constexpr int S = 2048, D = 640, H = 32, E = 128, HE = 4096, MM = 8, SEG = 32, NSEG = 64, F = 1728;
constexpr float SCALE = 0.08838834764831845f;

constexpr size_t OFF_KX = 0;
constexpr size_t OFF_VX = OFF_KX + (size_t)S * HE * 2;
constexpr size_t OFF_O  = OFF_VX + (size_t)S * HE * 2;
constexpr size_t OFF_HH = OFF_O + (size_t)S * HE * 2;
constexpr size_t OFF_WF = OFF_HH + (size_t)S * F * 2;
constexpr size_t OFF_G  = OFF_WF + (size_t)40 * 182 * 1024;
constexpr size_t OFF_QM = OFF_G + (size_t)S * 4;
constexpr size_t OFF_COMM = OFF_QM + (size_t)NSEG * H * 6144;
constexpr size_t OFF_END = OFF_COMM + (size_t)8 * 1048576;
constexpr size_t OFF_DBG = OFF_END;
constexpr unsigned PAGE = 2097152u, C_MEM = 65536u, C_QKV = 196608u, C_P = 65536u;
DI char* comm_base(char* ws) { return (char*)(((size_t)(ws + OFF_COMM) + (PAGE - 1)) & ~(size_t)(PAGE - 1)); }

DI float bf2f(u16 v) { return __builtin_bit_cast(float, (unsigned)v << 16); }
DI u16 f2bf(float f) { return __builtin_bit_cast(u16, (__bf16)f); }
DI float rbf(float f) { return (float)(__bf16)f; }
typedef float f32x2 __attribute__((ext_vector_type(2)));
typedef __bf16 b16x2 __attribute__((ext_vector_type(2)));
DI unsigned pack2(float lo, float hi) { f32x2 v = {lo, hi}; return __builtin_bit_cast(unsigned, __builtin_convertvector(v, b16x2)); }
DI f32x4 mfma16(b16x8 a, b16x8 b, f32x4 c) { return __builtin_amdgcn_mfma_f32_16x16x32_bf16(a, b, c, 0, 0, 0); }
DI b16x8 asb(u32x4 v) { return __builtin_bit_cast(b16x8, v); }

DI float gelu_tanh(float x) {
  float y = 0.7978845608028654f * (x + 0.044715f * x * x * x);
  float t = 1.f - 2.f / (__expf(2.f * y) + 1.f);
  return 0.5f * x * (1.f + t);
}

#define RBAR() asm volatile("s_waitcnt lgkmcnt(0)\n\ts_barrier" ::: "memory")
DI unsigned fB(unsigned row) { return (row & 3) | (((row >> 3) & 1) << 2); }
template <int BMODE>
DI void gemm256(const u16* __restrict__ A, int lda, const u16* __restrict__ B0, const u16* __restrict__ B1, int ldb, int ncols, int K,
                int m0, int n0, char* lds, f32x4 (&acc)[8][4]) {
  const int tid = threadIdx.x, lane = tid & 63, wave = __builtin_amdgcn_readfirstlane(tid >> 6), wr = wave >> 2, wc = wave & 3;
  const int fr = lane & 15, g = lane >> 4;
  unsigned offA[4], offB[2];
#pragma unroll
  for (int i = 0; i < 4; ++i) {
    const unsigned L = (unsigned)(i * 8 + wave) * 1024u + lane * 16u;
    const unsigned row = L >> 7, ch = (L >> 4) & 7; offA[i] = (m0 + row) * lda + ((ch ^ (row & 7)) * 8);
  }
#pragma unroll
  for (int i = 0; i < 2; ++i) {
    const unsigned L = (unsigned)(i * 8 + wave) * 1024u + lane * 16u;
    const unsigned row = L >> 8, chp = (L >> 4) & 15, ch = chp ^ (fB(row) << 1);
    int col = n0 + ch * 8; if (BMODE == 1) col = col < ncols ? col : ncols - 8;
    offB[i] = row * ldb + col;
  }
  const u16* Bs1 = BMODE == 0 ? B0 + 128 : B1;
  auto stage = [&](int buf, int k0) {
    const u16* Ak = A + k0; const u16* B0k = B0 + (size_t)k0 * ldb; const u16* B1k = Bs1 + (size_t)k0 * ldb;
#pragma unroll
    for (int i = 0; i < 4; ++i) {
      __builtin_amdgcn_global_load_lds((const unsigned*)(Ak + offA[i]), (LAS unsigned*)(lds + buf * 65536 + (i * 8 + wave) * 1024), 16, 0, 0);
      __builtin_amdgcn_global_load_lds((const unsigned*)((i < 2 ? B0k : B1k) + offB[i & 1]), (LAS unsigned*)(lds + buf * 65536 + 32768 + (i * 8 + wave) * 1024), 16, 0, 0);
    }
  };
  const unsigned ldsb = (unsigned)(size_t)lds;
  const unsigned q4 = fr >> 2, p4 = lane & 3;
  const int nt = K / 64;
  stage(0, 0);
  asm volatile("s_waitcnt vmcnt(0)" ::: "memory"); RBAR();
  if (wr == 1) RBAR();
  for (int t = 0; t < nt; ++t) {
    const int buf = t & 1;
    stage(buf ^ 1, t + 1 < nt ? (t + 1) * 64 : 0);
    const char* sA = lds + buf * 65536;
    const unsigned sB = ldsb + buf * 65536 + 32768;
#pragma unroll
    for (int ks = 0; ks < 2; ++ks) {
      b16x8 af[8], bfr[4];
#pragma unroll
      for (int jn = 0; jn < 4; ++jn) {
        const unsigned sub = BMODE == 0 ? (wc >> 1) : (jn >> 1), c = BMODE == 0 ? ((wc & 1) * 4 + jn) : (2 * wc + (jn & 1));
        const unsigned r0 = 32 * ks + 8 * g + q4, r1 = r0 + 4;
        s16x4 lo = __builtin_amdgcn_ds_read_tr16_b64_v4i16((LAS s16x4*)(sB + sub * 16384 + r0 * 256 + (((2 * c + (p4 >> 1)) ^ (fB(r0) << 1)) << 4) + 8 * (p4 & 1)));
        s16x4 hi = __builtin_amdgcn_ds_read_tr16_b64_v4i16((LAS s16x4*)(sB + sub * 16384 + r1 * 256 + (((2 * c + (p4 >> 1)) ^ (fB(r1) << 1)) << 4) + 8 * (p4 & 1)));
        typedef short s16x8 __attribute__((ext_vector_type(8)));
        s16x8 tt = {lo[0], lo[1], lo[2], lo[3], hi[0], hi[1], hi[2], hi[3]};
        bfr[jn] = __builtin_bit_cast(b16x8, tt);
      }
#pragma unroll
      for (int i = 0; i < 8; ++i) { const unsigned row = wr * 128 + i * 16 + fr; af[i] = *(const b16x8*)(sA + row * 128 + (((ks * 4 + g) ^ (row & 7)) << 4)); }
      if (ks == 1 && wr == 1) asm volatile("s_waitcnt vmcnt(0)" ::: "memory");
      RBAR();
      __builtin_amdgcn_s_setprio(1);
#pragma unroll
      for (int i = 0; i < 8; ++i)
#pragma unroll
        for (int jn = 0; jn < 4; ++jn) acc[i][jn] = mfma16(bfr[jn], af[i], acc[i][jn]);
      __builtin_amdgcn_s_setprio(0);
      if (ks == 1 && wr == 0) asm volatile("s_waitcnt vmcnt(0)" ::: "memory");
      RBAR();
    }
  }
  if (wr == 0) RBAR();
}

__global__ __launch_bounds__(512, 1) void k_pre(const u16* __restrict__ x, const u16* __restrict__ Wk, const u16* __restrict__ Wv,
                                                const u16* __restrict__ Wq, const u16* __restrict__ Wo, char* __restrict__ ws) {
  extern __shared__ __attribute__((aligned(16))) char lds[];
  const int b = blockIdx.x;
  const int nt = (b & 7) * 4 + (b >> 6), mt = (b >> 3) & 7, which = nt >> 4, n0 = (nt & 15) * 256, m0 = mt * 256;
  f32x4 acc[8][4];
#pragma unroll
  for (int i = 0; i < 8; ++i)
#pragma unroll
    for (int j = 0; j < 4; ++j) acc[i][j] = (f32x4){0, 0, 0, 0};
  gemm256<0>(x, D, which == 0 ? Wk : Wv, nullptr, HE, HE, D, m0, n0, lds, acc);
  char* out = ws + (which == 0 ? OFF_KX : OFF_VX);
  int tz = threadIdx.x; asm volatile("" : "+v"(tz));
  const int wr = tz >> 8, wc = (tz >> 6) & 3, fr = tz & 15, g = (tz >> 4) & 3;
#pragma unroll
  for (int i = 0; i < 8; ++i)
#pragma unroll
    for (int j = 0; j < 4; ++j) {
      u32x2 v = {pack2(acc[i][j][0], acc[i][j][1]), pack2(acc[i][j][2], acc[i][j][3])};
      const int tok = m0 + wr * 128 + i * 16 + fr, n = n0 + wc * 64 + j * 16 + 4 * g;
      const int seg = tok >> 5, kt = (tok >> 4) & 1, hh = n >> 7, e = n & 127;
      *(u32x2*)(out + (size_t)(seg * H + hh) * 8192 + (size_t)(((kt * 4 + (e >> 5)) * 64 + (tok & 15) + 16 * ((e & 31) >> 3)) * 16 + (e & 4) * 2)) = v;
    }
  const u32x4 ones = {0xFFFFFFFFu, 0xFFFFFFFFu, 0xFFFFFFFFu, 0xFFFFFFFFu};
  char* cbp = comm_base(ws);
#pragma unroll
  for (int t = 0; t < 3; ++t) *(u32x4*)(cbp + (size_t)t * PAGE + (size_t)(b * 512 + tz) * 16) = ones;
}

#define SC1 16
DI u32x4 ld128(__amdgpu_buffer_rsrc_t rs, unsigned off) { return __builtin_amdgcn_raw_buffer_load_b128(rs, off, 0, SC1); }
DI u32x2 ld64(__amdgpu_buffer_rsrc_t rs, unsigned off) { return __builtin_amdgcn_raw_buffer_load_b64(rs, off, 0, SC1); }
DI void st128(__amdgpu_buffer_rsrc_t rs, unsigned off, u32x4 v) { __builtin_amdgcn_raw_buffer_store_b128(v, rs, off, 0, SC1); }
DI void st64(__amdgpu_buffer_rsrc_t rs, unsigned off, u32x2 v) { __builtin_amdgcn_raw_buffer_store_b64(v, rs, off, 0, SC1); }
DI unsigned nosent(unsigned v) { return v == 0xFFFFFFFFu ? 0xFFFEFFFFu : v; }
DI unsigned umax3(unsigned a, unsigned b, unsigned c) { return max(max(a, b), c); }
DI float xsum16(float x) { unsigned u = __float_as_uint(x); auto r = __builtin_amdgcn_permlane16_swap(u, u, false, false); return __uint_as_float(r[0]) + __uint_as_float(r[1]); }
DI float xsum32(float x) { unsigned u = __float_as_uint(x); auto r = __builtin_amdgcn_permlane32_swap(u, u, false, false); return __uint_as_float(r[0]) + __uint_as_float(r[1]); }
DI float xmax16(float x) { unsigned u = __float_as_uint(x); auto r = __builtin_amdgcn_permlane16_swap(u, u, false, false); return fmaxf(__uint_as_float(r[0]), __uint_as_float(r[1])); }
DI float xmax32(float x) { unsigned u = __float_as_uint(x); auto r = __builtin_amdgcn_permlane32_swap(u, u, false, false); return fmaxf(__uint_as_float(r[0]), __uint_as_float(r[1])); }
DI float ror4(float x) { return __uint_as_float(__builtin_amdgcn_update_dpp(0u, __float_as_uint(x), 0x124, 0xf, 0xf, false)); }
DI float ror8(float x) { return __uint_as_float(__builtin_amdgcn_update_dpp(0u, __float_as_uint(x), 0x128, 0xf, 0xf, false)); }
DI u32x4 ror8x4(u32x4 v) { u32x4 r; for (int i = 0; i < 4; ++i) r[i] = __builtin_amdgcn_update_dpp(0u, v[i], 0x128, 0xf, 0xf, false); return r; }
#define LBAR() asm volatile("s_waitcnt lgkmcnt(0)\n\ts_barrier" ::: "memory")
template <int N> DI void slp() { if constexpr (N > 0) { __builtin_amdgcn_s_sleep(N > 15 ? 15 : N); if constexpr (N > 15) slp<N - 15>(); } }

#define SG(i)
#define TS(i)
#define SPN(k)
constexpr unsigned SPIN_CAP = 1u << 17;
#define SLP_A 15
#define SLP_B 13
#define SLP_B3 4
#define SLP_C 10

DI void chain_role(const int cb, const u16* __restrict__ mem0, const u16* __restrict__ Wq, const u16* __restrict__ Wk,
                   const u16* __restrict__ Wv, const u16* __restrict__ Wo, char* __restrict__ ws, char* dyn_lds) {
  const int xcd = cb & 7, y = cb >> 3, h = xcd * 4 + (y >> 3), j = y & 7;
  const int tid = threadIdx.x, lane = tid & 63, wave = __builtin_amdgcn_readfirstlane(tid >> 6);
  const int ml = lane & 15, g = lane >> 4;
  const __amdgpu_buffer_rsrc_t rs = __builtin_amdgcn_make_buffer_rsrc(comm_base(ws), 0, (int)(3 * PAGE), 0x00020000);
  unsigned selA, selP0, selP1;
  {
    unsigned* pl = (unsigned*)(dyn_lds + 156 * 1024 - 64);
    if (wave == 0) {
#pragma unroll 1
      for (int p = 0; p < 4; ++p) {
        const unsigned po = (unsigned)(p < 2 ? 0 : p - 1) * PAGE + (unsigned)(cb >> 6) * 16384u + (unsigned)(p == 1) * 8192u + (unsigned)(cb & 63) * 128u;
        asm volatile("s_waitcnt vmcnt(0) lgkmcnt(0)" ::: "memory");
        const unsigned long long t0 = __builtin_amdgcn_s_memtime();
        asm volatile("s_waitcnt lgkmcnt(0)" ::: "memory");
        const unsigned v = __builtin_amdgcn_raw_buffer_load_b32(rs, po, 0, SC1);
        asm volatile("s_waitcnt vmcnt(0)" :: "v"(v) : "memory");
        const unsigned long long t1 = __builtin_amdgcn_s_memtime();
        if (lane == 0) pl[p] = (unsigned)(t1 - t0);
      }
    }
    LBAR();
    selA = __builtin_amdgcn_readfirstlane(pl[1] < pl[0] ? 8192u : 0u);
    selP0 = __builtin_amdgcn_readfirstlane(2 * pl[2] > pl[0] + pl[1] ? 8192u : 0u);
    selP1 = __builtin_amdgcn_readfirstlane(2 * pl[3] > pl[0] + pl[1] ? 8192u : 0u);
  }
  const u32x4 ones4 = {0xFFFFFFFFu, 0xFFFFFFFFu, 0xFFFFFFFFu, 0xFFFFFFFFu};
  const u32x2 ones2 = {0xFFFFFFFFu, 0xFFFFFFFFu};
  f32x4* X1 = (f32x4*)dyn_lds;
  char* KV = dyn_lds + 24576;
  char* WL = dyn_lds + 57344;
  const f32x4 z4 = {0, 0, 0, 0};
  const unsigned OOB = 0x7FFFFF00u;
  const unsigned lo_off = ml < 8 ? (unsigned)(g * 8 + ml) * 16u : OOB;
  unsigned spin_cap = SPIN_CAP;
  typedef short s16x8 __attribute__((ext_vector_type(8)));
  char* my = WL + (wave < 4 ? wave * 15360 : 61440 + (wave - 4) * 8192);
  const unsigned trb = (unsigned)(size_t)my + (8 * g + (ml >> 2)) * 32 + (lane & 3) * 8;
  auto trfrag = [&](int i) {
    s16x4 lo = __builtin_amdgcn_ds_read_tr16_b64_v4i16((LAS s16x4*)(trb + i * 1024));
    s16x4 hi = __builtin_amdgcn_ds_read_tr16_b64_v4i16((LAS s16x4*)(trb + i * 1024 + 128));
    s16x8 tt = {lo[0], lo[1], lo[2], lo[3], hi[0], hi[1], hi[2], hi[3]};
    return __builtin_bit_cast(b16x8, tt);
  };

  if (wave < 4) {
    b16x8 w1[3][5];
    u32x4 mf0[5];
    {
      u32x4 t1[15], t[16];
#pragma unroll
      for (int tt = 0; tt < 3; ++tt) {
        const u16* W = tt == 0 ? Wq : (tt == 1 ? Wk : Wv);
#pragma unroll
        for (int i = 0; i < 5; ++i) t1[tt * 5 + i] = *(const u32x4*)(W + (size_t)(160 * wave + 32 * i + (lane >> 1)) * HE + h * E + 16 * j + (lane & 1) * 8);
      }
#pragma unroll
      for (int kk = 0; kk < 5; ++kk) mf0[kk] = *(const u32x4*)(mem0 + (ml & 7) * D + 32 * (5 * wave + kk) + 8 * g);
      const char* kvsrc = ws + ((wave & 1) ? OFF_VX : OFF_KX) + (size_t)((wave >> 1) * H + h) * 8192 + lane * 16;
#pragma unroll
      for (int i = 0; i < 8; ++i) t[i] = *(const u32x4*)(kvsrc + i * 1024);
#pragma unroll
      for (int i = 0; i < 15; ++i) *(u32x4*)(my + i * 1024 + lane * 16) = t1[i];
      asm volatile("s_waitcnt lgkmcnt(0)" ::: "memory");
#pragma unroll
      for (int i = 0; i < 15; ++i) w1[i / 5][i % 5] = trfrag(i);
#pragma unroll
      for (int i = 0; i < 8; ++i) *(u32x4*)(KV + (wave >> 1) * 16384 + (wave & 1) * 8192 + i * 1024 + lane * 16) = t[i];
    }
    LBAR();
    const unsigned m_off = (unsigned)(ml >> 3) * 512u + (unsigned)(g * 8 + (ml & 7)) * 16u;
    int r3 = 0;
    for (int s = 0; s < NSEG; ++s, r3 = r3 == 2 ? 0 : r3 + 1) {
      TS(0);
      const int r3p = r3 == 0 ? 2 : r3 - 1;
      f32x4 ac[3];
      if (s == 0) {
#pragma unroll
        for (int u = 0; u < 3; ++u) ac[u] = z4;
#pragma unroll
        for (int kk = 0; kk < 5; ++kk) {
#pragma unroll
          for (int u = 0; u < 3; ++u) ac[u] = mfma16(w1[u][kk], asb(mf0[kk]), ac[u]);
        }
      } else {
        const unsigned base = C_MEM + (unsigned)r3 * 32768u + (unsigned)(wave >> 1) * 16384u + (unsigned)(wave & 1) * 2560u + selA;
        SG(9); slp<SLP_A>(); SG(0);
        u32x4 mf[5];
        for (unsigned spin = 0;; ++spin) {
          asm volatile("" ::: "memory");
          mf[0] = ld128(rs, base + m_off); mf[2] = ld128(rs, base + m_off + 1024u); mf[4] = ld128(rs, base + 2048u + lo_off);
          const unsigned mx = umax3(umax3(mf[0][0], mf[0][2], mf[2][0]), mf[2][2], max(mf[4][0], mf[4][2]));
          if (__all(mx != 0xFFFFFFFFu) || spin >= spin_cap) { SPN(0); if (spin >= spin_cap) spin_cap = 0; break; }
        }
        mf[1] = ror8x4(mf[0]); mf[3] = ror8x4(mf[2]);
        SG(1);
#pragma unroll
        for (int u = 0; u < 3; ++u) ac[u] = z4;
#pragma unroll
        for (int kk = 0; kk < 5; ++kk) {
#pragma unroll
          for (int u = 0; u < 3; ++u) ac[u] = mfma16(w1[u][kk], asb(mf[kk]), ac[u]);
        }
      }
      TS(1);
#pragma unroll
      for (int u = 0; u < 3; ++u) X1[u * 256 + tid] = ac[u];
      LBAR();
      SG(2);
      if (wave < 2) {
        const int tl = 2 * wave + (ml >> 3);
        const f32x4* xp = X1 + (tl < 3 ? tl : 2) * 256 + g * 16 + (ml & 7);
        f32x4 a = (xp[0] + xp[64]) + (xp[128] + xp[192]);
        u32x2 v = {nosent(pack2(a[0], a[1])), nosent(pack2(a[2], a[3]))};
        const unsigned in = (unsigned)tl * 2048u + (unsigned)(((j >> 1) * 4 + 2 * (j & 1) + (g >> 1)) * 8 + (ml & 7)) * 16u + (g & 1) * 8u;
        if (tl < 3) {
          const unsigned off = C_QKV + (unsigned)(r3 * H + h) * 16384u + in;
          st64(rs, off + selA, v); SG(3); st64(rs, off + (8192u - selA), v);
          if (tl > 0) *(u32x2*)(ws + OFF_QM + (size_t)(s * H + h) * 6144 + in) = v;
          const unsigned offp = C_QKV + (unsigned)(r3p * H + h) * 16384u + in;
          st64(rs, offp, ones2); st64(rs, offp + 8192u, ones2);
        }
      }
      TS(2);
      if (s == NSEG - 1) break;
      LBAR(); LBAR();
      SG(8);
    }
    return;
  }
  const int w4 = wave - 4;
  const bool three = w4 < 1;
  b16x8 w2[2][4];
  {
    u32x4 t2[8];
#pragma unroll
    for (int t = 0; t < 2; ++t) {
      const int ct = (t < 1 || three) ? w4 + 4 * t : 0;
#pragma unroll
      for (int i = 0; i < 4; ++i) t2[t * 4 + i] = *(const u32x4*)(Wo + (size_t)(h * E + 32 * i + (lane >> 1)) * D + 80 * j + 16 * ct + (lane & 1) * 8);
    }
#pragma unroll
    for (int i = 0; i < 8; ++i) *(u32x4*)(my + i * 1024 + lane * 16) = t2[i];
    asm volatile("s_waitcnt lgkmcnt(0)" ::: "memory");
#pragma unroll
    for (int i = 0; i < 8; ++i) w2[i / 4][i % 4] = trfrag(i);
  }
  b16x8 kx[2][4]; u32x4 vwx[2];
  const b16x8 ones8 = asb((u32x4){0x3F803F80u, 0x3F803F80u, 0x3F803F80u, 0x3F803F80u});
  auto prep = [&](int sn) {
    const char* kb = KV + (sn & 1) * 16384 + lane * 16;
    b16x8 vf[2][4];
#pragma unroll
    for (int kt = 0; kt < 2; ++kt)
#pragma unroll
      for (int ks = 0; ks < 4; ++ks) { kx[kt][ks] = *(const b16x8*)(kb + (kt * 4 + ks) * 1024); vf[kt][ks] = *(const b16x8*)(kb + 8192 + (kt * 4 + ks) * 1024); }
#pragma unroll
    for (int t = 0; t < 2; ++t) {
      f32x4 d0 = z4, d1 = z4;
#pragma unroll
      for (int ks = 0; ks < 4; ++ks) { d0 = mfma16(vf[0][ks], w2[t][ks], d0); d1 = mfma16(vf[1][ks], w2[t][ks], d1); }
      vwx[t] = (u32x4){pack2(d0[0], d0[1]), pack2(d0[2], d0[3]), pack2(d1[0], d1[1]), pack2(d1[2], d1[3])};
    }
  };
  const char* kvs = ws + (w4 < 2 ? OFF_KX : OFF_VX) + (size_t)h * 8192 + (w4 & 1) * 4096 + lane * 16;
  u32x4 stg[4];
#pragma unroll
  for (int i = 0; i < 4; ++i) stg[i] = *(const u32x4*)(kvs + (size_t)2 * H * 8192 + i * 1024);
  LBAR();
  prep(0);
  const int cc = lane & 7, hg = lane >> 3;
  unsigned poff[2];
#pragma unroll
  for (int t = 0; t < 2; ++t) {
    const int T = 5 * j + w4 + 4 * t;
    const unsigned c16 = (unsigned)(((T >> 1) * 4 + 2 * (T & 1) + (g >> 1)) * 8 + (ml & 7)) * 2u + (unsigned)(g & 1), rb = c16 / 5u;
    poff[t] = C_P + (rb / 3u) * 16384u + (rb % 3u) * 2560u + (unsigned)h * 80u + (c16 % 5u) * 16u;
  }
  const unsigned q_off = (unsigned)(ml >> 3) * 512u + (unsigned)(g * 8 + (ml & 7)) * 16u, kv_off = 2048u + (unsigned)(ml >> 3) * 2048u + (unsigned)(g * 8 + (ml & 7)) * 16u;
  int r3 = 0;
  for (int s = 0; s < NSEG - 1; ++s, r3 = r3 == 2 ? 0 : r3 + 1) {
    const unsigned hb = C_QKV + (unsigned)(r3 * H + h) * 16384u + selA;
    SG(9);
    LBAR();
    SG(0);
    slp<SLP_B>(); SG(1);
    f32x4 a[2];
    u32x4 qf[4], kv[4];
    for (unsigned spin = 0;; ++spin) {
      asm volatile("" ::: "memory");
      qf[0] = ld128(rs, hb + q_off); qf[2] = ld128(rs, hb + q_off + 1024u);
#pragma unroll
      for (int ks = 0; ks < 4; ++ks) kv[ks] = ld128(rs, hb + kv_off + ks * 512u);
      unsigned mx = umax3(umax3(qf[0][0], qf[0][2], qf[2][0]), qf[2][2], 0u);
#pragma unroll
      for (int ks = 0; ks < 4; ++ks) mx = umax3(mx, kv[ks][0], kv[ks][2]);
      if (__all(mx != 0xFFFFFFFFu) || spin >= spin_cap) { SPN(1); if (spin >= spin_cap) spin_cap = 0; break; }
    }
    qf[1] = ror8x4(qf[0]); qf[3] = ror8x4(qf[2]);
    SG(2);
    {
#define SB() __builtin_amdgcn_sched_barrier(0)
      f32x4 s0 = z4, s1 = z4, sm = z4, dm[2] = {z4, z4};
#pragma unroll
      for (int ks = 0; ks < 4; ++ks) { s0 = mfma16(kx[0][ks], asb(qf[ks]), s0); s1 = mfma16(kx[1][ks], asb(qf[ks]), s1); SB(); }
#pragma unroll
      for (int ks = 0; ks < 4; ++ks) { sm = mfma16(asb(kv[ks]), asb(qf[ks]), sm); SB(); }
      TS(3);
      const float NEG = -INFINITY;
      const float c2 = SCALE * 1.4426950408889634f;
      float tv[12];
      dm[0] = mfma16(asb(kv[0]), w2[0][0], dm[0]);
#pragma unroll
      for (int r = 0; r < 4; ++r) tv[r] = s0[r] * c2;
      SB();
      dm[1] = mfma16(asb(kv[0]), w2[1][0], dm[1]);
#pragma unroll
      for (int r = 0; r < 4; ++r) tv[4 + r] = s1[r] * c2;
      SB();
      float m1 = fmaxf(fmaxf(tv[0], tv[1]), tv[2]), m2 = fmaxf(fmaxf(tv[3], tv[4]), tv[5]);
      m1 = fmaxf(fmaxf(m1, tv[6]), tv[7]);
      SB();
      dm[0] = mfma16(asb(kv[1]), w2[0][1], dm[0]);
#pragma unroll
      for (int r = 0; r < 4; ++r) tv[8 + r] = (g < 2) ? sm[r] * c2 : NEG;
      SB();
      dm[1] = mfma16(asb(kv[1]), w2[1][1], dm[1]);
      m2 = fmaxf(fmaxf(m2, tv[8]), tv[9]); m1 = fmaxf(fmaxf(m1, tv[10]), tv[11]);
      float mxv = fmaxf(m1, m2);
      { unsigned u = __float_as_uint(mxv); auto r = __builtin_amdgcn_permlane16_swap(u, u, false, false); mxv = fmaxf(__uint_as_float(r[0]) + 0.f, __uint_as_float(r[1]) + 0.f); }
      SB();
      { unsigned u = __float_as_uint(mxv); auto r = __builtin_amdgcn_permlane32_swap(u, u, false, false); mxv = fmaxf(__uint_as_float(r[0]) + 0.f, __uint_as_float(r[1]) + 0.f); }
      SB();
      dm[0] = mfma16(asb(kv[2]), w2[0][2], dm[0]);
#pragma unroll
      for (int r = 0; r < 2; ++r) tv[r] = __builtin_amdgcn_exp2f(tv[r] - mxv);
      SB();
      dm[1] = mfma16(asb(kv[2]), w2[1][2], dm[1]);
#pragma unroll
      for (int r = 2; r < 4; ++r) tv[r] = __builtin_amdgcn_exp2f(tv[r] - mxv);
      SB();
#pragma unroll
      for (int r = 4; r < 6; ++r) tv[r] = __builtin_amdgcn_exp2f(tv[r] - mxv);
      SB();
      dm[0] = mfma16(asb(kv[3]), w2[0][3], dm[0]);
#pragma unroll
      for (int r = 6; r < 8; ++r) tv[r] = __builtin_amdgcn_exp2f(tv[r] - mxv);
      SB();
      dm[1] = mfma16(asb(kv[3]), w2[1][3], dm[1]);
#pragma unroll
      for (int r = 8; r < 10; ++r) tv[r] = __builtin_amdgcn_exp2f(tv[r] - mxv);
      SB();
#pragma unroll
      for (int r = 10; r < 12; ++r) tv[r] = __builtin_amdgcn_exp2f(tv[r] - mxv);
      u32x4 px = {pack2(tv[0], tv[1]), pack2(tv[2], tv[3]), pack2(tv[4], tv[5]), pack2(tv[6], tv[7])};
      SB();
      f32x4 sx = mfma16(ones8, asb(px), z4);
      a[0] = mfma16(asb(vwx[0]), asb(px), z4);
      a[1] = mfma16(asb(vwx[1]), asb(px), z4);
      u32x4 pm;
      { const unsigned p0 = pack2(tv[8], tv[9]), p1 = pack2(tv[10], tv[11]);
        auto r0 = __builtin_amdgcn_permlane32_swap(0u, p0, false, false); auto r1 = __builtin_amdgcn_permlane32_swap(0u, p1, false, false);
        pm = (u32x4){r0[0], r1[0], 0u, 0u}; }
      SB();
      sx = mfma16(ones8, asb(pm), sx);
#pragma unroll
      for (int t = 0; t < 2; ++t) {
        u32x4 vwm = {g >= 2 ? pack2(dm[t][0], dm[t][1]) : 0u, g >= 2 ? pack2(dm[t][2], dm[t][3]) : 0u, 0u, 0u};
        a[t] = mfma16(asb(vwm), asb(pm), a[t]);
      }
      SB();
      const float inv = __builtin_amdgcn_rcpf(sx[0]);
      a[0] *= inv; a[1] *= inv;
    }
#pragma unroll
    for (int t = 0; t < 2; ++t) {
      if (t == 1 && !three) break;
      u32x4 v;
#pragma unroll
      for (int r = 0; r < 4; ++r) v[r] = min(__float_as_uint(a[t][r]), 0xFFFFFFFEu);
      const unsigned off = (unsigned)(1 + (s & 1)) * PAGE + poff[t];
      if (ml < 8) { st128(rs, off, v); st128(rs, off + 8192u, v); }
    }
    SG(3);
    TS(4);
    LBAR();
    SG(4);
    auto kvwrite = [&] {
      if (s + 2 < NSEG - 1) {
#pragma unroll
        for (int i = 0; i < 4; ++i) *(u32x4*)(KV + (s & 1) * 16384 + w4 * 4096 + i * 1024 + lane * 16) = stg[i];
      }
    };
    if (w4 == 3) {
      const int r3n = r3 == 2 ? 0 : r3 + 1;
      kvwrite();
        const unsigned offw = (unsigned)(1 + (s & 1)) * PAGE + C_P + (unsigned)(cb / 3) * 16384u + (unsigned)(cb % 3) * 2560u + (unsigned)hg * 320u + (unsigned)cc * 16u;
        const unsigned off = cc < 5 ? offw + ((s & 1) ? selP1 : selP0) : OOB;
        slp<SLP_C>(); SG(5);
        u32x4 v[4];
        for (unsigned spin = 0;; ++spin) {
          asm volatile("" ::: "memory");
#pragma unroll
          for (int i = 0; i < 4; ++i) v[i] = ld128(rs, off + i * 80u);
          unsigned mx = 0;
#pragma unroll
          for (int i = 0; i < 4; ++i) { mx = umax3(mx, v[i][0], v[i][1]); mx = umax3(mx, v[i][2], v[i][3]); }
          if (__all(mx != 0xFFFFFFFFu) || spin >= spin_cap) { SPN(2); if (spin >= spin_cap) spin_cap = 0; break; }
        }
        SG(6);
        TS(5);
        float f[4];
#pragma unroll
        for (int r = 0; r < 4; ++r) {
          float x = (__uint_as_float(v[0][r]) + __uint_as_float(v[1][r])) + (__uint_as_float(v[2][r]) + __uint_as_float(v[3][r]));
          x += ror8(x); x = xsum32(xsum16(x)); f[r] = x;
        }
        u32x2 w = {nosent(pack2(f[0], f[1])), nosent(pack2(f[2], f[3]))};
        const unsigned mq = (unsigned)cb >> 6, min_ = (mq >> 1) * 16384u + (mq & 1) * 2560u + (unsigned)(5 * (cb & 63) + lane) * 8u;
        if (lane < 5) { st64(rs, C_MEM + (unsigned)r3n * 32768u + min_, w); st64(rs, C_MEM + (unsigned)r3n * 32768u + 8192u + min_, w); }
        SG(7);
        TS(6);
        LBAR();
        if (cc < 5) {
#pragma unroll
          for (int i = 0; i < 4; ++i) { st128(rs, offw + i * 80u, ones4); st128(rs, offw + 8192u + i * 80u, ones4); }
        }
        if (lane < 5) { st64(rs, C_MEM + (unsigned)r3 * 32768u + min_, ones2); st64(rs, C_MEM + (unsigned)r3 * 32768u + 8192u + min_, ones2); }
      if (s + 1 < NSEG - 1) prep(s + 1);
    } else {
      if (s + 1 < NSEG - 1) prep(s + 1);
      kvwrite();
      LBAR();
    }
    if (s + 3 < NSEG - 1) {
#pragma unroll
      for (int i = 0; i < 4; ++i) stg[i] = *(const u32x4*)(kvs + (size_t)(s + 3) * H * 8192 + i * 1024);
    }
  }
  LBAR();
}

__global__ __launch_bounds__(512, 1) void k_chain(const u16* __restrict__ mem0, const u16* __restrict__ Wq, const u16* __restrict__ Wk,
                                                   const u16* __restrict__ Wv, const u16* __restrict__ Wo, char* __restrict__ ws) {
  extern __shared__ __attribute__((aligned(16))) char dyn_lds[];
  chain_role(blockIdx.x, mem0, Wq, Wk, Wv, Wo, ws, dyn_lds);
}

DI void attn_unit(const int s, const int h, const b16x8 (&qf)[2][4], char* __restrict__ ws, char* V) {
  const int lane = threadIdx.x & 63, ml = lane & 15, g = lane >> 4;
  const char* KX = ws + OFF_KX + (size_t)(s * H + h) * 8192 + lane * 16;
  const char* VX = ws + OFF_VX + (size_t)(s * H + h) * 8192 + lane * 16;
  const char* KM = ws + OFF_QM + (size_t)(s * H + h) * 6144 + 2048 + (g * 8 + (ml & 7)) * 16;
  b16x8 kx[2][4], km[4];
#pragma unroll
  for (int ks = 0; ks < 4; ++ks) {
#pragma unroll
    for (int t = 0; t < 2; ++t) {
      kx[t][ks] = *(const b16x8*)(KX + (t * 4 + ks) * 1024);
      *(u32x4*)(V + (16 * t + ml) * 272 + (32 * ks + 8 * g) * 2) = *(const u32x4*)(VX + (t * 4 + ks) * 1024);
    }
    km[ks] = *(const b16x8*)(KM + ks * 512);
    u32x4 vmr = *(const u32x4*)(KM + 2048 + ks * 512);
    if (ml >= 8) vmr = (u32x4){0, 0, 0, 0};
    *(u32x4*)(V + (32 + ml) * 272 + (32 * ks + 8 * g) * 2) = vmr;
  }
  u32x4 px[2], pm[2];
#pragma unroll
  for (int qt = 0; qt < 2; ++qt) {
    f32x4 s0 = {0, 0, 0, 0}, s1 = {0, 0, 0, 0}, sm = {0, 0, 0, 0};
#pragma unroll
    for (int ks = 0; ks < 4; ++ks) {
      s0 = mfma16(kx[0][ks], qf[qt][ks], s0);
      s1 = mfma16(kx[1][ks], qf[qt][ks], s1);
      sm = mfma16(km[ks], qf[qt][ks], sm);
    }
    const int qi = 16 * qt + ml;
    float sv[12];
#pragma unroll
    for (int r = 0; r < 4; ++r) {
      sv[r] = (4 * g + r <= qi) ? rbf(rbf(s0[r]) * SCALE) : -INFINITY;
      sv[4 + r] = (16 + 4 * g + r <= qi) ? rbf(rbf(s1[r]) * SCALE) : -INFINITY;
      sv[8 + r] = (g < 2) ? rbf(rbf(sm[r]) * SCALE) : -INFINITY;
    }
    float mxv = sv[0];
#pragma unroll
    for (int r = 1; r < 12; ++r) mxv = fmaxf(mxv, sv[r]);
    mxv = xmax32(xmax16(mxv));
    float sum = 0.f;
#pragma unroll
    for (int r = 0; r < 12; ++r) { sv[r] = __expf(sv[r] - mxv); sum += sv[r]; }
    sum = xsum32(xsum16(sum));
    const float inv = 1.f / sum;
    px[qt] = (u32x4){pack2(sv[0] * inv, sv[1] * inv), pack2(sv[2] * inv, sv[3] * inv), pack2(sv[4] * inv, sv[5] * inv), pack2(sv[6] * inv, sv[7] * inv)};
    pm[qt] = (u32x4){pack2(sv[8] * inv, sv[9] * inv), pack2(sv[10] * inv, sv[11] * inv), 0u, 0u};
  }
  asm volatile("s_waitcnt lgkmcnt(0)" ::: "memory");
  const unsigned vb = (unsigned)(size_t)V + (4 * g + (ml >> 2)) * 272 + (lane & 3) * 8;
#pragma unroll
  for (int kk = 0; kk < 4; ++kk) {
    u32x4 of[2];
#pragma unroll
    for (int hf = 0; hf < 2; ++hf) {
      const int et = 2 * kk + hf;
      s16x4 v0 = __builtin_amdgcn_ds_read_tr16_b64_v4i16((LAS s16x4*)(vb + et * 32));
      s16x4 v1 = __builtin_amdgcn_ds_read_tr16_b64_v4i16((LAS s16x4*)(vb + 16 * 272 + et * 32));
      s16x4 v2 = __builtin_amdgcn_ds_read_tr16_b64_v4i16((LAS s16x4*)(vb + 32 * 272 + et * 32));
      typedef short s16x8 __attribute__((ext_vector_type(8)));
      s16x8 va = {v0[0], v0[1], v0[2], v0[3], v1[0], v1[1], v1[2], v1[3]};
      s16x8 vbm = {v2[0], v2[1], v2[2], v2[3], 0, 0, 0, 0};
#pragma unroll
      for (int qt = 0; qt < 2; ++qt) {
        f32x4 o = {0, 0, 0, 0};
        o = mfma16(__builtin_bit_cast(b16x8, va), asb(px[qt]), o);
        o = mfma16(__builtin_bit_cast(b16x8, vbm), asb(pm[qt]), o);
        of[qt][2 * hf] = pack2(o[0], o[1]); of[qt][2 * hf + 1] = pack2(o[2], o[3]);
      }
    }
#pragma unroll
    for (int qt = 0; qt < 2; ++qt) *(u32x4*)(ws + OFF_O + (size_t)((s * 2 + qt) * 128 + h * 4 + kk) * 1024 + lane * 16) = of[qt];
  }
  asm volatile("s_waitcnt lgkmcnt(0)" ::: "memory");
}

__global__ __launch_bounds__(512, 1) void k_post1(const u16* __restrict__ x, const u16* __restrict__ Wq, const u16* __restrict__ wg,
                                                  const u16* __restrict__ wu, const u16* __restrict__ Wo, const u16* __restrict__ wd,
                                                  const u16* __restrict__ gate_w, char* __restrict__ ws) {
  extern __shared__ __attribute__((aligned(16))) char lds[];
  const int b = blockIdx.x, xc = b & 7, idx = b >> 3;
  if (idx >= 30) {
    const int lane = threadIdx.x & 63, wave = threadIdx.x >> 6, ml = lane & 15, g = lane >> 4;
    const int gw = (b - 240) * 8 + wave;
    for (int t = gw; t < S; t += 128) {
      float s = 0.f;
#pragma unroll
      for (int i = 0; i < 5; ++i) {
        const unsigned xv = *(const unsigned*)(x + (size_t)t * D + i * 128 + lane * 2), gv = *(const unsigned*)(gate_w + i * 128 + lane * 2);
        s += rbf(__uint_as_float(xv << 16) * __uint_as_float(gv << 16)) + rbf(__uint_as_float(xv & 0xFFFF0000u) * __uint_as_float(gv & 0xFFFF0000u));
      }
#pragma unroll
      for (int o = 32; o >= 1; o >>= 1) s += __shfl_xor(s, o);
      s = rbf(s);
      float gg = rbf(1.f / (1.f + __expf(-s)));
      if (lane == 0) ((float*)(ws + OFF_G))[t] = gg;
    }
    char* my = lds + wave * 8192;
    const unsigned trb = (unsigned)(size_t)my + (4 * g + (ml >> 2)) * 32 + (lane & 3) * 8;
    for (int u = gw; u < 182 * 5; u += 128) {
      const int kk = u / 5, c0 = (u % 5) * 8;
      const u16* W = kk < 128 ? Wo + (size_t)32 * kk * D : wd + (size_t)32 * (kk - 128) * D;
      u32x4 t8[8];
#pragma unroll
      for (int i = 0; i < 8; ++i) t8[i] = *(const u32x4*)(W + (size_t)(4 * i + (lane >> 4)) * D + 16 * c0 + (lane & 15) * 8);
#pragma unroll
      for (int i = 0; i < 8; ++i) *(u32x4*)(my + ((lane & 15) >> 1) * 1024 + (4 * i + (lane >> 4)) * 32 + (lane & 1) * 16) = t8[i];
      asm volatile("s_waitcnt lgkmcnt(0)" ::: "memory");
#pragma unroll
      for (int i = 0; i < 8; ++i) {
        s16x4 lo = __builtin_amdgcn_ds_read_tr16_b64_v4i16((LAS s16x4*)(trb + i * 1024));
        s16x4 hi = __builtin_amdgcn_ds_read_tr16_b64_v4i16((LAS s16x4*)(trb + i * 1024 + 512));
        typedef short s16x8 __attribute__((ext_vector_type(8)));
        s16x8 tt = {lo[0], lo[1], lo[2], lo[3], hi[0], hi[1], hi[2], hi[3]};
        *(s16x8*)(ws + OFF_WF + ((size_t)(c0 + i) * 182 + kk) * 1024 + lane * 16) = tt;
      }
      asm volatile("s_waitcnt lgkmcnt(0)" ::: "memory");
    }
    return;
  }
  f32x4 acc[8][4];
#pragma unroll
  for (int i = 0; i < 8; ++i)
#pragma unroll
    for (int j = 0; j < 4; ++j) acc[i][j] = (f32x4){0, 0, 0, 0};
  if (idx < 16) {
    const int nt = xc * 2 + (idx >> 3), mt = idx & 7, n0 = nt * 256, m0 = mt * 256;
    gemm256<0>(x, D, Wq, nullptr, HE, HE, D, m0, n0, lds, acc);
    int tz = threadIdx.x; asm volatile("" : "+v"(tz));
    const int wr = tz >> 8, wc = (tz >> 6) & 3, fr = tz & 15, g = (tz >> 4) & 3, lane = tz & 63, wave = __builtin_amdgcn_readfirstlane(tz >> 6);
#pragma unroll
    for (int i = 0; i < 8; ++i)
#pragma unroll
      for (int j = 0; j < 4; ++j) {
        u32x2 v = {pack2(acc[i][j][0], acc[i][j][1]), pack2(acc[i][j][2], acc[i][j][3])};
        const int tok = wr * 128 + i * 16 + fr, n = wc * 64 + j * 16 + 4 * g;
        const int seg = tok >> 5, kt = (tok >> 4) & 1, hh = n >> 7, e = n & 127;
        *(u32x2*)(lds + (seg * 2 + hh) * 8192 + (((kt * 4 + (e >> 5)) * 64 + (tok & 15) + 16 * ((e & 31) >> 3)) * 16 + (e & 4) * 2)) = v;
      }
    RBAR();
    b16x8 q0[2][4], q1[2][4];
#pragma unroll
    for (int t = 0; t < 2; ++t)
#pragma unroll
      for (int ks = 0; ks < 4; ++ks) {
        q0[t][ks] = *(const b16x8*)(lds + (wave * 2) * 8192 + (t * 4 + ks) * 1024 + lane * 16);
        q1[t][ks] = *(const b16x8*)(lds + (wave * 2 + 1) * 8192 + (t * 4 + ks) * 1024 + lane * 16);
      }
    RBAR();
    char* V = lds + wave * (48 * 272);
    attn_unit(mt * 8 + wave, nt * 2, q0, ws, V);
    attn_unit(mt * 8 + wave, nt * 2 + 1, q1, ws, V);
  } else {
    const int tile = xc * 14 + (idx - 16), ft = tile >> 3, mt = tile & 7, f0 = ft * 128, m0 = mt * 256;
    gemm256<1>(x, D, wg, wu, F, F, D, m0, f0, lds, acc);
    int tz = threadIdx.x; asm volatile("" : "+v"(tz));
    const int wr = tz >> 8, wc = (tz >> 6) & 3, ln = tz & 63;
    if (f0 + wc * 32 < F) {
      const int kk = (f0 + wc * 32) >> 5;
#pragma unroll
      for (int i = 0; i < 8; ++i) {
        u32x4 v;
#pragma unroll
        for (int j = 0; j < 2; ++j) {
          float hv[4];
#pragma unroll
          for (int r = 0; r < 4; ++r) {
            float gg = rbf(acc[i][j][r]), uu = rbf(acc[i][j + 2][r]);
            hv[r] = rbf(gelu_tanh(gg)) * uu;
          }
          v[2 * j] = pack2(hv[0], hv[1]); v[2 * j + 1] = pack2(hv[2], hv[3]);
        }
        const int t16 = (m0 + wr * 128 + i * 16) >> 4;
        *(u32x4*)(ws + OFF_HH + (size_t)(t16 * 54 + kk) * 1024 + ln * 16) = v;
      }
    }
  }
}

__global__ __launch_bounds__(512, 1) void k_post2(const char* __restrict__ ws, u16* __restrict__ out) {
  extern __shared__ __attribute__((aligned(16))) char lds[];
  const int tid = threadIdx.x, lane = tid & 63, wave = __builtin_amdgcn_readfirstlane(tid >> 6), ml = lane & 15, g = lane >> 4;
  const int b = blockIdx.x, xc = b & 7, idx = b >> 3, tt = xc * 4 + (idx >> 3), cgp = idx & 7;
  f32x4* X = (f32x4*)lds;
  f32x4 asum[2][2];
#pragma unroll
  for (int ph = 0; ph < 2; ++ph) {
    const int kt = ph == 0 ? 128 : 54, nk = ph == 0 ? 16 : (wave < 6 ? 7 : 6);
    const char* tb = ws + (ph ? OFF_HH : OFF_O) + (size_t)(tt * 4) * kt * 1024 + (size_t)wave * 1024 + lane * 16;
    const size_t tstr = (size_t)kt * 1024;
    const char* wb = ws + OFF_WF + ((size_t)(cgp * 5) * 182 + (ph ? 128 : 0) + wave) * 1024 + lane * 16;
    f32x4 acc[4][5];
#pragma unroll
    for (int i = 0; i < 4; ++i)
#pragma unroll
      for (int c = 0; c < 5; ++c) acc[i][c] = (f32x4){0, 0, 0, 0};
    b16x8 tf[4][4], wf[4][5];
    auto ldk = [&](int slot, int k) {
      const int kn = k < nk ? k : nk - 1;
#pragma unroll
      for (int i = 0; i < 4; ++i) tf[slot][i] = *(const b16x8*)(tb + i * tstr + (size_t)kn * 8192);
#pragma unroll
      for (int c = 0; c < 5; ++c) wf[slot][c] = *(const b16x8*)(wb + (size_t)c * 182 * 1024 + (size_t)kn * 8192);
    };
    ldk(0, 0); ldk(1, 1); ldk(2, 2);
    for (int k = 0; k < nk; k += 4) {
#pragma unroll
      for (int u = 0; u < 4; ++u) {
        ldk((u + 3) & 3, k + u + 3);
        if (k + u < nk) {
#pragma unroll
          for (int i = 0; i < 4; ++i)
#pragma unroll
            for (int c = 0; c < 5; ++c) acc[i][c] = mfma16(wf[u][c], tf[u][i], acc[i][c]);
        }
      }
    }
#pragma unroll
    for (int rd = 0; rd < 2; ++rd) {
#pragma unroll
      for (int t = 0; t < 10; ++t) { const int tl = rd * 10 + t; X[(wave * 10 + t) * 64 + lane] = acc[tl / 5][tl % 5]; }
      RBAR();
#pragma unroll
      for (int u = 0; u < 2; ++u) {
        const int t = wave + 8 * u;
        if (t < 10) {
          const f32x4* xp = X + t * 64 + lane;
          const f32x4 v = ((xp[0] + xp[640]) + (xp[1280] + xp[1920])) + ((xp[2560] + xp[3200]) + (xp[3840] + xp[4480]));
          if (ph == 0) asum[rd][u] = v;
          else {
            const int tl = rd * 10 + t, i = tl / 5, c = tl % 5;
            const int tok = tt * 64 + 16 * i + ml;
            const float gg = ((const float*)(ws + OFF_G))[tok], og = rbf(1.f - gg);
            float r[4];
#pragma unroll
            for (int q = 0; q < 4; ++q) { float ta = rbf(gg * rbf(asum[rd][u][q])), tb2 = rbf(og * rbf(v[q])); asm volatile("" : "+v"(ta), "+v"(tb2)); r[q] = ta + tb2; }
            *(u32x2*)(out + (size_t)tok * D + 80 * cgp + 16 * c + 4 * g) = (u32x2){pack2(r[0], r[1]), pack2(r[2], r[3])};
          }
        }
      }
      RBAR();
    }
  }
}

extern "C" void kernel_launch(void* const* d_in, const int* in_sizes, int n_in, void* d_out, int out_size, void* d_ws, size_t ws_size,
                              hipStream_t stream) {
  const u16* x = (const u16*)d_in[0];
  const u16* mem0 = (const u16*)d_in[1];
  const u16* Wq = (const u16*)d_in[2];
  const u16* Wk = (const u16*)d_in[3];
  const u16* Wv = (const u16*)d_in[4];
  const u16* Wo = (const u16*)d_in[5];
  const u16* wg = (const u16*)d_in[8];
  const u16* wu = (const u16*)d_in[9];
  const u16* wd = (const u16*)d_in[10];
  const u16* gw = (const u16*)d_in[11];
  char* ws = (char*)d_ws;
  hipFuncSetAttribute((const void*)k_pre, hipFuncAttributeMaxDynamicSharedMemorySize, 128 * 1024);
  k_pre<<<256, 512, 128 * 1024, stream>>>(x, Wk, Wv, Wq, Wo, ws);
  hipFuncSetAttribute((const void*)k_chain, hipFuncAttributeMaxDynamicSharedMemorySize, 156 * 1024);
  k_chain<<<256, 512, 156 * 1024, stream>>>(mem0, Wq, Wk, Wv, Wo, ws);
  hipFuncSetAttribute((const void*)k_post1, hipFuncAttributeMaxDynamicSharedMemorySize, 128 * 1024);
  k_post1<<<256, 512, 128 * 1024, stream>>>(x, Wq, wg, wu, Wo, wd, gw, ws);
  hipFuncSetAttribute((const void*)k_post2, hipFuncAttributeMaxDynamicSharedMemorySize, 80 * 1024);
  k_post2<<<256, 512, 80 * 1024, stream>>>(ws, (u16*)d_out);
}
```

```cpp
#include <hip/hip_runtime.h>
#include <hip/hip_bf16.h>
#include <stdint.h>
#include <stdio.h>

typedef unsigned short u16;
typedef __bf16 b16x8 __attribute__((ext_vector_type(8)));
typedef short s16x4 __attribute__((ext_vector_type(4)));
typedef float f32x4 __attribute__((ext_vector_type(4)));
typedef unsigned u32x4 __attribute__((ext_vector_type(4)));
typedef unsigned u32x2 __attribute__((ext_vector_type(2)));
#define LAS __attribute__((address_space(3)))
#define DI __device__ __forceinline__

constexpr int S = 2048, D = 640, H = 32, E = 128, HE = 4096, MM = 8, SEG = 32, NSEG = 64, F = 1728;
constexpr float SCALE = 0.08838834764831845f;

constexpr size_t OFF_KX = 0;
constexpr size_t OFF_VX = OFF_KX + (size_t)S * HE * 2;
constexpr size_t OFF_QX = OFF_VX + (size_t)S * HE * 2;
constexpr size_t OFF_O  = OFF_QX + (size_t)S * HE * 2;
constexpr size_t OFF_HH = OFF_O + (size_t)S * HE * 2;
constexpr size_t OFF_WF = OFF_HH + (size_t)S * F * 2;
constexpr size_t OFF_XF = OFF_WF + (size_t)40 * 182 * 1024;
constexpr size_t OFF_G  = OFF_XF + (size_t)S * D * 2;
constexpr size_t OFF_QM = OFF_G + (size_t)S * 4;
constexpr size_t OFF_COMM = OFF_QM + (size_t)NSEG * H * 6144;
constexpr size_t OFF_END = OFF_COMM + (size_t)8 * 1048576;
constexpr size_t OFF_DBG = OFF_END;
constexpr unsigned PAGE = 2097152u, C_MEM = 65536u, C_TOK = 180224u, C_QKV = 196608u, C_P = 65536u;
__host__ __device__ inline char* comm_base(char* ws) { return (char*)(((size_t)(ws + OFF_COMM) + (PAGE - 1)) & ~(size_t)(PAGE - 1)); }

DI float bf2f(u16 v) { return __builtin_bit_cast(float, (unsigned)v << 16); }
DI u16 f2bf(float f) { return __builtin_bit_cast(u16, (__bf16)f); }
DI float rbf(float f) { return (float)(__bf16)f; }
typedef float f32x2 __attribute__((ext_vector_type(2)));
typedef __bf16 b16x2 __attribute__((ext_vector_type(2)));
DI unsigned pack2(float lo, float hi) { f32x2 v = {lo, hi}; return __builtin_bit_cast(unsigned, __builtin_convertvector(v, b16x2)); }
DI f32x4 mfma16(b16x8 a, b16x8 b, f32x4 c) { return __builtin_amdgcn_mfma_f32_16x16x32_bf16(a, b, c, 0, 0, 0); }
DI b16x8 asb(u32x4 v) { return __builtin_bit_cast(b16x8, v); }

DI float gelu_tanh(float x) {
  float y = 0.7978845608028654f * (x + 0.044715f * x * x * x);
  float t = 1.f - 2.f / (__expf(2.f * y) + 1.f);
  return 0.5f * x * (1.f + t);
}

#define RBAR() asm volatile("s_waitcnt lgkmcnt(0)\n\ts_barrier" ::: "memory")

__global__ __launch_bounds__(256) void k_init(const u16* __restrict__ x, char* __restrict__ ws) {
  const unsigned i = blockIdx.x * 256 + threadIdx.x;
  constexpr unsigned NX = S * D / 8, NF = (2 * PAGE + C_P + 86 * 16384) / 16;
  if (i < NX) {
    const unsigned lane = i & 63, f = i >> 6, kk = f % 20, t16 = f / 20;
    *(u32x4*)(ws + OFF_XF + (size_t)i * 16) = *(const u32x4*)(x + (size_t)(16 * t16 + (lane & 15)) * D + 32 * kk + 8 * (lane >> 4));
  } else if (i - NX < NF) *(u32x4*)(comm_base(ws) + (size_t)(i - NX) * 16) = (u32x4){0xFFFFFFFFu, 0xFFFFFFFFu, 0xFFFFFFFFu, 0xFFFFFFFFu};
}

#define SC1 16
DI u32x4 ld128(__amdgpu_buffer_rsrc_t rs, unsigned off) { return __builtin_amdgcn_raw_buffer_load_b128(rs, off, 0, SC1); }
DI u32x2 ld64(__amdgpu_buffer_rsrc_t rs, unsigned off) { return __builtin_amdgcn_raw_buffer_load_b64(rs, off, 0, SC1); }
DI void st128(__amdgpu_buffer_rsrc_t rs, unsigned off, u32x4 v) { __builtin_amdgcn_raw_buffer_store_b128(v, rs, off, 0, SC1); }
DI void st64(__amdgpu_buffer_rsrc_t rs, unsigned off, u32x2 v) { __builtin_amdgcn_raw_buffer_store_b64(v, rs, off, 0, SC1); }
DI unsigned nosent(unsigned v) { return v == 0xFFFFFFFFu ? 0xFFFEFFFFu : v; }
DI unsigned umax3(unsigned a, unsigned b, unsigned c) { return max(max(a, b), c); }
DI float xsum16(float x) { unsigned u = __float_as_uint(x); auto r = __builtin_amdgcn_permlane16_swap(u, u, false, false); return __uint_as_float(r[0]) + __uint_as_float(r[1]); }
DI float xsum32(float x) { unsigned u = __float_as_uint(x); auto r = __builtin_amdgcn_permlane32_swap(u, u, false, false); return __uint_as_float(r[0]) + __uint_as_float(r[1]); }
DI float xmax16(float x) { unsigned u = __float_as_uint(x); auto r = __builtin_amdgcn_permlane16_swap(u, u, false, false); return fmaxf(__uint_as_float(r[0]), __uint_as_float(r[1])); }
DI float xmax32(float x) { unsigned u = __float_as_uint(x); auto r = __builtin_amdgcn_permlane32_swap(u, u, false, false); return fmaxf(__uint_as_float(r[0]), __uint_as_float(r[1])); }
DI float ror4(float x) { return __uint_as_float(__builtin_amdgcn_update_dpp(0u, __float_as_uint(x), 0x124, 0xf, 0xf, false)); }
DI float ror8(float x) { return __uint_as_float(__builtin_amdgcn_update_dpp(0u, __float_as_uint(x), 0x128, 0xf, 0xf, false)); }
DI u32x4 ror8x4(u32x4 v) { u32x4 r; for (int i = 0; i < 4; ++i) r[i] = __builtin_amdgcn_update_dpp(0u, v[i], 0x128, 0xf, 0xf, false); return r; }
template <int N> struct IC { static constexpr int value = N; };
#define LBAR() asm volatile("s_waitcnt lgkmcnt(0)\n\ts_barrier" ::: "memory")
template <int N> DI void slp() { if constexpr (N > 0) { __builtin_amdgcn_s_sleep(N > 15 ? 15 : N); if constexpr (N > 15) slp<N - 15>(); } }

#define SG(i)
#define TS(i)
#define SPN(k)
constexpr unsigned SPIN_CAP = 1u << 17;
#define SLP_A 15
#define SLP_B 13
#define SLP_B3 4
#define XK1 5
#define XK2 5
#define SLP_X 20
#define SLP_C 10

DI void chain_role(const int cb, const u16* __restrict__ mem0, const u16* __restrict__ Wq, const u16* __restrict__ Wk,
                   const u16* __restrict__ Wv, const u16* __restrict__ Wo, const u16* __restrict__ wg, const u16* __restrict__ wu,
                   char* __restrict__ ws, char* dyn_lds) {
  const int xcd = cb & 7, y = cb >> 3, h = xcd * 4 + (y >> 3), j = y & 7;
  const int tid = threadIdx.x, lane = tid & 63, wave = __builtin_amdgcn_readfirstlane(tid >> 6);
  const int ml = lane & 15, g = lane >> 4;
  const __amdgpu_buffer_rsrc_t rs = __builtin_amdgcn_make_buffer_rsrc(comm_base(ws), 0, (int)(3 * PAGE), 0x00020000);
  unsigned selA, selP0, selP1;
  {
    unsigned* pl = (unsigned*)(dyn_lds + 156 * 1024 - 64);
    if (wave == 0) {
      {
        const unsigned v = __builtin_amdgcn_raw_buffer_load_b32(rs, (unsigned)(lane < 3 ? lane : 0) * PAGE + (PAGE - 4096u), 0, SC1);
        asm volatile("s_waitcnt vmcnt(0)" :: "v"(v) : "memory");
      }
#pragma unroll 1
      for (int p = 0; p < 4; ++p) {
        const unsigned po = (unsigned)(p < 2 ? 0 : p - 1) * PAGE + (unsigned)(cb >> 6) * 16384u + (unsigned)(p == 1) * 8192u + (unsigned)(cb & 63) * 128u;
        asm volatile("s_waitcnt vmcnt(0) lgkmcnt(0)" ::: "memory");
        const unsigned long long t0 = __builtin_amdgcn_s_memtime();
        asm volatile("s_waitcnt lgkmcnt(0)" ::: "memory");
        const unsigned v = __builtin_amdgcn_raw_buffer_load_b32(rs, po, 0, SC1);
        asm volatile("s_waitcnt vmcnt(0)" :: "v"(v) : "memory");
        const unsigned long long t1 = __builtin_amdgcn_s_memtime();
        if (lane == 0) pl[p] = (unsigned)(t1 - t0);
      }
    }
    LBAR();
    selA = __builtin_amdgcn_readfirstlane(pl[1] < pl[0] ? 8192u : 0u);
    selP0 = __builtin_amdgcn_readfirstlane(2 * pl[2] > pl[0] + pl[1] ? 8192u : 0u);
    selP1 = __builtin_amdgcn_readfirstlane(2 * pl[3] > pl[0] + pl[1] ? 8192u : 0u);
  }
  const u32x4 ones4 = {0xFFFFFFFFu, 0xFFFFFFFFu, 0xFFFFFFFFu, 0xFFFFFFFFu};
  const u32x2 ones2 = {0xFFFFFFFFu, 0xFFFFFFFFu};
  f32x4* X1 = (f32x4*)dyn_lds;
  f32x4* X2 = (f32x4*)(dyn_lds + 57344);
  const __amdgpu_buffer_rsrc_t rw = __builtin_amdgcn_make_buffer_rsrc(ws, 0, (int)OFF_O, 0x00020000);
  char* KV = dyn_lds + 24576;
  char* WL = dyn_lds + 57344;
  const f32x4 z4 = {0, 0, 0, 0};
  const unsigned OOB = 0x7FFFFF00u;
  const unsigned lo_off = ml < 8 ? (unsigned)(g * 8 + ml) * 16u : OOB;
  unsigned spin_cap = SPIN_CAP;
  typedef short s16x8 __attribute__((ext_vector_type(8)));
  char* my = WL + (wave < 4 ? wave * 15360 : 61440 + (wave - 4) * 8192);
  const unsigned trb = (unsigned)(size_t)my + (8 * g + (ml >> 2)) * 32 + (lane & 3) * 8;
  auto trfrag = [&](int i) {
    s16x4 lo = __builtin_amdgcn_ds_read_tr16_b64_v4i16((LAS s16x4*)(trb + i * 1024));
    s16x4 hi = __builtin_amdgcn_ds_read_tr16_b64_v4i16((LAS s16x4*)(trb + i * 1024 + 128));
    s16x8 tt = {lo[0], lo[1], lo[2], lo[3], hi[0], hi[1], hi[2], hi[3]};
    return __builtin_bit_cast(b16x8, tt);
  };

  if (wave < 4) {
    b16x8 w1[3][5], wgl[2][5];
    const bool glu = cb < 108;
    u32x4 mf0[5];
    u32x4 xf[2][5];
    auto xloadp = [&](int t, auto k0c, auto k1c) {
      constexpr int k0 = decltype(k0c)::value, k1 = decltype(k1c)::value;
#pragma unroll
      for (int qt = 0; qt < 2; ++qt)
#pragma unroll
        for (int kk = k0; kk < k1; ++kk) xf[qt][kk] = *(const u32x4*)(ws + OFF_XF + (size_t)((2 * t + qt) * 20 + 5 * wave + kk) * 1024 + lane * 16);
    };
    auto xload = [&](int t) { xloadp(t, IC<0>{}, IC<5>{}); };
    auto xmma = [&](int pb) {
#pragma unroll
      for (int u = 0; u < 3; ++u)
#pragma unroll
        for (int qt = 0; qt < 2; ++qt) {
          f32x4 a = z4;
#pragma unroll
          for (int kk = 0; kk < 5; ++kk) a = mfma16(w1[u][kk], asb(xf[qt][kk]), a);
          X2[pb * 2560 + (u * 2 + qt) * 256 + tid] = a;
        }
      if (glu) {
#pragma unroll
        for (int u = 0; u < 2; ++u)
#pragma unroll
          for (int qt = 0; qt < 2; ++qt) {
            f32x4 a = z4;
#pragma unroll
            for (int kk = 0; kk < 5; ++kk) a = mfma16(wgl[u][kk], asb(xf[qt][kk]), a);
            X2[pb * 2560 + (6 + u * 2 + qt) * 256 + tid] = a;
          }
      }
    };
    auto xstore = [&](int t, int pb) {
      if (wave < 3) {
        const unsigned ob = (wave == 0 ? (unsigned)OFF_QX : (wave == 1 ? (unsigned)OFF_KX : (unsigned)OFF_VX)) + (unsigned)(t * H + h) * 8192u;
#pragma unroll
        for (int qt = 0; qt < 2; ++qt) {
          const f32x4* xp = X2 + pb * 2560 + (wave * 2 + qt) * 256 + lane;
          const f32x4 a = (xp[0] + xp[64]) + (xp[128] + xp[192]);
          const unsigned oo = ob + (unsigned)(((qt * 4 + (j >> 1)) * 64 + ml + 16 * (2 * (j & 1) + (g >> 1))) * 16 + (g & 1) * 8);
          const u32x2 vv = {pack2(a[0], a[1]), pack2(a[2], a[3])};
          st64(rw, oo, vv);
        }
      }
      if (glu && wave < 2) {
        const f32x4* xp = X2 + pb * 2560 + (6 + wave) * 256 + lane;
        const f32x4 ga = (xp[0] + xp[64]) + (xp[128] + xp[192]), ua = (xp[512] + xp[576]) + (xp[640] + xp[704]);
        float hv[4];
#pragma unroll
        for (int r = 0; r < 4; ++r) hv[r] = rbf(gelu_tanh(rbf(ga[r]))) * rbf(ua[r]);
        *(u32x2*)(ws + OFF_HH + (size_t)((2 * t + wave) * 54 + (cb >> 1)) * 1024 + lane * 16 + (cb & 1) * 8) = (u32x2){pack2(hv[0], hv[1]), pack2(hv[2], hv[3])};
      }
    };
    {
      u32x4 t1[15], tg[10];
#pragma unroll
      for (int i = 0; i < 10; ++i) tg[i] = *(const u32x4*)((i < 5 ? wg : wu) + (size_t)(160 * wave + 32 * (i % 5) + (lane >> 1)) * F + 16 * (glu ? cb : 0) + (lane & 1) * 8);
#pragma unroll
      for (int tt = 0; tt < 3; ++tt) {
        const u16* W = tt == 0 ? Wq : (tt == 1 ? Wk : Wv);
#pragma unroll
        for (int i = 0; i < 5; ++i) t1[tt * 5 + i] = *(const u32x4*)(W + (size_t)(160 * wave + 32 * i + (lane >> 1)) * HE + h * E + 16 * j + (lane & 1) * 8);
      }
#pragma unroll
      for (int kk = 0; kk < 5; ++kk) mf0[kk] = *(const u32x4*)(mem0 + (ml & 7) * D + 32 * (5 * wave + kk) + 8 * g);
      xload(0);
#pragma unroll
      for (int i = 0; i < 15; ++i) *(u32x4*)(my + i * 1024 + lane * 16) = t1[i];
      asm volatile("s_waitcnt lgkmcnt(0)" ::: "memory");
#pragma unroll
      for (int i = 0; i < 15; ++i) w1[i / 5][i % 5] = trfrag(i);
      asm volatile("s_waitcnt lgkmcnt(0)" ::: "memory");
#pragma unroll
      for (int i = 0; i < 10; ++i) *(u32x4*)(my + i * 1024 + lane * 16) = tg[i];
      asm volatile("s_waitcnt lgkmcnt(0)" ::: "memory");
#pragma unroll
      for (int i = 0; i < 10; ++i) wgl[i / 5][i % 5] = trfrag(i);
    }
    LBAR();
#pragma unroll 1
    for (int t = 0; t < 4; ++t) {
      xmma(0);
      xload(t + 1);
      LBAR();
      xstore(t, 0);
      LBAR();
    }
    xmma(1);
    xload(5);
    asm volatile("s_waitcnt vmcnt(0)" ::: "memory");
    LBAR();
    if (tid == 0) st128(rs, C_TOK + (unsigned)h * 128u + (unsigned)j * 16u, (u32x4){1u, 1u, 1u, 1u});
    LBAR();
    const unsigned m_off = (unsigned)(ml >> 3) * 512u + (unsigned)(g * 8 + (ml & 7)) * 16u;
    int r3 = 0;
    for (int s = 0; s < NSEG; ++s, r3 = r3 == 2 ? 0 : r3 + 1) {
      TS(0);
      const int r3p = r3 == 0 ? 2 : r3 - 1;
      f32x4 ac[3];
      if (s == 0) {
#pragma unroll
        for (int u = 0; u < 3; ++u) ac[u] = z4;
#pragma unroll
        for (int kk = 0; kk < 5; ++kk) {
#pragma unroll
          for (int u = 0; u < 3; ++u) ac[u] = mfma16(w1[u][kk], asb(mf0[kk]), ac[u]);
        }
      } else {
        const unsigned base = C_MEM + (unsigned)r3 * 32768u + (unsigned)(wave >> 1) * 16384u + (unsigned)(wave & 1) * 2560u + selA;
        SG(9); slp<SLP_A>(); SG(0);
        u32x4 mf[5];
        for (unsigned spin = 0;; ++spin) {
          asm volatile("" ::: "memory");
          mf[0] = ld128(rs, base + m_off); mf[2] = ld128(rs, base + m_off + 1024u); mf[4] = ld128(rs, base + 2048u + lo_off);
          const unsigned mx = umax3(umax3(mf[0][0], mf[0][2], mf[2][0]), mf[2][2], max(mf[4][0], mf[4][2]));
          if (__all(mx != 0xFFFFFFFFu) || spin >= spin_cap) { SPN(0); if (spin >= spin_cap) spin_cap = 0; break; }
        }
        mf[1] = ror8x4(mf[0]); mf[3] = ror8x4(mf[2]);
        SG(1);
#pragma unroll
        for (int u = 0; u < 3; ++u) ac[u] = z4;
#pragma unroll
        for (int kk = 0; kk < 5; ++kk) {
#pragma unroll
          for (int u = 0; u < 3; ++u) ac[u] = mfma16(w1[u][kk], asb(mf[kk]), ac[u]);
        }
      }
      TS(1);
#pragma unroll
      for (int u = 0; u < 3; ++u) X1[u * 256 + tid] = ac[u];
      LBAR();
      SG(2);
      if (wave < 2) {
        const int tl = 2 * wave + (ml >> 3);
        const f32x4* xp = X1 + (tl < 3 ? tl : 2) * 256 + g * 16 + (ml & 7);
        f32x4 a = (xp[0] + xp[64]) + (xp[128] + xp[192]);
        u32x2 v = {nosent(pack2(a[0], a[1])), nosent(pack2(a[2], a[3]))};
        const unsigned in = (unsigned)tl * 2048u + (unsigned)(((j >> 1) * 4 + 2 * (j & 1) + (g >> 1)) * 8 + (ml & 7)) * 16u + (g & 1) * 8u;
        if (tl < 3) {
          const unsigned off = C_QKV + (unsigned)(r3 * H + h) * 16384u + in;
          st64(rs, off + selA, v); SG(3); st64(rs, off + (8192u - selA), v);
          if (tl > 0) *(u32x2*)(ws + OFF_QM + (size_t)(s * H + h) * 6144 + in) = v;
          const unsigned offp = C_QKV + (unsigned)(r3p * H + h) * 16384u + in;
          st64(rs, offp, ones2); st64(rs, offp + 8192u, ones2);
        }
      }
      TS(2);
      if (s == NSEG - 1) break;
#define XSPOS 1
      if (s + 5 < NSEG) xmma(s & 1);
      if (XSPOS == 0 && s + 4 < NSEG) xstore(s + 4, (s + 1) & 1);
      if (s + 6 < NSEG) { if (glu) slp<SLP_X - 5>(); else slp<SLP_X>(); xload(s + 6); }
      LBAR();
      if (XSPOS == 1 && s + 4 < NSEG) xstore(s + 4, (s + 1) & 1);
      LBAR();
      if (XSPOS == 2 && s + 4 < NSEG) xstore(s + 4, (s + 1) & 1);
      SG(8);
    }
    return;
  }
  const int w4 = wave - 4;
  const bool three = w4 < 1;
  b16x8 w2[2][4];
  {
    u32x4 t2[8];
#pragma unroll
    for (int t = 0; t < 2; ++t) {
      const int ct = (t < 1 || three) ? w4 + 4 * t : 0;
#pragma unroll
      for (int i = 0; i < 4; ++i) t2[t * 4 + i] = *(const u32x4*)(Wo + (size_t)(h * E + 32 * i + (lane >> 1)) * D + 80 * j + 16 * ct + (lane & 1) * 8);
    }
#pragma unroll
    for (int i = 0; i < 8; ++i) *(u32x4*)(my + i * 1024 + lane * 16) = t2[i];
    asm volatile("s_waitcnt lgkmcnt(0)" ::: "memory");
#pragma unroll
    for (int i = 0; i < 8; ++i) w2[i / 4][i % 4] = trfrag(i);
  }
  b16x8 kx[2][4]; u32x4 vwx[2];
  const b16x8 ones8 = asb((u32x4){0x3F803F80u, 0x3F803F80u, 0x3F803F80u, 0x3F803F80u});
  auto prep = [&](int sn) {
    const char* kb = KV + (sn & 1) * 16384 + lane * 16;
    b16x8 vf[2][4];
#pragma unroll
    for (int kt = 0; kt < 2; ++kt)
#pragma unroll
      for (int ks = 0; ks < 4; ++ks) { kx[kt][ks] = *(const b16x8*)(kb + (kt * 4 + ks) * 1024); vf[kt][ks] = *(const b16x8*)(kb + 8192 + (kt * 4 + ks) * 1024); }
#pragma unroll
    for (int t = 0; t < 2; ++t) {
      f32x4 d0 = z4, d1 = z4;
#pragma unroll
      for (int ks = 0; ks < 4; ++ks) { d0 = mfma16(vf[0][ks], w2[t][ks], d0); d1 = mfma16(vf[1][ks], w2[t][ks], d1); }
      vwx[t] = (u32x4){pack2(d0[0], d0[1]), pack2(d0[2], d0[3]), pack2(d1[0], d1[1]), pack2(d1[2], d1[3])};
    }
  };
  const unsigned kvs = (w4 < 2 ? (unsigned)OFF_KX : (unsigned)OFF_VX) + (unsigned)h * 8192u + (unsigned)(w4 & 1) * 4096u + (unsigned)lane * 16u;
  u32x4 stg[4];
  LBAR();
#pragma unroll 1
  for (int t = 0; t < 4; ++t) { LBAR(); LBAR(); }
  LBAR();
  for (unsigned spin = 0;; ++spin) {
    asm volatile("" ::: "memory");
    const u32x4 tk = ld128(rs, C_TOK + (unsigned)h * 128u + (unsigned)(lane & 7) * 16u);
    if (__all(tk[0] != 0xFFFFFFFFu) || spin >= spin_cap) { if (spin >= spin_cap) spin_cap = 0; break; }
    __builtin_amdgcn_s_sleep(4);
  }
  {
    u32x4 t8[8];
#pragma unroll
    for (int i = 0; i < 8; ++i) t8[i] = ld128(rw, kvs + (unsigned)(i >> 2) * (unsigned)(H * 8192) + (unsigned)(i & 3) * 1024u);
#pragma unroll
    for (int i = 0; i < 4; ++i) stg[i] = ld128(rw, kvs + 2u * (unsigned)(H * 8192) + (unsigned)i * 1024u);
#pragma unroll
    for (int i = 0; i < 8; ++i) *(u32x4*)(KV + (i >> 2) * 16384 + w4 * 4096 + (i & 3) * 1024 + lane * 16) = t8[i];
  }
  LBAR();
  prep(0);
  const int cc = lane & 7, hg = lane >> 3;
  unsigned poff[2];
#pragma unroll
  for (int t = 0; t < 2; ++t) {
    const int T = 5 * j + w4 + 4 * t;
    const unsigned c16 = (unsigned)(((T >> 1) * 4 + 2 * (T & 1) + (g >> 1)) * 8 + (ml & 7)) * 2u + (unsigned)(g & 1), rb = c16 / 5u;
    poff[t] = C_P + (rb / 3u) * 16384u + (rb % 3u) * 2560u + (unsigned)h * 80u + (c16 % 5u) * 16u;
  }
  const unsigned q_off = (unsigned)(ml >> 3) * 512u + (unsigned)(g * 8 + (ml & 7)) * 16u, kv_off = 2048u + (unsigned)(ml >> 3) * 2048u + (unsigned)(g * 8 + (ml & 7)) * 16u;
  int r3 = 0;
  for (int s = 0; s < NSEG - 1; ++s, r3 = r3 == 2 ? 0 : r3 + 1) {
    const unsigned hb = C_QKV + (unsigned)(r3 * H + h) * 16384u + selA;
    SG(9);
    LBAR();
    SG(0);
    slp<SLP_B>(); SG(1);
    f32x4 a[2];
    u32x4 qf[4], kv[4];
    for (unsigned spin = 0;; ++spin) {
      asm volatile("" ::: "memory");
      qf[0] = ld128(rs, hb + q_off); qf[2] = ld128(rs, hb + q_off + 1024u);
#pragma unroll
      for (int ks = 0; ks < 4; ++ks) kv[ks] = ld128(rs, hb + kv_off + ks * 512u);
      unsigned mx = umax3(umax3(qf[0][0], qf[0][2], qf[2][0]), qf[2][2], 0u);
#pragma unroll
      for (int ks = 0; ks < 4; ++ks) mx = umax3(mx, kv[ks][0], kv[ks][2]);
      if (__all(mx != 0xFFFFFFFFu) || spin >= spin_cap) { SPN(1); if (spin >= spin_cap) spin_cap = 0; break; }
    }
    qf[1] = ror8x4(qf[0]); qf[3] = ror8x4(qf[2]);
    SG(2);
    {
#define SB() __builtin_amdgcn_sched_barrier(0)
      f32x4 s0 = z4, s1 = z4, sm = z4, dm[2] = {z4, z4};
#pragma unroll
      for (int ks = 0; ks < 4; ++ks) { s0 = mfma16(kx[0][ks], asb(qf[ks]), s0); s1 = mfma16(kx[1][ks], asb(qf[ks]), s1); SB(); }
#pragma unroll
      for (int ks = 0; ks < 4; ++ks) { sm = mfma16(asb(kv[ks]), asb(qf[ks]), sm); SB(); }
      TS(3);
      const float NEG = -INFINITY;
      const float c2 = SCALE * 1.4426950408889634f;
      float tv[12];
      dm[0] = mfma16(asb(kv[0]), w2[0][0], dm[0]);
#pragma unroll
      for (int r = 0; r < 4; ++r) tv[r] = s0[r] * c2;
      SB();
      dm[1] = mfma16(asb(kv[0]), w2[1][0], dm[1]);
#pragma unroll
      for (int r = 0; r < 4; ++r) tv[4 + r] = s1[r] * c2;
      SB();
      float m1 = fmaxf(fmaxf(tv[0], tv[1]), tv[2]), m2 = fmaxf(fmaxf(tv[3], tv[4]), tv[5]);
      m1 = fmaxf(fmaxf(m1, tv[6]), tv[7]);
      SB();
      dm[0] = mfma16(asb(kv[1]), w2[0][1], dm[0]);
#pragma unroll
      for (int r = 0; r < 4; ++r) tv[8 + r] = (g < 2) ? sm[r] * c2 : NEG;
      SB();
      dm[1] = mfma16(asb(kv[1]), w2[1][1], dm[1]);
      m2 = fmaxf(fmaxf(m2, tv[8]), tv[9]); m1 = fmaxf(fmaxf(m1, tv[10]), tv[11]);
      float mxv = fmaxf(m1, m2);
      { unsigned u = __float_as_uint(mxv); auto r = __builtin_amdgcn_permlane16_swap(u, u, false, false); mxv = fmaxf(__uint_as_float(r[0]) + 0.f, __uint_as_float(r[1]) + 0.f); }
      SB();
      { unsigned u = __float_as_uint(mxv); auto r = __builtin_amdgcn_permlane32_swap(u, u, false, false); mxv = fmaxf(__uint_as_float(r[0]) + 0.f, __uint_as_float(r[1]) + 0.f); }
      SB();
      dm[0] = mfma16(asb(kv[2]), w2[0][2], dm[0]);
#pragma unroll
      for (int r = 0; r < 2; ++r) tv[r] = __builtin_amdgcn_exp2f(tv[r] - mxv);
      SB();
      dm[1] = mfma16(asb(kv[2]), w2[1][2], dm[1]);
#pragma unroll
      for (int r = 2; r < 4; ++r) tv[r] = __builtin_amdgcn_exp2f(tv[r] - mxv);
      SB();
#pragma unroll
      for (int r = 4; r < 6; ++r) tv[r] = __builtin_amdgcn_exp2f(tv[r] - mxv);
      SB();
      dm[0] = mfma16(asb(kv[3]), w2[0][3], dm[0]);
#pragma unroll
      for (int r = 6; r < 8; ++r) tv[r] = __builtin_amdgcn_exp2f(tv[r] - mxv);
      SB();
      dm[1] = mfma16(asb(kv[3]), w2[1][3], dm[1]);
#pragma unroll
      for (int r = 8; r < 10; ++r) tv[r] = __builtin_amdgcn_exp2f(tv[r] - mxv);
      SB();
#pragma unroll
      for (int r = 10; r < 12; ++r) tv[r] = __builtin_amdgcn_exp2f(tv[r] - mxv);
      u32x4 px = {pack2(tv[0], tv[1]), pack2(tv[2], tv[3]), pack2(tv[4], tv[5]), pack2(tv[6], tv[7])};
      SB();
      f32x4 sx = mfma16(ones8, asb(px), z4);
      a[0] = mfma16(asb(vwx[0]), asb(px), z4);
      a[1] = mfma16(asb(vwx[1]), asb(px), z4);
      u32x4 pm;
      { const unsigned p0 = pack2(tv[8], tv[9]), p1 = pack2(tv[10], tv[11]);
        auto r0 = __builtin_amdgcn_permlane32_swap(0u, p0, false, false); auto r1 = __builtin_amdgcn_permlane32_swap(0u, p1, false, false);
        pm = (u32x4){r0[0], r1[0], 0u, 0u}; }
      SB();
      sx = mfma16(ones8, asb(pm), sx);
#pragma unroll
      for (int t = 0; t < 2; ++t) {
        u32x4 vwm = {g >= 2 ? pack2(dm[t][0], dm[t][1]) : 0u, g >= 2 ? pack2(dm[t][2], dm[t][3]) : 0u, 0u, 0u};
        a[t] = mfma16(asb(vwm), asb(pm), a[t]);
      }
      SB();
      const float inv = __builtin_amdgcn_rcpf(sx[0]);
      a[0] *= inv; a[1] *= inv;
    }
#pragma unroll
    for (int t = 0; t < 2; ++t) {
      if (t == 1 && !three) break;
      u32x4 v;
#pragma unroll
      for (int r = 0; r < 4; ++r) v[r] = min(__float_as_uint(a[t][r]), 0xFFFFFFFEu);
      const unsigned off = (unsigned)(1 + (s & 1)) * PAGE + poff[t];
      if (ml < 8) { st128(rs, off, v); st128(rs, off + 8192u, v); }
    }
    SG(3);
    TS(4);
    LBAR();
    SG(4);
    auto kvwrite = [&] {
      if (s + 2 < NSEG - 1) {
#pragma unroll
        for (int i = 0; i < 4; ++i) *(u32x4*)(KV + (s & 1) * 16384 + w4 * 4096 + i * 1024 + lane * 16) = stg[i];
      }
    };
    if (w4 == 3) {
      const int r3n = r3 == 2 ? 0 : r3 + 1;
      kvwrite();
        const unsigned offw = (unsigned)(1 + (s & 1)) * PAGE + C_P + (unsigned)(cb / 3) * 16384u + (unsigned)(cb % 3) * 2560u + (unsigned)hg * 320u + (unsigned)cc * 16u;
        const unsigned off = cc < 5 ? offw + ((s & 1) ? selP1 : selP0) : OOB;
        slp<SLP_C>(); SG(5);
        u32x4 v[4];
        for (unsigned spin = 0;; ++spin) {
          asm volatile("" ::: "memory");
#pragma unroll
          for (int i = 0; i < 4; ++i) v[i] = ld128(rs, off + i * 80u);
          unsigned mx = 0;
#pragma unroll
          for (int i = 0; i < 4; ++i) { mx = umax3(mx, v[i][0], v[i][1]); mx = umax3(mx, v[i][2], v[i][3]); }
          if (__all(mx != 0xFFFFFFFFu) || spin >= spin_cap) { SPN(2); if (spin >= spin_cap) spin_cap = 0; break; }
        }
        SG(6);
        TS(5);
        float f[4];
#pragma unroll
        for (int r = 0; r < 4; ++r) {
          float x = (__uint_as_float(v[0][r]) + __uint_as_float(v[1][r])) + (__uint_as_float(v[2][r]) + __uint_as_float(v[3][r]));
          x += ror8(x); x = xsum32(xsum16(x)); f[r] = x;
        }
        u32x2 w = {nosent(pack2(f[0], f[1])), nosent(pack2(f[2], f[3]))};
        const unsigned mq = (unsigned)cb >> 6, min_ = (mq >> 1) * 16384u + (mq & 1) * 2560u + (unsigned)(5 * (cb & 63) + lane) * 8u;
        if (lane < 5) { st64(rs, C_MEM + (unsigned)r3n * 32768u + min_, w); st64(rs, C_MEM + (unsigned)r3n * 32768u + 8192u + min_, w); }
        SG(7);
        TS(6);
        LBAR();
        if (cc < 5) {
#pragma unroll
          for (int i = 0; i < 4; ++i) { st128(rs, offw + i * 80u, ones4); st128(rs, offw + 8192u + i * 80u, ones4); }
        }
        if (lane < 5) { st64(rs, C_MEM + (unsigned)r3 * 32768u + min_, ones2); st64(rs, C_MEM + (unsigned)r3 * 32768u + 8192u + min_, ones2); }
      if (s + 1 < NSEG - 1) prep(s + 1);
    } else {
      if (s + 1 < NSEG - 1) prep(s + 1);
      kvwrite();
      LBAR();
    }
    if (s + 3 < NSEG - 1) {
#pragma unroll
      for (int i = 0; i < 4; ++i) stg[i] = ld128(rw, kvs + (unsigned)(s + 3) * (unsigned)(H * 8192) + (unsigned)i * 1024u);
    }
  }
  LBAR();
}

__global__ __launch_bounds__(512, 1) void k_chain(const u16* __restrict__ mem0, const u16* __restrict__ Wq, const u16* __restrict__ Wk,
                                                   const u16* __restrict__ Wv, const u16* __restrict__ Wo, const u16* __restrict__ wg,
                                                   const u16* __restrict__ wu, char* __restrict__ ws) {
  extern __shared__ __attribute__((aligned(16))) char dyn_lds[];
  chain_role(blockIdx.x, mem0, Wq, Wk, Wv, Wo, wg, wu, ws, dyn_lds);
}

DI void attn_unit(const int s, const int h, const b16x8 (&qf)[2][4], char* __restrict__ ws, char* V) {
  const int lane = threadIdx.x & 63, ml = lane & 15, g = lane >> 4;
  const char* KX = ws + OFF_KX + (size_t)(s * H + h) * 8192 + lane * 16;
  const char* VX = ws + OFF_VX + (size_t)(s * H + h) * 8192 + lane * 16;
  const char* KM = ws + OFF_QM + (size_t)(s * H + h) * 6144 + 2048 + (g * 8 + (ml & 7)) * 16;
  b16x8 kx[2][4], km[4];
#pragma unroll
  for (int ks = 0; ks < 4; ++ks) {
#pragma unroll
    for (int t = 0; t < 2; ++t) {
      kx[t][ks] = *(const b16x8*)(KX + (t * 4 + ks) * 1024);
      *(u32x4*)(V + (16 * t + ml) * 272 + (32 * ks + 8 * g) * 2) = *(const u32x4*)(VX + (t * 4 + ks) * 1024);
    }
    km[ks] = *(const b16x8*)(KM + ks * 512);
    u32x4 vmr = *(const u32x4*)(KM + 2048 + ks * 512);
    if (ml >= 8) vmr = (u32x4){0, 0, 0, 0};
    *(u32x4*)(V + (32 + ml) * 272 + (32 * ks + 8 * g) * 2) = vmr;
  }
  u32x4 px[2], pm[2];
#pragma unroll
  for (int qt = 0; qt < 2; ++qt) {
    f32x4 s0 = {0, 0, 0, 0}, s1 = {0, 0, 0, 0}, sm = {0, 0, 0, 0};
#pragma unroll
    for (int ks = 0; ks < 4; ++ks) {
      s0 = mfma16(kx[0][ks], qf[qt][ks], s0);
      s1 = mfma16(kx[1][ks], qf[qt][ks], s1);
      sm = mfma16(km[ks], qf[qt][ks], sm);
    }
    const int qi = 16 * qt + ml;
    float sv[12];
#pragma unroll
    for (int r = 0; r < 4; ++r) {
      sv[r] = (4 * g + r <= qi) ? rbf(rbf(s0[r]) * SCALE) : -INFINITY;
      sv[4 + r] = (16 + 4 * g + r <= qi) ? rbf(rbf(s1[r]) * SCALE) : -INFINITY;
      sv[8 + r] = (g < 2) ? rbf(rbf(sm[r]) * SCALE) : -INFINITY;
    }
    float mxv = sv[0];
#pragma unroll
    for (int r = 1; r < 12; ++r) mxv = fmaxf(mxv, sv[r]);
    mxv = xmax32(xmax16(mxv));
    float sum = 0.f;
#pragma unroll
    for (int r = 0; r < 12; ++r) { sv[r] = __expf(sv[r] - mxv); sum += sv[r]; }
    sum = xsum32(xsum16(sum));
    const float inv = 1.f / sum;
    px[qt] = (u32x4){pack2(sv[0] * inv, sv[1] * inv), pack2(sv[2] * inv, sv[3] * inv), pack2(sv[4] * inv, sv[5] * inv), pack2(sv[6] * inv, sv[7] * inv)};
    pm[qt] = (u32x4){pack2(sv[8] * inv, sv[9] * inv), pack2(sv[10] * inv, sv[11] * inv), 0u, 0u};
  }
  asm volatile("s_waitcnt lgkmcnt(0)" ::: "memory");
  const unsigned vb = (unsigned)(size_t)V + (4 * g + (ml >> 2)) * 272 + (lane & 3) * 8;
#pragma unroll
  for (int kk = 0; kk < 4; ++kk) {
    u32x4 of[2];
#pragma unroll
    for (int hf = 0; hf < 2; ++hf) {
      const int et = 2 * kk + hf;
      s16x4 v0 = __builtin_amdgcn_ds_read_tr16_b64_v4i16((LAS s16x4*)(vb + et * 32));
      s16x4 v1 = __builtin_amdgcn_ds_read_tr16_b64_v4i16((LAS s16x4*)(vb + 16 * 272 + et * 32));
      s16x4 v2 = __builtin_amdgcn_ds_read_tr16_b64_v4i16((LAS s16x4*)(vb + 32 * 272 + et * 32));
      typedef short s16x8 __attribute__((ext_vector_type(8)));
      s16x8 va = {v0[0], v0[1], v0[2], v0[3], v1[0], v1[1], v1[2], v1[3]};
      s16x8 vbm = {v2[0], v2[1], v2[2], v2[3], 0, 0, 0, 0};
#pragma unroll
      for (int qt = 0; qt < 2; ++qt) {
        f32x4 o = {0, 0, 0, 0};
        o = mfma16(__builtin_bit_cast(b16x8, va), asb(px[qt]), o);
        o = mfma16(__builtin_bit_cast(b16x8, vbm), asb(pm[qt]), o);
        of[qt][2 * hf] = pack2(o[0], o[1]); of[qt][2 * hf + 1] = pack2(o[2], o[3]);
      }
    }
#pragma unroll
    for (int qt = 0; qt < 2; ++qt) *(u32x4*)(ws + OFF_O + (size_t)((s * 2 + qt) * 128 + h * 4 + kk) * 1024 + lane * 16) = of[qt];
  }
  asm volatile("s_waitcnt lgkmcnt(0)" ::: "memory");
}

__global__ __launch_bounds__(512, 1) void k_post1(const u16* __restrict__ x, const u16* __restrict__ Wo, const u16* __restrict__ wd,
                                                  const u16* __restrict__ gate_w, char* __restrict__ ws) {
  extern __shared__ __attribute__((aligned(16))) char lds[];
  const int lane = threadIdx.x & 63, wave = __builtin_amdgcn_readfirstlane(threadIdx.x >> 6);
  const int gw = blockIdx.x * 8 + wave;
  char* V = lds + wave * (48 * 272);
  {
    b16x8 q0[2][4];
#pragma unroll
    for (int t = 0; t < 2; ++t)
#pragma unroll
      for (int ks = 0; ks < 4; ++ks) q0[t][ks] = *(const b16x8*)(ws + OFF_QX + (size_t)gw * 8192 + (t * 4 + ks) * 1024 + lane * 16);
    attn_unit(gw >> 5, gw & 31, q0, ws, V);
  }
  {
    const int ml = lane & 15, g = lane >> 4;
    for (int t = gw; t < S; t += 2048) {
      float s = 0.f;
#pragma unroll
      for (int i = 0; i < 5; ++i) {
        const unsigned xv = *(const unsigned*)(x + (size_t)t * D + i * 128 + lane * 2), gv = *(const unsigned*)(gate_w + i * 128 + lane * 2);
        s += rbf(__uint_as_float(xv << 16) * __uint_as_float(gv << 16)) + rbf(__uint_as_float(xv & 0xFFFF0000u) * __uint_as_float(gv & 0xFFFF0000u));
      }
#pragma unroll
      for (int o = 32; o >= 1; o >>= 1) s += __shfl_xor(s, o);
      s = rbf(s);
      float gg = rbf(1.f / (1.f + __expf(-s)));
      if (lane == 0) ((float*)(ws + OFF_G))[t] = gg;
    }
    char* my = V;
    const unsigned trb = (unsigned)(size_t)my + (4 * g + (ml >> 2)) * 32 + (lane & 3) * 8;
    for (int u = gw; u < 182 * 5; u += 2048) {
      const int kk = u / 5, c0 = (u % 5) * 8;
      const u16* W = kk < 128 ? Wo + (size_t)32 * kk * D : wd + (size_t)32 * (kk - 128) * D;
      u32x4 t8[8];
#pragma unroll
      for (int i = 0; i < 8; ++i) t8[i] = *(const u32x4*)(W + (size_t)(4 * i + (lane >> 4)) * D + 16 * c0 + (lane & 15) * 8);
#pragma unroll
      for (int i = 0; i < 8; ++i) *(u32x4*)(my + ((lane & 15) >> 1) * 1024 + (4 * i + (lane >> 4)) * 32 + (lane & 1) * 16) = t8[i];
      asm volatile("s_waitcnt lgkmcnt(0)" ::: "memory");
#pragma unroll
      for (int i = 0; i < 8; ++i) {
        s16x4 lo = __builtin_amdgcn_ds_read_tr16_b64_v4i16((LAS s16x4*)(trb + i * 1024));
        s16x4 hi = __builtin_amdgcn_ds_read_tr16_b64_v4i16((LAS s16x4*)(trb + i * 1024 + 512));
        typedef short s16x8 __attribute__((ext_vector_type(8)));
        s16x8 tt = {lo[0], lo[1], lo[2], lo[3], hi[0], hi[1], hi[2], hi[3]};
        *(s16x8*)(ws + OFF_WF + ((size_t)(c0 + i) * 182 + kk) * 1024 + lane * 16) = tt;
      }
      asm volatile("s_waitcnt lgkmcnt(0)" ::: "memory");
    }
  }
}

__global__ __launch_bounds__(512, 1) void k_post2(const char* __restrict__ ws, u16* __restrict__ out) {
  extern __shared__ __attribute__((aligned(16))) char lds[];
  const int tid = threadIdx.x, lane = tid & 63, wave = __builtin_amdgcn_readfirstlane(tid >> 6), ml = lane & 15, g = lane >> 4;
  const int b = blockIdx.x, xc = b & 7, idx = b >> 3, tt = xc * 4 + (idx >> 3), cgp = idx & 7;
  f32x4* X = (f32x4*)lds;
  f32x4 asum[2][2];
#pragma unroll
  for (int ph = 0; ph < 2; ++ph) {
    const int kt = ph == 0 ? 128 : 54, nk = ph == 0 ? 16 : (wave < 6 ? 7 : 6);
    const char* tb = ws + (ph ? OFF_HH : OFF_O) + (size_t)(tt * 4) * kt * 1024 + (size_t)wave * 1024 + lane * 16;
    const size_t tstr = (size_t)kt * 1024;
    const char* wb = ws + OFF_WF + ((size_t)(cgp * 5) * 182 + (ph ? 128 : 0) + wave) * 1024 + lane * 16;
    f32x4 acc[4][5];
#pragma unroll
    for (int i = 0; i < 4; ++i)
#pragma unroll
      for (int c = 0; c < 5; ++c) acc[i][c] = (f32x4){0, 0, 0, 0};
    b16x8 tf[4][4], wf[4][5];
    auto ldk = [&](int slot, int k) {
      const int kn = k < nk ? k : nk - 1;
#pragma unroll
      for (int i = 0; i < 4; ++i) tf[slot][i] = *(const b16x8*)(tb + i * tstr + (size_t)kn * 8192);
#pragma unroll
      for (int c = 0; c < 5; ++c) wf[slot][c] = *(const b16x8*)(wb + (size_t)c * 182 * 1024 + (size_t)kn * 8192);
    };
    ldk(0, 0); ldk(1, 1); ldk(2, 2);
    for (int k = 0; k < nk; k += 4) {
#pragma unroll
      for (int u = 0; u < 4; ++u) {
        ldk((u + 3) & 3, k + u + 3);
        if (k + u < nk) {
#pragma unroll
          for (int i = 0; i < 4; ++i)
#pragma unroll
            for (int c = 0; c < 5; ++c) acc[i][c] = mfma16(wf[u][c], tf[u][i], acc[i][c]);
        }
      }
    }
#pragma unroll
    for (int rd = 0; rd < 2; ++rd) {
#pragma unroll
      for (int t = 0; t < 10; ++t) { const int tl = rd * 10 + t; X[(wave * 10 + t) * 64 + lane] = acc[tl / 5][tl % 5]; }
      RBAR();
#pragma unroll
      for (int u = 0; u < 2; ++u) {
        const int t = wave + 8 * u;
        if (t < 10) {
          const f32x4* xp = X + t * 64 + lane;
          const f32x4 v = ((xp[0] + xp[640]) + (xp[1280] + xp[1920])) + ((xp[2560] + xp[3200]) + (xp[3840] + xp[4480]));
          if (ph == 0) asum[rd][u] = v;
          else {
            const int tl = rd * 10 + t, i = tl / 5, c = tl % 5;
            const int tok = tt * 64 + 16 * i + ml;
            const float gg = ((const float*)(ws + OFF_G))[tok], og = rbf(1.f - gg);
            float r[4];
#pragma unroll
            for (int q = 0; q < 4; ++q) { float ta = rbf(gg * rbf(asum[rd][u][q])), tb2 = rbf(og * rbf(v[q])); asm volatile("" : "+v"(ta), "+v"(tb2)); r[q] = ta + tb2; }
            *(u32x2*)(out + (size_t)tok * D + 80 * cgp + 16 * c + 4 * g) = (u32x2){pack2(r[0], r[1]), pack2(r[2], r[3])};
          }
        }
      }
      RBAR();
    }
  }
}

extern "C" void kernel_launch(void* const* d_in, const int* in_sizes, int n_in, void* d_out, int out_size, void* d_ws, size_t ws_size,
                              hipStream_t stream) {
  const u16* x = (const u16*)d_in[0];
  const u16* mem0 = (const u16*)d_in[1];
  const u16* Wq = (const u16*)d_in[2];
  const u16* Wk = (const u16*)d_in[3];
  const u16* Wv = (const u16*)d_in[4];
  const u16* Wo = (const u16*)d_in[5];
  const u16* wg = (const u16*)d_in[8];
  const u16* wu = (const u16*)d_in[9];
  const u16* wd = (const u16*)d_in[10];
  const u16* gw = (const u16*)d_in[11];
  char* ws = (char*)d_ws;
  k_init<<<(S * D / 8 + (2 * PAGE + C_P + 86 * 16384) / 16 + 255) / 256, 256, 0, stream>>>(x, ws);
  hipFuncSetAttribute((const void*)k_chain, hipFuncAttributeMaxDynamicSharedMemorySize, 156 * 1024);
  k_chain<<<256, 512, 156 * 1024, stream>>>(mem0, Wq, Wk, Wv, Wo, wg, wu, ws);
  hipFuncSetAttribute((const void*)k_post1, hipFuncAttributeMaxDynamicSharedMemorySize, 128 * 1024);
  k_post1<<<256, 512, 104 * 1024 + 512, stream>>>(x, Wo, wd, gw, ws);
  hipFuncSetAttribute((const void*)k_post2, hipFuncAttributeMaxDynamicSharedMemorySize, 80 * 1024);
  k_post2<<<256, 512, 80 * 1024, stream>>>(ws, (u16*)d_out);
}
```

```cpp
#include <hip/hip_runtime.h>
#include <hip/hip_bf16.h>
#include <stdint.h>
#include <stdio.h>

typedef unsigned short u16;
typedef __bf16 b16x8 __attribute__((ext_vector_type(8)));
typedef short s16x4 __attribute__((ext_vector_type(4)));
typedef float f32x4 __attribute__((ext_vector_type(4)));
typedef unsigned u32x4 __attribute__((ext_vector_type(4)));
typedef unsigned u32x2 __attribute__((ext_vector_type(2)));
#define LAS __attribute__((address_space(3)))
#define DI __device__ __forceinline__

constexpr int S = 2048, D = 640, H = 32, E = 128, HE = 4096, MM = 8, SEG = 32, NSEG = 64, F = 1728;
constexpr float SCALE = 0.08838834764831845f;

constexpr size_t OFF_KX = 0;
constexpr size_t OFF_VX = OFF_KX + (size_t)S * HE * 2;
constexpr size_t OFF_QX = OFF_VX + (size_t)S * HE * 2;
constexpr size_t OFF_O  = OFF_QX + (size_t)S * HE * 2;
constexpr size_t OFF_HH = OFF_O + (size_t)S * HE * 2;
constexpr size_t OFF_WF = OFF_HH + (size_t)S * F * 2;
constexpr size_t OFF_XF = OFF_WF + (size_t)40 * 182 * 1024;
constexpr size_t OFF_G  = OFF_XF + (size_t)S * D * 2;
constexpr size_t OFF_QM = OFF_G + (size_t)S * 4;
constexpr size_t OFF_COMM = OFF_QM + (size_t)NSEG * H * 6144;
constexpr size_t OFF_END = OFF_COMM + (size_t)8 * 1048576;
constexpr size_t OFF_DBG = OFF_END;
constexpr unsigned PAGE = 2097152u, C_MEM = 65536u, C_TOK = 180224u, C_QKV = 196608u, C_P = 65536u;
__host__ __device__ inline char* comm_base(char* ws) { return (char*)(((size_t)(ws + OFF_COMM) + (PAGE - 1)) & ~(size_t)(PAGE - 1)); }

DI float bf2f(u16 v) { return __builtin_bit_cast(float, (unsigned)v << 16); }
DI u16 f2bf(float f) { return __builtin_bit_cast(u16, (__bf16)f); }
DI float rbf(float f) { return (float)(__bf16)f; }
typedef float f32x2 __attribute__((ext_vector_type(2)));
typedef __bf16 b16x2 __attribute__((ext_vector_type(2)));
DI unsigned pack2(float lo, float hi) { f32x2 v = {lo, hi}; return __builtin_bit_cast(unsigned, __builtin_convertvector(v, b16x2)); }
DI f32x4 mfma16(b16x8 a, b16x8 b, f32x4 c) { return __builtin_amdgcn_mfma_f32_16x16x32_bf16(a, b, c, 0, 0, 0); }
DI b16x8 asb(u32x4 v) { return __builtin_bit_cast(b16x8, v); }

DI float gelu_tanh(float x) {
  float y = 0.7978845608028654f * (x + 0.044715f * x * x * x);
  float t = 1.f - 2.f / (__expf(2.f * y) + 1.f);
  return 0.5f * x * (1.f + t);
}

#define RBAR() asm volatile("s_waitcnt lgkmcnt(0)\n\ts_barrier" ::: "memory")

constexpr unsigned NX_INIT = S * D / 8, NF_INIT = (2 * PAGE + C_P + 86 * 16384) / 16, NB_INIT1 = (NX_INIT + NF_INIT + 255) / 256;
__global__ __launch_bounds__(256) void k_init(const u16* __restrict__ x, const u16* __restrict__ Wo, const u16* __restrict__ wd,
                                              const u16* __restrict__ gate_w, char* __restrict__ ws) {
  __shared__ __attribute__((aligned(16))) char lds[4 * 8192];
  if (blockIdx.x >= 512) {
    const unsigned i = (blockIdx.x - 512) * 256 + threadIdx.x;
    if (i < NF_INIT) *(u32x4*)(comm_base(ws) + (size_t)i * 16) = (u32x4){0xFFFFFFFFu, 0xFFFFFFFFu, 0xFFFFFFFFu, 0xFFFFFFFFu};
    else if (i - NF_INIT < NX_INIT) {
      const unsigned k = i - NF_INIT, lane = k & 63, f = k >> 6, kk = f % 20, t16 = f / 20;
      *(u32x4*)(ws + OFF_XF + (size_t)k * 16) = *(const u32x4*)(x + (size_t)(16 * t16 + (lane & 15)) * D + 32 * kk + 8 * (lane >> 4));
    }
    return;
  }
  const int lane = threadIdx.x & 63, wave = __builtin_amdgcn_readfirstlane(threadIdx.x >> 6), ml = lane & 15, g = lane >> 4;
  const int gw = blockIdx.x * 4 + wave;
  char* V = lds + wave * 8192;
  {
    for (int t = gw; t < S; t += 2048) {
      float s = 0.f;
#pragma unroll
      for (int i = 0; i < 5; ++i) {
        const unsigned xv = *(const unsigned*)(x + (size_t)t * D + i * 128 + lane * 2), gv = *(const unsigned*)(gate_w + i * 128 + lane * 2);
        s += rbf(__uint_as_float(xv << 16) * __uint_as_float(gv << 16)) + rbf(__uint_as_float(xv & 0xFFFF0000u) * __uint_as_float(gv & 0xFFFF0000u));
      }
#pragma unroll
      for (int o = 32; o >= 1; o >>= 1) s += __shfl_xor(s, o);
      s = rbf(s);
      float gg = rbf(1.f / (1.f + __expf(-s)));
      if (lane == 0) ((float*)(ws + OFF_G))[t] = gg;
    }
    char* my = V;
    const unsigned trb = (unsigned)(size_t)my + (4 * g + (ml >> 2)) * 32 + (lane & 3) * 8;
    for (int u = gw; u < 182 * 5; u += 2048) {
      const int kk = u / 5, c0 = (u % 5) * 8;
      const u16* W = kk < 128 ? Wo + (size_t)32 * kk * D : wd + (size_t)32 * (kk - 128) * D;
      u32x4 t8[8];
#pragma unroll
      for (int i = 0; i < 8; ++i) t8[i] = *(const u32x4*)(W + (size_t)(4 * i + (lane >> 4)) * D + 16 * c0 + (lane & 15) * 8);
#pragma unroll
      for (int i = 0; i < 8; ++i) *(u32x4*)(my + ((lane & 15) >> 1) * 1024 + (4 * i + (lane >> 4)) * 32 + (lane & 1) * 16) = t8[i];
      asm volatile("s_waitcnt lgkmcnt(0)" ::: "memory");
#pragma unroll
      for (int i = 0; i < 8; ++i) {
        s16x4 lo = __builtin_amdgcn_ds_read_tr16_b64_v4i16((LAS s16x4*)(trb + i * 1024));
        s16x4 hi = __builtin_amdgcn_ds_read_tr16_b64_v4i16((LAS s16x4*)(trb + i * 1024 + 512));
        typedef short s16x8 __attribute__((ext_vector_type(8)));
        s16x8 tt = {lo[0], lo[1], lo[2], lo[3], hi[0], hi[1], hi[2], hi[3]};
        *(s16x8*)(ws + OFF_WF + ((size_t)(c0 + i) * 182 + kk) * 1024 + lane * 16) = tt;
      }
      asm volatile("s_waitcnt lgkmcnt(0)" ::: "memory");
    }
  }
}

#define SC1 16
DI u32x4 ld128(__amdgpu_buffer_rsrc_t rs, unsigned off) { return __builtin_amdgcn_raw_buffer_load_b128(rs, off, 0, SC1); }
DI u32x2 ld64(__amdgpu_buffer_rsrc_t rs, unsigned off) { return __builtin_amdgcn_raw_buffer_load_b64(rs, off, 0, SC1); }
DI void st128(__amdgpu_buffer_rsrc_t rs, unsigned off, u32x4 v) { __builtin_amdgcn_raw_buffer_store_b128(v, rs, off, 0, SC1); }
DI void st64(__amdgpu_buffer_rsrc_t rs, unsigned off, u32x2 v) { __builtin_amdgcn_raw_buffer_store_b64(v, rs, off, 0, SC1); }
DI unsigned nosent(unsigned v) { return v == 0xFFFFFFFFu ? 0xFFFEFFFFu : v; }
DI unsigned umax3(unsigned a, unsigned b, unsigned c) { return max(max(a, b), c); }
DI float xsum16(float x) { unsigned u = __float_as_uint(x); auto r = __builtin_amdgcn_permlane16_swap(u, u, false, false); return __uint_as_float(r[0]) + __uint_as_float(r[1]); }
DI float xsum32(float x) { unsigned u = __float_as_uint(x); auto r = __builtin_amdgcn_permlane32_swap(u, u, false, false); return __uint_as_float(r[0]) + __uint_as_float(r[1]); }
DI float xmax16(float x) { unsigned u = __float_as_uint(x); auto r = __builtin_amdgcn_permlane16_swap(u, u, false, false); return fmaxf(__uint_as_float(r[0]), __uint_as_float(r[1])); }
DI float xmax32(float x) { unsigned u = __float_as_uint(x); auto r = __builtin_amdgcn_permlane32_swap(u, u, false, false); return fmaxf(__uint_as_float(r[0]), __uint_as_float(r[1])); }
DI float ror4(float x) { return __uint_as_float(__builtin_amdgcn_update_dpp(0u, __float_as_uint(x), 0x124, 0xf, 0xf, false)); }
DI float ror8(float x) { return __uint_as_float(__builtin_amdgcn_update_dpp(0u, __float_as_uint(x), 0x128, 0xf, 0xf, false)); }
DI u32x4 ror8x4(u32x4 v) { u32x4 r; for (int i = 0; i < 4; ++i) r[i] = __builtin_amdgcn_update_dpp(0u, v[i], 0x128, 0xf, 0xf, false); return r; }
template <int N> struct IC { static constexpr int value = N; };
#define LBAR() asm volatile("s_waitcnt lgkmcnt(0)\n\ts_barrier" ::: "memory")
template <int N> DI void slp() { if constexpr (N > 0) { __builtin_amdgcn_s_sleep(N > 15 ? 15 : N); if constexpr (N > 15) slp<N - 15>(); } }

#define SG(i)
#define TS(i)
#define SPN(k)
constexpr unsigned SPIN_CAP = 1u << 17;
#define SLP_A 16
#define SLP_B 14
#define SLP_B3 4
#define XK1 5
#define XK2 5
#define XATN 8
#define SLP_X 21
#define SLP_C 11

DI void chain_role(const int cb, const u16* __restrict__ mem0, const u16* __restrict__ Wq, const u16* __restrict__ Wk,
                   const u16* __restrict__ Wv, const u16* __restrict__ Wo, const u16* __restrict__ wg, const u16* __restrict__ wu,
                   char* __restrict__ ws, char* dyn_lds) {
  const int xcd = cb & 7, y = cb >> 3, h = xcd * 4 + (y >> 3), j = y & 7;
  const int tid = threadIdx.x, lane = tid & 63, wave = __builtin_amdgcn_readfirstlane(tid >> 6);
  const int ml = lane & 15, g = lane >> 4;
  const __amdgpu_buffer_rsrc_t rs = __builtin_amdgcn_make_buffer_rsrc(comm_base(ws), 0, (int)(3 * PAGE), 0x00020000);
  unsigned selA, selP0, selP1;
  {
    unsigned* pl = (unsigned*)(dyn_lds + 156 * 1024 - 64);
    if (wave == 0) {
      {
        const unsigned v = __builtin_amdgcn_raw_buffer_load_b32(rs, (unsigned)(lane < 3 ? lane : 0) * PAGE + (PAGE - 4096u), 0, SC1);
        asm volatile("s_waitcnt vmcnt(0)" :: "v"(v) : "memory");
      }
#pragma unroll 1
      for (int p = 0; p < 4; ++p) {
        const unsigned po = (unsigned)(p < 2 ? 0 : p - 1) * PAGE + (unsigned)(cb >> 6) * 16384u + (unsigned)(p == 1) * 8192u + (unsigned)(cb & 63) * 128u;
        asm volatile("s_waitcnt vmcnt(0) lgkmcnt(0)" ::: "memory");
        const unsigned long long t0 = __builtin_amdgcn_s_memtime();
        asm volatile("s_waitcnt lgkmcnt(0)" ::: "memory");
        const unsigned v = __builtin_amdgcn_raw_buffer_load_b32(rs, po, 0, SC1);
        asm volatile("s_waitcnt vmcnt(0)" :: "v"(v) : "memory");
        const unsigned long long t1 = __builtin_amdgcn_s_memtime();
        if (lane == 0) pl[p] = (unsigned)(t1 - t0);
      }
    }
    LBAR();
    selA = __builtin_amdgcn_readfirstlane(pl[1] < pl[0] ? 8192u : 0u);
    selP0 = __builtin_amdgcn_readfirstlane(2 * pl[2] > pl[0] + pl[1] ? 8192u : 0u);
    selP1 = __builtin_amdgcn_readfirstlane(2 * pl[3] > pl[0] + pl[1] ? 8192u : 0u);
  }
  const u32x4 ones4 = {0xFFFFFFFFu, 0xFFFFFFFFu, 0xFFFFFFFFu, 0xFFFFFFFFu};
  const u32x2 ones2 = {0xFFFFFFFFu, 0xFFFFFFFFu};
  f32x4* X1 = (f32x4*)dyn_lds;
  f32x4* X2 = (f32x4*)(dyn_lds + 61440);
  const __amdgpu_buffer_rsrc_t rw = __builtin_amdgcn_make_buffer_rsrc(ws, 0, (int)OFF_O, 0x00020000);
  char* KV = dyn_lds + 12288;
  char* VMS = dyn_lds + 143360;
  char* WL = dyn_lds + 61440;
  const f32x4 z4 = {0, 0, 0, 0};
  const unsigned OOB = 0x7FFFFF00u;
  const unsigned lo_off = ml < 8 ? (unsigned)(g * 8 + ml) * 16u : OOB;
  unsigned spin_cap = SPIN_CAP;
  typedef short s16x8 __attribute__((ext_vector_type(8)));
  char* my = WL + (wave < 4 ? wave * 15360 : 61440 + (wave - 4) * 8192);
  const unsigned trb = (unsigned)(size_t)my + (8 * g + (ml >> 2)) * 32 + (lane & 3) * 8;
  auto trfrag = [&](int i) {
    s16x4 lo = __builtin_amdgcn_ds_read_tr16_b64_v4i16((LAS s16x4*)(trb + i * 1024));
    s16x4 hi = __builtin_amdgcn_ds_read_tr16_b64_v4i16((LAS s16x4*)(trb + i * 1024 + 128));
    s16x8 tt = {lo[0], lo[1], lo[2], lo[3], hi[0], hi[1], hi[2], hi[3]};
    return __builtin_bit_cast(b16x8, tt);
  };

  if (wave < 4) {
    b16x8 w1[3][5], wgl[2][5];
    const bool glu = cb < 108;
    u32x4 mf0[5];
    u32x4 xf[2][5];
    auto xloadp = [&](int t, auto k0c, auto k1c) {
      constexpr int k0 = decltype(k0c)::value, k1 = decltype(k1c)::value;
#pragma unroll
      for (int qt = 0; qt < 2; ++qt)
#pragma unroll
        for (int kk = k0; kk < k1; ++kk) xf[qt][kk] = *(const u32x4*)(ws + OFF_XF + (size_t)((2 * t + qt) * 20 + 5 * wave + kk) * 1024 + lane * 16);
    };
    auto xload = [&](int t) { xloadp(t, IC<0>{}, IC<5>{}); };
    auto xmma = [&](int pb) {
#pragma unroll
      for (int u = 0; u < 3; ++u)
#pragma unroll
        for (int qt = 0; qt < 2; ++qt) {
          f32x4 a = z4;
#pragma unroll
          for (int kk = 0; kk < 5; ++kk) a = mfma16(w1[u][kk], asb(xf[qt][kk]), a);
          X2[pb * 2560 + (u * 2 + qt) * 256 + tid] = a;
        }
      if (glu) {
#pragma unroll
        for (int u = 0; u < 2; ++u)
#pragma unroll
          for (int qt = 0; qt < 2; ++qt) {
            f32x4 a = z4;
#pragma unroll
            for (int kk = 0; kk < 5; ++kk) a = mfma16(wgl[u][kk], asb(xf[qt][kk]), a);
            X2[pb * 2560 + (6 + u * 2 + qt) * 256 + tid] = a;
          }
      }
    };
    auto xstore = [&](int t, int pb) {
      if (wave < 3) {
        const unsigned ob = (wave == 0 ? (unsigned)OFF_QX : (wave == 1 ? (unsigned)OFF_KX : (unsigned)OFF_VX)) + (unsigned)(t * H + h) * 8192u;
#pragma unroll
        for (int qt = 0; qt < 2; ++qt) {
          const f32x4* xp = X2 + pb * 2560 + (wave * 2 + qt) * 256 + lane;
          const f32x4 a = (xp[0] + xp[64]) + (xp[128] + xp[192]);
          const unsigned oo = ob + (unsigned)(((qt * 4 + (j >> 1)) * 64 + ml + 16 * (2 * (j & 1) + (g >> 1))) * 16 + (g & 1) * 8);
          const u32x2 vv = {pack2(a[0], a[1]), pack2(a[2], a[3])};
          st64(rw, oo, vv);
        }
      }
      if (glu && wave < 2) {
        const f32x4* xp = X2 + pb * 2560 + (6 + wave) * 256 + lane;
        const f32x4 ga = (xp[0] + xp[64]) + (xp[128] + xp[192]), ua = (xp[512] + xp[576]) + (xp[640] + xp[704]);
        float hv[4];
#pragma unroll
        for (int r = 0; r < 4; ++r) hv[r] = rbf(gelu_tanh(rbf(ga[r]))) * rbf(ua[r]);
        *(u32x2*)(ws + OFF_HH + (size_t)((2 * t + wave) * 54 + (cb >> 1)) * 1024 + lane * 16 + (cb & 1) * 8) = (u32x2){pack2(hv[0], hv[1]), pack2(hv[2], hv[3])};
      }
    };
    {
      u32x4 t1[15], tg[10];
#pragma unroll
      for (int i = 0; i < 10; ++i) tg[i] = *(const u32x4*)((i < 5 ? wg : wu) + (size_t)(160 * wave + 32 * (i % 5) + (lane >> 1)) * F + 16 * (glu ? cb : 0) + (lane & 1) * 8);
#pragma unroll
      for (int tt = 0; tt < 3; ++tt) {
        const u16* W = tt == 0 ? Wq : (tt == 1 ? Wk : Wv);
#pragma unroll
        for (int i = 0; i < 5; ++i) t1[tt * 5 + i] = *(const u32x4*)(W + (size_t)(160 * wave + 32 * i + (lane >> 1)) * HE + h * E + 16 * j + (lane & 1) * 8);
      }
#pragma unroll
      for (int kk = 0; kk < 5; ++kk) mf0[kk] = *(const u32x4*)(mem0 + (ml & 7) * D + 32 * (5 * wave + kk) + 8 * g);
      xload(0);
#pragma unroll
      for (int i = 0; i < 15; ++i) *(u32x4*)(my + i * 1024 + lane * 16) = t1[i];
      asm volatile("s_waitcnt lgkmcnt(0)" ::: "memory");
#pragma unroll
      for (int i = 0; i < 15; ++i) w1[i / 5][i % 5] = trfrag(i);
      asm volatile("s_waitcnt lgkmcnt(0)" ::: "memory");
#pragma unroll
      for (int i = 0; i < 10; ++i) *(u32x4*)(my + i * 1024 + lane * 16) = tg[i];
      asm volatile("s_waitcnt lgkmcnt(0)" ::: "memory");
#pragma unroll
      for (int i = 0; i < 10; ++i) wgl[i / 5][i % 5] = trfrag(i);
    }
    LBAR();
#pragma unroll 1
    for (int t = 0; t < 4; ++t) {
      xmma(0);
      xload(t + 1);
      LBAR();
      xstore(t, 0);
      LBAR();
    }
    xmma(1);
    xload(5);
    asm volatile("s_waitcnt vmcnt(0)" ::: "memory");
    LBAR();
    if (tid == 0) st128(rs, C_TOK + (unsigned)h * 128u + (unsigned)j * 16u, (u32x4){1u, 1u, 1u, 1u});
    LBAR();
    const unsigned m_off = (unsigned)(ml >> 3) * 512u + (unsigned)(g * 8 + (ml & 7)) * 16u;
    int r3 = 0;
    for (int s = 0; s < NSEG; ++s, r3 = r3 == 2 ? 0 : r3 + 1) {
      TS(0);
      const int r3p = r3 == 0 ? 2 : r3 - 1;
      f32x4 ac[3];
      if (s == 0) {
#pragma unroll
        for (int u = 0; u < 3; ++u) ac[u] = z4;
#pragma unroll
        for (int kk = 0; kk < 5; ++kk) {
#pragma unroll
          for (int u = 0; u < 3; ++u) ac[u] = mfma16(w1[u][kk], asb(mf0[kk]), ac[u]);
        }
      } else {
        const unsigned base = C_MEM + (unsigned)r3 * 32768u + (unsigned)(wave >> 1) * 16384u + (unsigned)(wave & 1) * 2560u + selA;
        SG(9); slp<SLP_A>(); SG(0);
        u32x4 mf[5];
        for (unsigned spin = 0;; ++spin) {
          asm volatile("" ::: "memory");
          mf[0] = ld128(rs, base + m_off); mf[2] = ld128(rs, base + m_off + 1024u); mf[4] = ld128(rs, base + 2048u + lo_off);
          const unsigned mx = umax3(umax3(mf[0][0], mf[0][2], mf[2][0]), mf[2][2], max(mf[4][0], mf[4][2]));
          if (__all(mx != 0xFFFFFFFFu) || spin >= spin_cap) { SPN(0); if (spin >= spin_cap) spin_cap = 0; break; }
        }
        mf[1] = ror8x4(mf[0]); mf[3] = ror8x4(mf[2]);
        SG(1);
#pragma unroll
        for (int u = 0; u < 3; ++u) ac[u] = z4;
#pragma unroll
        for (int kk = 0; kk < 5; ++kk) {
#pragma unroll
          for (int u = 0; u < 3; ++u) ac[u] = mfma16(w1[u][kk], asb(mf[kk]), ac[u]);
        }
      }
      TS(1);
#pragma unroll
      for (int u = 0; u < 3; ++u) X1[u * 256 + tid] = ac[u];
      LBAR();
      SG(2);
      if (wave < 2) {
        const int tl = 2 * wave + (ml >> 3);
        const f32x4* xp = X1 + (tl < 3 ? tl : 2) * 256 + g * 16 + (ml & 7);
        f32x4 a = (xp[0] + xp[64]) + (xp[128] + xp[192]);
        u32x2 v = {nosent(pack2(a[0], a[1])), nosent(pack2(a[2], a[3]))};
        const unsigned in = (unsigned)tl * 2048u + (unsigned)(((j >> 1) * 4 + 2 * (j & 1) + (g >> 1)) * 8 + (ml & 7)) * 16u + (g & 1) * 8u;
        if (tl < 3) {
          const unsigned off = C_QKV + (unsigned)(r3 * H + h) * 16384u + in;
          st64(rs, off + selA, v); SG(3); st64(rs, off + (8192u - selA), v);
          const unsigned offp = C_QKV + (unsigned)(r3p * H + h) * 16384u + in;
          st64(rs, offp, ones2); st64(rs, offp + 8192u, ones2);
        }
      }
      TS(2);
      if (s == NSEG - 1) break;
#define XSPOS 1
      if (s + 5 < NSEG) xmma(s & 1);
      if (XSPOS == 0 && s + 4 < NSEG) xstore(s + 4, (s + 1) & 1);
      if (s + 6 < NSEG) { if (glu) slp<SLP_X - 5>(); else slp<SLP_X>(); xload(s + 6); }
      LBAR();
      if (XSPOS == 1 && s + 4 < NSEG) xstore(s + 4, (s + 1) & 1);
      LBAR();
      if (XSPOS == 2 && s + 4 < NSEG) xstore(s + 4, (s + 1) & 1);
      SG(8);
    }
    return;
  }
  const int w4 = wave - 4;
  const bool three = w4 < 1;
  b16x8 w2[2][4];
  {
    u32x4 t2[8];
#pragma unroll
    for (int t = 0; t < 2; ++t) {
      const int ct = (t < 1 || three) ? w4 + 4 * t : 0;
#pragma unroll
      for (int i = 0; i < 4; ++i) t2[t * 4 + i] = *(const u32x4*)(Wo + (size_t)(h * E + 32 * i + (lane >> 1)) * D + 80 * j + 16 * ct + (lane & 1) * 8);
    }
#pragma unroll
    for (int i = 0; i < 8; ++i) *(u32x4*)(my + i * 1024 + lane * 16) = t2[i];
    asm volatile("s_waitcnt lgkmcnt(0)" ::: "memory");
#pragma unroll
    for (int i = 0; i < 8; ++i) w2[i / 4][i % 4] = trfrag(i);
  }
  b16x8 kx[2][4]; u32x4 vwx[2];
  const b16x8 ones8 = asb((u32x4){0x3F803F80u, 0x3F803F80u, 0x3F803F80u, 0x3F803F80u});
  auto prep = [&](int slot) {
    const char* kb = KV + slot * 16384 + lane * 16;
    b16x8 vf[2][4];
#pragma unroll
    for (int kt = 0; kt < 2; ++kt)
#pragma unroll
      for (int ks = 0; ks < 4; ++ks) { kx[kt][ks] = *(const b16x8*)(kb + (kt * 4 + ks) * 1024); vf[kt][ks] = *(const b16x8*)(kb + 8192 + (kt * 4 + ks) * 1024); }
#pragma unroll
    for (int t = 0; t < 2; ++t) {
      f32x4 d0 = z4, d1 = z4;
#pragma unroll
      for (int ks = 0; ks < 4; ++ks) { d0 = mfma16(vf[0][ks], w2[t][ks], d0); d1 = mfma16(vf[1][ks], w2[t][ks], d1); }
      vwx[t] = (u32x4){pack2(d0[0], d0[1]), pack2(d0[2], d0[3]), pack2(d1[0], d1[1]), pack2(d1[2], d1[3])};
    }
  };
  const unsigned kvs = (w4 < 2 ? (unsigned)OFF_KX : (unsigned)OFF_VX) + (unsigned)h * 8192u + (unsigned)(w4 & 1) * 4096u + (unsigned)lane * 16u;
  u32x4 stg[4];
  LBAR();
#pragma unroll 1
  for (int t = 0; t < 4; ++t) { LBAR(); LBAR(); }
  LBAR();
  for (unsigned spin = 0;; ++spin) {
    asm volatile("" ::: "memory");
    const u32x4 tk = ld128(rs, C_TOK + (unsigned)h * 128u + (unsigned)(lane & 7) * 16u);
    if (__all(tk[0] != 0xFFFFFFFFu) || spin >= spin_cap) { if (spin >= spin_cap) spin_cap = 0; break; }
    __builtin_amdgcn_s_sleep(4);
  }
  {
    u32x4 t8[8];
#pragma unroll
    for (int i = 0; i < 8; ++i) t8[i] = ld128(rw, kvs + (unsigned)(i >> 2) * (unsigned)(H * 8192) + (unsigned)(i & 3) * 1024u);
#pragma unroll
    for (int i = 0; i < 4; ++i) stg[i] = ld128(rw, kvs + 2u * (unsigned)(H * 8192) + (unsigned)i * 1024u);
#pragma unroll
    for (int i = 0; i < 8; ++i) *(u32x4*)(KV + (i >> 2) * 16384 + w4 * 4096 + (i & 3) * 1024 + lane * 16) = t8[i];
  }
  LBAR();
  prep(0);
  const int cc = lane & 7, hg = lane >> 3;
  unsigned poff[2];
#pragma unroll
  for (int t = 0; t < 2; ++t) {
    const int T = 5 * j + w4 + 4 * t;
    const unsigned c16 = (unsigned)(((T >> 1) * 4 + 2 * (T & 1) + (g >> 1)) * 8 + (ml & 7)) * 2u + (unsigned)(g & 1), rb = c16 / 5u;
    poff[t] = C_P + (rb / 3u) * 16384u + (rb % 3u) * 2560u + (unsigned)h * 80u + (c16 % 5u) * 16u;
  }
  const bool xat = (w4 == 1) && (j < XATN);
  const int xq = j & 1, xk0 = (j >> 1) * (8 / XATN);
  u32x4 qx[4], pxx, pmx;
  auto qxload = [&](int sn) {
#pragma unroll
    for (int ks = 0; ks < 4; ++ks) qx[ks] = ld128(rw, (unsigned)OFF_QX + (unsigned)(sn * H + h) * 8192u + (unsigned)(xq * 4 + ks) * 1024u + (unsigned)lane * 16u);
  };
  auto xattn1 = [&](const u32x4 (&kv)[4]) {
#pragma unroll
    for (int ks = 0; ks < 4; ++ks) *(u32x4*)(VMS + ks * 1024 + lane * 16) = kv[ks];
    f32x4 s0 = z4, s1 = z4, sm = z4;
#pragma unroll
    for (int ks = 0; ks < 4; ++ks) { s0 = mfma16(kx[0][ks], asb(qx[ks]), s0); s1 = mfma16(kx[1][ks], asb(qx[ks]), s1); sm = mfma16(asb(kv[ks]), asb(qx[ks]), sm); }
    const int qi = 16 * xq + ml;
    float sv[12];
#pragma unroll
    for (int r = 0; r < 4; ++r) {
      sv[r] = (4 * g + r <= qi) ? rbf(rbf(s0[r]) * SCALE) : -INFINITY;
      sv[4 + r] = (16 + 4 * g + r <= qi) ? rbf(rbf(s1[r]) * SCALE) : -INFINITY;
      sv[8 + r] = (g < 2) ? rbf(rbf(sm[r]) * SCALE) : -INFINITY;
    }
    float mxv = sv[0];
#pragma unroll
    for (int r = 1; r < 12; ++r) mxv = fmaxf(mxv, sv[r]);
    mxv = xmax32(xmax16(mxv));
    float sum = 0.f;
#pragma unroll
    for (int r = 0; r < 12; ++r) { sv[r] = __expf(sv[r] - mxv); sum += sv[r]; }
    sum = xsum32(xsum16(sum));
    const float inv = 1.f / sum;
    pxx = (u32x4){pack2(sv[0] * inv, sv[1] * inv), pack2(sv[2] * inv, sv[3] * inv), pack2(sv[4] * inv, sv[5] * inv), pack2(sv[6] * inv, sv[7] * inv)};
    pmx = (u32x4){pack2(sv[8] * inv, sv[9] * inv), pack2(sv[10] * inv, sv[11] * inv), 0u, 0u};
  };
  auto xattn2 = [&](int sn, int slot) {
    const unsigned q4 = (unsigned)ml >> 2, p4 = (unsigned)lane & 3u;
    const unsigned vb = (unsigned)(size_t)(KV + slot * 16384 + 8192) + (4u * g + q4) * 16u + (p4 >> 1) * 256u + (p4 & 1u) * 8u;
    const unsigned mb = (unsigned)(size_t)VMS + (8u + 4u * (g & 1) + q4) * 16u + (p4 >> 1) * 256u + (p4 & 1u) * 8u;
#pragma unroll
    for (int kq = 0; kq < 8 / XATN; ++kq) {
      const int kk = xk0 + kq;
      u32x4 of;
#pragma unroll
      for (int hf = 0; hf < 2; ++hf) {
        const unsigned eo = kk * 1024u + hf * 512u;
        s16x4 v0 = __builtin_amdgcn_ds_read_tr16_b64_v4i16((LAS s16x4*)(vb + eo));
        s16x4 v1 = __builtin_amdgcn_ds_read_tr16_b64_v4i16((LAS s16x4*)(vb + 4096u + eo));
        s16x4 v2 = __builtin_amdgcn_ds_read_tr16_b64_v4i16((LAS s16x4*)(mb + eo));
        s16x8 va = {v0[0], v0[1], v0[2], v0[3], v1[0], v1[1], v1[2], v1[3]};
        s16x8 vm8 = {v2[0], v2[1], v2[2], v2[3], 0, 0, 0, 0};
        f32x4 o = mfma16(__builtin_bit_cast(b16x8, va), asb(pxx), z4);
        o = mfma16(__builtin_bit_cast(b16x8, vm8), asb(pmx), o);
        of[2 * hf] = pack2(o[0], o[1]); of[2 * hf + 1] = pack2(o[2], o[3]);
      }
      *(u32x4*)(ws + OFF_O + (size_t)((sn * 2 + xq) * 128 + h * 4 + kk) * 1024 + lane * 16) = of;
    }
  };
  if (xat) qxload(0);
  const unsigned q_off = (unsigned)(ml >> 3) * 512u + (unsigned)(g * 8 + (ml & 7)) * 16u, kv_off = 2048u + (unsigned)(ml >> 3) * 2048u + (unsigned)(g * 8 + (ml & 7)) * 16u;
  int r3 = 0;
  for (int s = 0; s < NSEG - 1; ++s, r3 = r3 == 2 ? 0 : r3 + 1) {
    const unsigned hb = C_QKV + (unsigned)(r3 * H + h) * 16384u + selA;
    SG(9);
    LBAR();
    SG(0);
    slp<SLP_B>(); SG(1);
    f32x4 a[2];
    u32x4 qf[4], kv[4];
    for (unsigned spin = 0;; ++spin) {
      asm volatile("" ::: "memory");
      qf[0] = ld128(rs, hb + q_off); qf[2] = ld128(rs, hb + q_off + 1024u);
#pragma unroll
      for (int ks = 0; ks < 4; ++ks) kv[ks] = ld128(rs, hb + kv_off + ks * 512u);
      unsigned mx = umax3(umax3(qf[0][0], qf[0][2], qf[2][0]), qf[2][2], 0u);
#pragma unroll
      for (int ks = 0; ks < 4; ++ks) mx = umax3(mx, kv[ks][0], kv[ks][2]);
      if (__all(mx != 0xFFFFFFFFu) || spin >= spin_cap) { SPN(1); if (spin >= spin_cap) spin_cap = 0; break; }
    }
    qf[1] = ror8x4(qf[0]); qf[3] = ror8x4(qf[2]);
    SG(2);
    {
#define SB() __builtin_amdgcn_sched_barrier(0)
      f32x4 s0 = z4, s1 = z4, sm = z4, dm[2] = {z4, z4};
#pragma unroll
      for (int ks = 0; ks < 4; ++ks) { s0 = mfma16(kx[0][ks], asb(qf[ks]), s0); s1 = mfma16(kx[1][ks], asb(qf[ks]), s1); SB(); }
#pragma unroll
      for (int ks = 0; ks < 4; ++ks) { sm = mfma16(asb(kv[ks]), asb(qf[ks]), sm); SB(); }
      TS(3);
      const float NEG = -INFINITY;
      const float c2 = SCALE * 1.4426950408889634f;
      float tv[12];
      dm[0] = mfma16(asb(kv[0]), w2[0][0], dm[0]);
#pragma unroll
      for (int r = 0; r < 4; ++r) tv[r] = s0[r] * c2;
      SB();
      dm[1] = mfma16(asb(kv[0]), w2[1][0], dm[1]);
#pragma unroll
      for (int r = 0; r < 4; ++r) tv[4 + r] = s1[r] * c2;
      SB();
      float m1 = fmaxf(fmaxf(tv[0], tv[1]), tv[2]), m2 = fmaxf(fmaxf(tv[3], tv[4]), tv[5]);
      m1 = fmaxf(fmaxf(m1, tv[6]), tv[7]);
      SB();
      dm[0] = mfma16(asb(kv[1]), w2[0][1], dm[0]);
#pragma unroll
      for (int r = 0; r < 4; ++r) tv[8 + r] = (g < 2) ? sm[r] * c2 : NEG;
      SB();
      dm[1] = mfma16(asb(kv[1]), w2[1][1], dm[1]);
      m2 = fmaxf(fmaxf(m2, tv[8]), tv[9]); m1 = fmaxf(fmaxf(m1, tv[10]), tv[11]);
      float mxv = fmaxf(m1, m2);
      { unsigned u = __float_as_uint(mxv); auto r = __builtin_amdgcn_permlane16_swap(u, u, false, false); mxv = fmaxf(__uint_as_float(r[0]) + 0.f, __uint_as_float(r[1]) + 0.f); }
      SB();
      { unsigned u = __float_as_uint(mxv); auto r = __builtin_amdgcn_permlane32_swap(u, u, false, false); mxv = fmaxf(__uint_as_float(r[0]) + 0.f, __uint_as_float(r[1]) + 0.f); }
      SB();
      dm[0] = mfma16(asb(kv[2]), w2[0][2], dm[0]);
#pragma unroll
      for (int r = 0; r < 2; ++r) tv[r] = __builtin_amdgcn_exp2f(tv[r] - mxv);
      SB();
      dm[1] = mfma16(asb(kv[2]), w2[1][2], dm[1]);
#pragma unroll
      for (int r = 2; r < 4; ++r) tv[r] = __builtin_amdgcn_exp2f(tv[r] - mxv);
      SB();
#pragma unroll
      for (int r = 4; r < 6; ++r) tv[r] = __builtin_amdgcn_exp2f(tv[r] - mxv);
      SB();
      dm[0] = mfma16(asb(kv[3]), w2[0][3], dm[0]);
#pragma unroll
      for (int r = 6; r < 8; ++r) tv[r] = __builtin_amdgcn_exp2f(tv[r] - mxv);
      SB();
      dm[1] = mfma16(asb(kv[3]), w2[1][3], dm[1]);
#pragma unroll
      for (int r = 8; r < 10; ++r) tv[r] = __builtin_amdgcn_exp2f(tv[r] - mxv);
      SB();
#pragma unroll
      for (int r = 10; r < 12; ++r) tv[r] = __builtin_amdgcn_exp2f(tv[r] - mxv);
      u32x4 px = {pack2(tv[0], tv[1]), pack2(tv[2], tv[3]), pack2(tv[4], tv[5]), pack2(tv[6], tv[7])};
      SB();
      f32x4 sx = mfma16(ones8, asb(px), z4);
      a[0] = mfma16(asb(vwx[0]), asb(px), z4);
      a[1] = mfma16(asb(vwx[1]), asb(px), z4);
      u32x4 pm;
      { const unsigned p0 = pack2(tv[8], tv[9]), p1 = pack2(tv[10], tv[11]);
        auto r0 = __builtin_amdgcn_permlane32_swap(0u, p0, false, false); auto r1 = __builtin_amdgcn_permlane32_swap(0u, p1, false, false);
        pm = (u32x4){r0[0], r1[0], 0u, 0u}; }
      SB();
      sx = mfma16(ones8, asb(pm), sx);
#pragma unroll
      for (int t = 0; t < 2; ++t) {
        u32x4 vwm = {g >= 2 ? pack2(dm[t][0], dm[t][1]) : 0u, g >= 2 ? pack2(dm[t][2], dm[t][3]) : 0u, 0u, 0u};
        a[t] = mfma16(asb(vwm), asb(pm), a[t]);
      }
      SB();
      const float inv = __builtin_amdgcn_rcpf(sx[0]);
      a[0] *= inv; a[1] *= inv;
    }
#pragma unroll
    for (int t = 0; t < 2; ++t) {
      if (t == 1 && !three) break;
      u32x4 v;
#pragma unroll
      for (int r = 0; r < 4; ++r) v[r] = min(__float_as_uint(a[t][r]), 0xFFFFFFFEu);
      const unsigned off = (unsigned)(1 + (s & 1)) * PAGE + poff[t];
      if (ml < 8) { st128(rs, off, v); st128(rs, off + 8192u, v); }
    }
    SG(3);
    TS(4);
    LBAR();
    SG(4);
    const int r3n = r3 == 2 ? 0 : r3 + 1, r3nn = r3 == 0 ? 2 : r3 - 1;
    auto kvwrite = [&] {
      if (s + 2 < NSEG) {
#pragma unroll
        for (int i = 0; i < 4; ++i) *(u32x4*)(KV + r3nn * 16384 + w4 * 4096 + i * 1024 + lane * 16) = stg[i];
      }
    };
    if (w4 == 3) {
      kvwrite();
        const unsigned offw = (unsigned)(1 + (s & 1)) * PAGE + C_P + (unsigned)(cb / 3) * 16384u + (unsigned)(cb % 3) * 2560u + (unsigned)hg * 320u + (unsigned)cc * 16u;
        const unsigned off = cc < 5 ? offw + ((s & 1) ? selP1 : selP0) : OOB;
        slp<SLP_C>(); SG(5);
        u32x4 v[4];
        for (unsigned spin = 0;; ++spin) {
          asm volatile("" ::: "memory");
#pragma unroll
          for (int i = 0; i < 4; ++i) v[i] = ld128(rs, off + i * 80u);
          unsigned mx = 0;
#pragma unroll
          for (int i = 0; i < 4; ++i) { mx = umax3(mx, v[i][0], v[i][1]); mx = umax3(mx, v[i][2], v[i][3]); }
          if (__all(mx != 0xFFFFFFFFu) || spin >= spin_cap) { SPN(2); if (spin >= spin_cap) spin_cap = 0; break; }
        }
        SG(6);
        TS(5);
        float f[4];
#pragma unroll
        for (int r = 0; r < 4; ++r) {
          float x = (__uint_as_float(v[0][r]) + __uint_as_float(v[1][r])) + (__uint_as_float(v[2][r]) + __uint_as_float(v[3][r]));
          x += ror8(x); x = xsum32(xsum16(x)); f[r] = x;
        }
        u32x2 w = {nosent(pack2(f[0], f[1])), nosent(pack2(f[2], f[3]))};
        const unsigned mq = (unsigned)cb >> 6, min_ = (mq >> 1) * 16384u + (mq & 1) * 2560u + (unsigned)(5 * (cb & 63) + lane) * 8u;
        if (lane < 5) { st64(rs, C_MEM + (unsigned)r3n * 32768u + min_, w); st64(rs, C_MEM + (unsigned)r3n * 32768u + 8192u + min_, w); }
        SG(7);
        TS(6);
        LBAR();
        if (cc < 5) {
#pragma unroll
          for (int i = 0; i < 4; ++i) { st128(rs, offw + i * 80u, ones4); st128(rs, offw + 8192u + i * 80u, ones4); }
        }
        if (lane < 5) { st64(rs, C_MEM + (unsigned)r3 * 32768u + min_, ones2); st64(rs, C_MEM + (unsigned)r3 * 32768u + 8192u + min_, ones2); }
      prep(r3n);
    } else if (xat) {
      kvwrite();
      xattn1(kv);
      LBAR();
      xattn2(s, r3);
      prep(r3n);
      qxload(s + 1);
    } else {
      prep(r3n);
      kvwrite();
      LBAR();
    }
    if (s + 3 < NSEG) {
#pragma unroll
      for (int i = 0; i < 4; ++i) stg[i] = ld128(rw, kvs + (unsigned)(s + 3) * (unsigned)(H * 8192) + (unsigned)i * 1024u);
    }
  }
  LBAR();
  if (xat) {
    const unsigned hb = C_QKV + (unsigned)(r3 * H + h) * 16384u + selA;
    slp<SLP_B>();
    u32x4 kv[4];
    for (unsigned spin = 0;; ++spin) {
      asm volatile("" ::: "memory");
      unsigned mx = 0;
#pragma unroll
      for (int ks = 0; ks < 4; ++ks) { kv[ks] = ld128(rs, hb + kv_off + ks * 512u); mx = umax3(mx, kv[ks][0], kv[ks][2]); }
      if (__all(mx != 0xFFFFFFFFu) || spin >= spin_cap) break;
    }
    xattn1(kv);
    asm volatile("s_waitcnt lgkmcnt(0)" ::: "memory");
    xattn2(NSEG - 1, r3);
  }
}

__global__ __launch_bounds__(512, 1) void k_chain(const u16* __restrict__ mem0, const u16* __restrict__ Wq, const u16* __restrict__ Wk,
                                                   const u16* __restrict__ Wv, const u16* __restrict__ Wo, const u16* __restrict__ wg,
                                                   const u16* __restrict__ wu, char* __restrict__ ws) {
  extern __shared__ __attribute__((aligned(16))) char dyn_lds[];
  chain_role(blockIdx.x, mem0, Wq, Wk, Wv, Wo, wg, wu, ws, dyn_lds);
}

__global__ __launch_bounds__(512, 1) void k_post2(const char* __restrict__ ws, u16* __restrict__ out) {
  extern __shared__ __attribute__((aligned(16))) char lds[];
  const int tid = threadIdx.x, lane = tid & 63, wave = __builtin_amdgcn_readfirstlane(tid >> 6), ml = lane & 15, g = lane >> 4;
  const int b = blockIdx.x, xc = b & 7, idx = b >> 3, tt = xc * 4 + (idx >> 3), cgp = idx & 7;
  f32x4* X = (f32x4*)lds;
  f32x4 asum[2][2];
#pragma unroll
  for (int ph = 0; ph < 2; ++ph) {
    const int kt = ph == 0 ? 128 : 54, nk = ph == 0 ? 16 : (wave < 6 ? 7 : 6);
    const char* tb = ws + (ph ? OFF_HH : OFF_O) + (size_t)(tt * 4) * kt * 1024 + (size_t)wave * 1024 + lane * 16;
    const size_t tstr = (size_t)kt * 1024;
    const char* wb = ws + OFF_WF + ((size_t)(cgp * 5) * 182 + (ph ? 128 : 0) + wave) * 1024 + lane * 16;
    f32x4 acc[4][5];
#pragma unroll
    for (int i = 0; i < 4; ++i)
#pragma unroll
      for (int c = 0; c < 5; ++c) acc[i][c] = (f32x4){0, 0, 0, 0};
    b16x8 tf[4][4], wf[4][5];
    auto ldk = [&](int slot, int k) {
      const int kn = k < nk ? k : nk - 1;
#pragma unroll
      for (int i = 0; i < 4; ++i) tf[slot][i] = *(const b16x8*)(tb + i * tstr + (size_t)kn * 8192);
#pragma unroll
      for (int c = 0; c < 5; ++c) wf[slot][c] = *(const b16x8*)(wb + (size_t)c * 182 * 1024 + (size_t)kn * 8192);
    };
    ldk(0, 0); ldk(1, 1); ldk(2, 2);
    for (int k = 0; k < nk; k += 4) {
#pragma unroll
      for (int u = 0; u < 4; ++u) {
        ldk((u + 3) & 3, k + u + 3);
        if (k + u < nk) {
#pragma unroll
          for (int i = 0; i < 4; ++i)
#pragma unroll
            for (int c = 0; c < 5; ++c) acc[i][c] = mfma16(wf[u][c], tf[u][i], acc[i][c]);
        }
      }
    }
#pragma unroll
    for (int rd = 0; rd < 2; ++rd) {
#pragma unroll
      for (int t = 0; t < 10; ++t) { const int tl = rd * 10 + t; X[(wave * 10 + t) * 64 + lane] = acc[tl / 5][tl % 5]; }
      RBAR();
#pragma unroll
      for (int u = 0; u < 2; ++u) {
        const int t = wave + 8 * u;
        if (t < 10) {
          const f32x4* xp = X + t * 64 + lane;
          const f32x4 v = ((xp[0] + xp[640]) + (xp[1280] + xp[1920])) + ((xp[2560] + xp[3200]) + (xp[3840] + xp[4480]));
          if (ph == 0) asum[rd][u] = v;
          else {
            const int tl = rd * 10 + t, i = tl / 5, c = tl % 5;
            const int tok = tt * 64 + 16 * i + ml;
            const float gg = ((const float*)(ws + OFF_G))[tok], og = rbf(1.f - gg);
            float r[4];
#pragma unroll
            for (int q = 0; q < 4; ++q) { float ta = rbf(gg * rbf(asum[rd][u][q])), tb2 = rbf(og * rbf(v[q])); asm volatile("" : "+v"(ta), "+v"(tb2)); r[q] = ta + tb2; }
            *(u32x2*)(out + (size_t)tok * D + 80 * cgp + 16 * c + 4 * g) = (u32x2){pack2(r[0], r[1]), pack2(r[2], r[3])};
          }
        }
      }
      RBAR();
    }
  }
}

extern "C" void kernel_launch(void* const* d_in, const int* in_sizes, int n_in, void* d_out, int out_size, void* d_ws, size_t ws_size,
                              hipStream_t stream) {
  const u16* x = (const u16*)d_in[0];
  const u16* mem0 = (const u16*)d_in[1];
  const u16* Wq = (const u16*)d_in[2];
  const u16* Wk = (const u16*)d_in[3];
  const u16* Wv = (const u16*)d_in[4];
  const u16* Wo = (const u16*)d_in[5];
  const u16* wg = (const u16*)d_in[8];
  const u16* wu = (const u16*)d_in[9];
  const u16* wd = (const u16*)d_in[10];
  const u16* gw = (const u16*)d_in[11];
  char* ws = (char*)d_ws;
  k_init<<<NB_INIT1 + 512, 256, 0, stream>>>(x, Wo, wd, gw, ws);
  hipFuncSetAttribute((const void*)k_chain, hipFuncAttributeMaxDynamicSharedMemorySize, 156 * 1024);
  k_chain<<<256, 512, 156 * 1024, stream>>>(mem0, Wq, Wk, Wv, Wo, wg, wu, ws);
  hipFuncSetAttribute((const void*)k_post2, hipFuncAttributeMaxDynamicSharedMemorySize, 80 * 1024);
  k_post2<<<256, 512, 80 * 1024, stream>>>(ws, (u16*)d_out);
}
```

```cpp
#include <hip/hip_runtime.h>
#include <hip/hip_bf16.h>
#include <stdint.h>
#include <stdio.h>

typedef unsigned short u16;
typedef __bf16 b16x8 __attribute__((ext_vector_type(8)));
typedef short s16x4 __attribute__((ext_vector_type(4)));
typedef float f32x4 __attribute__((ext_vector_type(4)));
typedef unsigned u32x4 __attribute__((ext_vector_type(4)));
typedef unsigned u32x2 __attribute__((ext_vector_type(2)));
#define LAS __attribute__((address_space(3)))
#define DI __device__ __forceinline__

constexpr int S = 2048, D = 640, H = 32, E = 128, HE = 4096, MM = 8, SEG = 32, NSEG = 64, F = 1728;
constexpr float SCALE = 0.08838834764831845f;

constexpr size_t OFF_KX = 0;
constexpr size_t OFF_VX = OFF_KX + (size_t)S * HE * 2;
constexpr size_t OFF_QX = OFF_VX + (size_t)S * HE * 2;
constexpr size_t OFF_O  = OFF_QX + (size_t)S * HE * 2;
constexpr size_t OFF_HH = OFF_O + (size_t)S * HE * 2;
constexpr size_t OFF_WF = OFF_HH + (size_t)S * F * 2;
constexpr size_t OFF_XF = OFF_WF + (size_t)40 * 182 * 1024;
constexpr size_t OFF_G  = OFF_XF + (size_t)S * D * 2;
constexpr size_t OFF_QM = OFF_G + (size_t)S * 4;
constexpr size_t OFF_COMM = OFF_QM + (size_t)NSEG * H * 6144;
constexpr size_t OFF_END = OFF_COMM + (size_t)8 * 1048576;
constexpr size_t OFF_DBG = OFF_END;
constexpr unsigned PAGE = 2097152u, C_MEM = 65536u, C_TOK = 180224u, C_QKV = 196608u, C_P = 65536u;
__host__ __device__ inline char* comm_base(char* ws) { return (char*)(((size_t)(ws + OFF_COMM) + (PAGE - 1)) & ~(size_t)(PAGE - 1)); }

DI float bf2f(u16 v) { return __builtin_bit_cast(float, (unsigned)v << 16); }
DI u16 f2bf(float f) { return __builtin_bit_cast(u16, (__bf16)f); }
DI float rbf(float f) { return (float)(__bf16)f; }
typedef float f32x2 __attribute__((ext_vector_type(2)));
typedef __bf16 b16x2 __attribute__((ext_vector_type(2)));
DI unsigned pack2(float lo, float hi) { f32x2 v = {lo, hi}; return __builtin_bit_cast(unsigned, __builtin_convertvector(v, b16x2)); }
DI f32x4 mfma16(b16x8 a, b16x8 b, f32x4 c) { return __builtin_amdgcn_mfma_f32_16x16x32_bf16(a, b, c, 0, 0, 0); }
DI b16x8 asb(u32x4 v) { return __builtin_bit_cast(b16x8, v); }

DI float gelu_tanh(float x) {
  float y = 0.7978845608028654f * (x + 0.044715f * x * x * x);
  float t = 1.f - 2.f / (__expf(2.f * y) + 1.f);
  return 0.5f * x * (1.f + t);
}

#define RBAR() asm volatile("s_waitcnt lgkmcnt(0)\n\ts_barrier" ::: "memory")

constexpr unsigned NX_INIT = S * D / 8, NF_INIT = (2 * PAGE + C_P + 86 * 16384) / 16, NB_INIT1 = (NX_INIT + NF_INIT + 255) / 256;
__global__ __launch_bounds__(256) void k_init(const u16* __restrict__ x, const u16* __restrict__ Wo, const u16* __restrict__ wd,
                                              const u16* __restrict__ gate_w, char* __restrict__ ws) {
  __shared__ __attribute__((aligned(16))) char lds[4 * 8192];
  if (blockIdx.x >= 512) {
    const unsigned i = (blockIdx.x - 512) * 256 + threadIdx.x;
    if (i < NF_INIT) *(u32x4*)(comm_base(ws) + (size_t)i * 16) = (u32x4){0xFFFFFFFFu, 0xFFFFFFFFu, 0xFFFFFFFFu, 0xFFFFFFFFu};
    else if (i - NF_INIT < NX_INIT) {
      const unsigned k = i - NF_INIT, lane = k & 63, f = k >> 6, kk = f % 20, t16 = f / 20;
      *(u32x4*)(ws + OFF_XF + (size_t)k * 16) = *(const u32x4*)(x + (size_t)(16 * t16 + (lane & 15)) * D + 32 * kk + 8 * (lane >> 4));
    }
    return;
  }
  const int lane = threadIdx.x & 63, wave = __builtin_amdgcn_readfirstlane(threadIdx.x >> 6), ml = lane & 15, g = lane >> 4;
  const int gw = blockIdx.x * 4 + wave;
  char* V = lds + wave * 8192;
  {
    for (int t = gw; t < S; t += 2048) {
      float s = 0.f;
#pragma unroll
      for (int i = 0; i < 5; ++i) {
        const unsigned xv = *(const unsigned*)(x + (size_t)t * D + i * 128 + lane * 2), gv = *(const unsigned*)(gate_w + i * 128 + lane * 2);
        s += rbf(__uint_as_float(xv << 16) * __uint_as_float(gv << 16)) + rbf(__uint_as_float(xv & 0xFFFF0000u) * __uint_as_float(gv & 0xFFFF0000u));
      }
#pragma unroll
      for (int o = 32; o >= 1; o >>= 1) s += __shfl_xor(s, o);
      s = rbf(s);
      float gg = rbf(1.f / (1.f + __expf(-s)));
      if (lane == 0) ((float*)(ws + OFF_G))[t] = gg;
    }
    char* my = V;
    const unsigned trb = (unsigned)(size_t)my + (4 * g + (ml >> 2)) * 32 + (lane & 3) * 8;
    for (int u = gw; u < 182 * 5; u += 2048) {
      const int kk = u / 5, c0 = (u % 5) * 8;
      const u16* W = kk < 128 ? Wo + (size_t)32 * kk * D : wd + (size_t)32 * (kk - 128) * D;
      u32x4 t8[8];
#pragma unroll
      for (int i = 0; i < 8; ++i) t8[i] = *(const u32x4*)(W + (size_t)(4 * i + (lane >> 4)) * D + 16 * c0 + (lane & 15) * 8);
#pragma unroll
      for (int i = 0; i < 8; ++i) *(u32x4*)(my + ((lane & 15) >> 1) * 1024 + (4 * i + (lane >> 4)) * 32 + (lane & 1) * 16) = t8[i];
      asm volatile("s_waitcnt lgkmcnt(0)" ::: "memory");
#pragma unroll
      for (int i = 0; i < 8; ++i) {
        s16x4 lo = __builtin_amdgcn_ds_read_tr16_b64_v4i16((LAS s16x4*)(trb + i * 1024));
        s16x4 hi = __builtin_amdgcn_ds_read_tr16_b64_v4i16((LAS s16x4*)(trb + i * 1024 + 512));
        typedef short s16x8 __attribute__((ext_vector_type(8)));
        s16x8 tt = {lo[0], lo[1], lo[2], lo[3], hi[0], hi[1], hi[2], hi[3]};
        *(s16x8*)(ws + OFF_WF + ((size_t)(c0 + i) * 182 + kk) * 1024 + lane * 16) = tt;
      }
      asm volatile("s_waitcnt lgkmcnt(0)" ::: "memory");
    }
  }
}

#define SC1 16
DI u32x4 ld128(__amdgpu_buffer_rsrc_t rs, unsigned off) { return __builtin_amdgcn_raw_buffer_load_b128(rs, off, 0, SC1); }
DI u32x2 ld64(__amdgpu_buffer_rsrc_t rs, unsigned off) { return __builtin_amdgcn_raw_buffer_load_b64(rs, off, 0, SC1); }
DI void st128(__amdgpu_buffer_rsrc_t rs, unsigned off, u32x4 v) { __builtin_amdgcn_raw_buffer_store_b128(v, rs, off, 0, SC1); }
DI void st64(__amdgpu_buffer_rsrc_t rs, unsigned off, u32x2 v) { __builtin_amdgcn_raw_buffer_store_b64(v, rs, off, 0, SC1); }
DI unsigned nosent(unsigned v) { return v == 0xFFFFFFFFu ? 0xFFFEFFFFu : v; }
DI unsigned umax3(unsigned a, unsigned b, unsigned c) { return max(max(a, b), c); }
DI float xsum16(float x) { unsigned u = __float_as_uint(x); auto r = __builtin_amdgcn_permlane16_swap(u, u, false, false); return __uint_as_float(r[0]) + __uint_as_float(r[1]); }
DI float xsum32(float x) { unsigned u = __float_as_uint(x); auto r = __builtin_amdgcn_permlane32_swap(u, u, false, false); return __uint_as_float(r[0]) + __uint_as_float(r[1]); }
DI float xmax16(float x) { unsigned u = __float_as_uint(x); auto r = __builtin_amdgcn_permlane16_swap(u, u, false, false); return fmaxf(__uint_as_float(r[0]), __uint_as_float(r[1])); }
DI float xmax32(float x) { unsigned u = __float_as_uint(x); auto r = __builtin_amdgcn_permlane32_swap(u, u, false, false); return fmaxf(__uint_as_float(r[0]), __uint_as_float(r[1])); }
DI float ror4(float x) { return __uint_as_float(__builtin_amdgcn_update_dpp(0u, __float_as_uint(x), 0x124, 0xf, 0xf, false)); }
DI float ror8(float x) { return __uint_as_float(__builtin_amdgcn_update_dpp(0u, __float_as_uint(x), 0x128, 0xf, 0xf, false)); }
DI u32x4 ror8x4(u32x4 v) { u32x4 r; for (int i = 0; i < 4; ++i) r[i] = __builtin_amdgcn_update_dpp(0u, v[i], 0x128, 0xf, 0xf, false); return r; }
template <int N> struct IC { static constexpr int value = N; };
#define LBAR() asm volatile("s_waitcnt lgkmcnt(0)\n\ts_barrier" ::: "memory")
template <int N> DI void slp() { if constexpr (N > 0) { __builtin_amdgcn_s_sleep(N > 15 ? 15 : N); if constexpr (N > 15) slp<N - 15>(); } }

#define SG(i)
#define TS(i)
#define SPN(k)
constexpr unsigned SPIN_CAP = 1u << 17;
#define SLP_A 16
#define SLP_B 14
#define SLP_B3 4
#define XK1 5
#define XK2 5
#define XATN 8
#define XFLAG 0
#define SLP_X 21
#define SLP_C 11

DI void chain_role(const int cb, const u16* __restrict__ mem0, const u16* __restrict__ Wq, const u16* __restrict__ Wk,
                   const u16* __restrict__ Wv, const u16* __restrict__ Wo, const u16* __restrict__ wg, const u16* __restrict__ wu,
                   char* __restrict__ ws, char* dyn_lds) {
  const int xcd = cb & 7, y = cb >> 3, h = xcd * 4 + (y >> 3), j = y & 7;
  const int tid = threadIdx.x, lane = tid & 63, wave = __builtin_amdgcn_readfirstlane(tid >> 6);
  const int ml = lane & 15, g = lane >> 4;
  const __amdgpu_buffer_rsrc_t rs = __builtin_amdgcn_make_buffer_rsrc(comm_base(ws), 0, (int)(3 * PAGE), 0x00020000);
  unsigned selA, selP0, selP1;
  {
    unsigned* pl = (unsigned*)(dyn_lds + 156 * 1024 - 64);
    if (tid == 64) *(volatile LAS unsigned*)(dyn_lds + 156 * 1024 - 32) = 0u;
    if (wave == 0) {
      {
        const unsigned v = __builtin_amdgcn_raw_buffer_load_b32(rs, (unsigned)(lane < 3 ? lane : 0) * PAGE + (PAGE - 4096u), 0, SC1);
        asm volatile("s_waitcnt vmcnt(0)" :: "v"(v) : "memory");
      }
#pragma unroll 1
      for (int p = 0; p < 4; ++p) {
        const unsigned po = (unsigned)(p < 2 ? 0 : p - 1) * PAGE + (unsigned)(cb >> 6) * 16384u + (unsigned)(p == 1) * 8192u + (unsigned)(cb & 63) * 128u;
        asm volatile("s_waitcnt vmcnt(0) lgkmcnt(0)" ::: "memory");
        const unsigned long long t0 = __builtin_amdgcn_s_memtime();
        asm volatile("s_waitcnt lgkmcnt(0)" ::: "memory");
        const unsigned v = __builtin_amdgcn_raw_buffer_load_b32(rs, po, 0, SC1);
        asm volatile("s_waitcnt vmcnt(0)" :: "v"(v) : "memory");
        const unsigned long long t1 = __builtin_amdgcn_s_memtime();
        if (lane == 0) pl[p] = (unsigned)(t1 - t0);
      }
    }
    LBAR();
    selA = __builtin_amdgcn_readfirstlane(pl[1] < pl[0] ? 8192u : 0u);
    selP0 = __builtin_amdgcn_readfirstlane(2 * pl[2] > pl[0] + pl[1] ? 8192u : 0u);
    selP1 = __builtin_amdgcn_readfirstlane(2 * pl[3] > pl[0] + pl[1] ? 8192u : 0u);
  }
  volatile LAS unsigned* FLG = (volatile LAS unsigned*)(dyn_lds + 156 * 1024 - 32);
  const u32x4 ones4 = {0xFFFFFFFFu, 0xFFFFFFFFu, 0xFFFFFFFFu, 0xFFFFFFFFu};
  const u32x2 ones2 = {0xFFFFFFFFu, 0xFFFFFFFFu};
  f32x4* X1 = (f32x4*)dyn_lds;
  f32x4* X2 = (f32x4*)(dyn_lds + 61440);
  const __amdgpu_buffer_rsrc_t rw = __builtin_amdgcn_make_buffer_rsrc(ws, 0, (int)OFF_O, 0x00020000);
  char* KV = dyn_lds + 12288;
  char* VMS = dyn_lds + 143360;
  char* WL = dyn_lds + 61440;
  const f32x4 z4 = {0, 0, 0, 0};
  const unsigned OOB = 0x7FFFFF00u;
  const unsigned lo_off = ml < 8 ? (unsigned)(g * 8 + ml) * 16u : OOB;
  unsigned spin_cap = SPIN_CAP;
  typedef short s16x8 __attribute__((ext_vector_type(8)));
  char* my = WL + (wave < 4 ? wave * 15360 : 61440 + (wave - 4) * 8192);
  const unsigned trb = (unsigned)(size_t)my + (8 * g + (ml >> 2)) * 32 + (lane & 3) * 8;
  auto trfrag = [&](int i) {
    s16x4 lo = __builtin_amdgcn_ds_read_tr16_b64_v4i16((LAS s16x4*)(trb + i * 1024));
    s16x4 hi = __builtin_amdgcn_ds_read_tr16_b64_v4i16((LAS s16x4*)(trb + i * 1024 + 128));
    s16x8 tt = {lo[0], lo[1], lo[2], lo[3], hi[0], hi[1], hi[2], hi[3]};
    return __builtin_bit_cast(b16x8, tt);
  };

  if (wave < 4) {
    b16x8 w1[3][5], wgl[2][5];
    const bool glu = cb < 108;
    u32x4 mf0[5];
    u32x4 xf[2][5];
    auto xloadp = [&](int t, auto k0c, auto k1c) {
      constexpr int k0 = decltype(k0c)::value, k1 = decltype(k1c)::value;
#pragma unroll
      for (int qt = 0; qt < 2; ++qt)
#pragma unroll
        for (int kk = k0; kk < k1; ++kk) xf[qt][kk] = *(const u32x4*)(ws + OFF_XF + (size_t)((2 * t + qt) * 20 + 5 * wave + kk) * 1024 + lane * 16);
    };
    auto xload = [&](int t) { xloadp(t, IC<0>{}, IC<5>{}); };
    auto xmma = [&](int pb) {
#pragma unroll
      for (int u = 0; u < 3; ++u)
#pragma unroll
        for (int qt = 0; qt < 2; ++qt) {
          f32x4 a = z4;
#pragma unroll
          for (int kk = 0; kk < 5; ++kk) a = mfma16(w1[u][kk], asb(xf[qt][kk]), a);
          X2[pb * 2560 + (u * 2 + qt) * 256 + tid] = a;
        }
      if (glu) {
#pragma unroll
        for (int u = 0; u < 2; ++u)
#pragma unroll
          for (int qt = 0; qt < 2; ++qt) {
            f32x4 a = z4;
#pragma unroll
            for (int kk = 0; kk < 5; ++kk) a = mfma16(wgl[u][kk], asb(xf[qt][kk]), a);
            X2[pb * 2560 + (6 + u * 2 + qt) * 256 + tid] = a;
          }
      }
    };
    auto xstore = [&](int t, int pb) {
      if (wave < 3) {
        const unsigned ob = (wave == 0 ? (unsigned)OFF_QX : (wave == 1 ? (unsigned)OFF_KX : (unsigned)OFF_VX)) + (unsigned)(t * H + h) * 8192u;
#pragma unroll
        for (int qt = 0; qt < 2; ++qt) {
          const f32x4* xp = X2 + pb * 2560 + (wave * 2 + qt) * 256 + lane;
          const f32x4 a = (xp[0] + xp[64]) + (xp[128] + xp[192]);
          const unsigned oo = ob + (unsigned)(((qt * 4 + (j >> 1)) * 64 + ml + 16 * (2 * (j & 1) + (g >> 1))) * 16 + (g & 1) * 8);
          const u32x2 vv = {pack2(a[0], a[1]), pack2(a[2], a[3])};
          st64(rw, oo, vv);
        }
      }
      if (glu && wave < 2) {
        const f32x4* xp = X2 + pb * 2560 + (6 + wave) * 256 + lane;
        const f32x4 ga = (xp[0] + xp[64]) + (xp[128] + xp[192]), ua = (xp[512] + xp[576]) + (xp[640] + xp[704]);
        float hv[4];
#pragma unroll
        for (int r = 0; r < 4; ++r) hv[r] = rbf(gelu_tanh(rbf(ga[r]))) * rbf(ua[r]);
        *(u32x2*)(ws + OFF_HH + (size_t)((2 * t + wave) * 54 + (cb >> 1)) * 1024 + lane * 16 + (cb & 1) * 8) = (u32x2){pack2(hv[0], hv[1]), pack2(hv[2], hv[3])};
      }
    };
    {
      u32x4 t1[15], tg[10];
#pragma unroll
      for (int i = 0; i < 10; ++i) tg[i] = *(const u32x4*)((i < 5 ? wg : wu) + (size_t)(160 * wave + 32 * (i % 5) + (lane >> 1)) * F + 16 * (glu ? cb : 0) + (lane & 1) * 8);
#pragma unroll
      for (int tt = 0; tt < 3; ++tt) {
        const u16* W = tt == 0 ? Wq : (tt == 1 ? Wk : Wv);
#pragma unroll
        for (int i = 0; i < 5; ++i) t1[tt * 5 + i] = *(const u32x4*)(W + (size_t)(160 * wave + 32 * i + (lane >> 1)) * HE + h * E + 16 * j + (lane & 1) * 8);
      }
#pragma unroll
      for (int kk = 0; kk < 5; ++kk) mf0[kk] = *(const u32x4*)(mem0 + (ml & 7) * D + 32 * (5 * wave + kk) + 8 * g);
      xload(0);
#pragma unroll
      for (int i = 0; i < 15; ++i) *(u32x4*)(my + i * 1024 + lane * 16) = t1[i];
      asm volatile("s_waitcnt lgkmcnt(0)" ::: "memory");
#pragma unroll
      for (int i = 0; i < 15; ++i) w1[i / 5][i % 5] = trfrag(i);
      asm volatile("s_waitcnt lgkmcnt(0)" ::: "memory");
#pragma unroll
      for (int i = 0; i < 10; ++i) *(u32x4*)(my + i * 1024 + lane * 16) = tg[i];
      asm volatile("s_waitcnt lgkmcnt(0)" ::: "memory");
#pragma unroll
      for (int i = 0; i < 10; ++i) wgl[i / 5][i % 5] = trfrag(i);
    }
    LBAR();
#pragma unroll 1
    for (int t = 0; t < 4; ++t) {
      xmma(0);
      xload(t + 1);
      LBAR();
      xstore(t, 0);
      LBAR();
    }
    xmma(1);
    xload(5);
    asm volatile("s_waitcnt vmcnt(0)" ::: "memory");
    LBAR();
    if (tid == 0) st128(rs, C_TOK + (unsigned)h * 128u + (unsigned)j * 16u, (u32x4){1u, 1u, 1u, 1u});
    LBAR();
    const unsigned m_off = (unsigned)(ml >> 3) * 512u + (unsigned)(g * 8 + (ml & 7)) * 16u;
    int r3 = 0;
    for (int s = 0; s < NSEG; ++s, r3 = r3 == 2 ? 0 : r3 + 1) {
      TS(0);
      const int r3p = r3 == 0 ? 2 : r3 - 1;
      f32x4 ac[3];
      if (s == 0) {
#pragma unroll
        for (int u = 0; u < 3; ++u) ac[u] = z4;
#pragma unroll
        for (int kk = 0; kk < 5; ++kk) {
#pragma unroll
          for (int u = 0; u < 3; ++u) ac[u] = mfma16(w1[u][kk], asb(mf0[kk]), ac[u]);
        }
      } else {
        const unsigned base = C_MEM + (unsigned)r3 * 32768u + (unsigned)(wave >> 1) * 16384u + (unsigned)(wave & 1) * 2560u + selA;
        SG(9); slp<SLP_A>(); SG(0);
        u32x4 mf[5];
        for (unsigned spin = 0;; ++spin) {
          asm volatile("" ::: "memory");
          mf[0] = ld128(rs, base + m_off); mf[2] = ld128(rs, base + m_off + 1024u); mf[4] = ld128(rs, base + 2048u + lo_off);
          const unsigned mx = umax3(umax3(mf[0][0], mf[0][2], mf[2][0]), mf[2][2], max(mf[4][0], mf[4][2]));
          if (__all(mx != 0xFFFFFFFFu) || spin >= spin_cap) { SPN(0); if (spin >= spin_cap) spin_cap = 0; break; }
        }
        mf[1] = ror8x4(mf[0]); mf[3] = ror8x4(mf[2]);
        SG(1);
#pragma unroll
        for (int u = 0; u < 3; ++u) ac[u] = z4;
#pragma unroll
        for (int kk = 0; kk < 5; ++kk) {
#pragma unroll
          for (int u = 0; u < 3; ++u) ac[u] = mfma16(w1[u][kk], asb(mf[kk]), ac[u]);
        }
      }
      TS(1);
#pragma unroll
      for (int u = 0; u < 3; ++u) X1[u * 256 + tid] = ac[u];
      LBAR();
      SG(2);
      if (wave < 2) {
        const int tl = 2 * wave + (ml >> 3);
        const f32x4* xp = X1 + (tl < 3 ? tl : 2) * 256 + g * 16 + (ml & 7);
        f32x4 a = (xp[0] + xp[64]) + (xp[128] + xp[192]);
        u32x2 v = {nosent(pack2(a[0], a[1])), nosent(pack2(a[2], a[3]))};
        const unsigned in = (unsigned)tl * 2048u + (unsigned)(((j >> 1) * 4 + 2 * (j & 1) + (g >> 1)) * 8 + (ml & 7)) * 16u + (g & 1) * 8u;
        if (tl < 3) {
          const unsigned off = C_QKV + (unsigned)(r3 * H + h) * 16384u + in;
          st64(rs, off + selA, v); SG(3); st64(rs, off + (8192u - selA), v);
          const unsigned offp = C_QKV + (unsigned)(r3p * H + h) * 16384u + in;
          st64(rs, offp, ones2); st64(rs, offp + 8192u, ones2);
        }
      }
      TS(2);
      if (s == NSEG - 1) break;
#define XSPOS 1
      if (s + 5 < NSEG) xmma(s & 1);
      if (XSPOS == 0 && s + 4 < NSEG) xstore(s + 4, (s + 1) & 1);
      if (s + 6 < NSEG) {
        for (int sp = 0; sp < 4096 && *FLG != (unsigned)(s + 1); ++sp) __builtin_amdgcn_s_sleep(1);
        xload(s + 6);
      }
      LBAR();
      if (XSPOS == 1 && s + 4 < NSEG) xstore(s + 4, (s + 1) & 1);
      LBAR();
      if (XSPOS == 2 && s + 4 < NSEG) xstore(s + 4, (s + 1) & 1);
      SG(8);
    }
    return;
  }
  const int w4 = wave - 4;
  const bool three = w4 < 1;
  b16x8 w2[2][4];
  {
    u32x4 t2[8];
#pragma unroll
    for (int t = 0; t < 2; ++t) {
      const int ct = (t < 1 || three) ? w4 + 4 * t : 0;
#pragma unroll
      for (int i = 0; i < 4; ++i) t2[t * 4 + i] = *(const u32x4*)(Wo + (size_t)(h * E + 32 * i + (lane >> 1)) * D + 80 * j + 16 * ct + (lane & 1) * 8);
    }
#pragma unroll
    for (int i = 0; i < 8; ++i) *(u32x4*)(my + i * 1024 + lane * 16) = t2[i];
    asm volatile("s_waitcnt lgkmcnt(0)" ::: "memory");
#pragma unroll
    for (int i = 0; i < 8; ++i) w2[i / 4][i % 4] = trfrag(i);
  }
  b16x8 kx[2][4]; u32x4 vwx[2];
  const b16x8 ones8 = asb((u32x4){0x3F803F80u, 0x3F803F80u, 0x3F803F80u, 0x3F803F80u});
  auto prep = [&](int slot) {
    const char* kb = KV + slot * 16384 + lane * 16;
    b16x8 vf[2][4];
#pragma unroll
    for (int kt = 0; kt < 2; ++kt)
#pragma unroll
      for (int ks = 0; ks < 4; ++ks) { kx[kt][ks] = *(const b16x8*)(kb + (kt * 4 + ks) * 1024); vf[kt][ks] = *(const b16x8*)(kb + 8192 + (kt * 4 + ks) * 1024); }
#pragma unroll
    for (int t = 0; t < 2; ++t) {
      f32x4 d0 = z4, d1 = z4;
#pragma unroll
      for (int ks = 0; ks < 4; ++ks) { d0 = mfma16(vf[0][ks], w2[t][ks], d0); d1 = mfma16(vf[1][ks], w2[t][ks], d1); }
      vwx[t] = (u32x4){pack2(d0[0], d0[1]), pack2(d0[2], d0[3]), pack2(d1[0], d1[1]), pack2(d1[2], d1[3])};
    }
  };
  const unsigned kvs = (w4 < 2 ? (unsigned)OFF_KX : (unsigned)OFF_VX) + (unsigned)h * 8192u + (unsigned)(w4 & 1) * 4096u + (unsigned)lane * 16u;
  u32x4 stg[4];
  LBAR();
#pragma unroll 1
  for (int t = 0; t < 4; ++t) { LBAR(); LBAR(); }
  LBAR();
  for (unsigned spin = 0;; ++spin) {
    asm volatile("" ::: "memory");
    const u32x4 tk = ld128(rs, C_TOK + (unsigned)h * 128u + (unsigned)(lane & 7) * 16u);
    if (__all(tk[0] != 0xFFFFFFFFu) || spin >= spin_cap) { if (spin >= spin_cap) spin_cap = 0; break; }
    __builtin_amdgcn_s_sleep(4);
  }
  {
    u32x4 t8[8];
#pragma unroll
    for (int i = 0; i < 8; ++i) t8[i] = ld128(rw, kvs + (unsigned)(i >> 2) * (unsigned)(H * 8192) + (unsigned)(i & 3) * 1024u);
#pragma unroll
    for (int i = 0; i < 4; ++i) stg[i] = ld128(rw, kvs + 2u * (unsigned)(H * 8192) + (unsigned)i * 1024u);
#pragma unroll
    for (int i = 0; i < 8; ++i) *(u32x4*)(KV + (i >> 2) * 16384 + w4 * 4096 + (i & 3) * 1024 + lane * 16) = t8[i];
  }
  LBAR();
  prep(0);
  const int cc = lane & 7, hg = lane >> 3;
  unsigned poff[2];
#pragma unroll
  for (int t = 0; t < 2; ++t) {
    const int T = 5 * j + w4 + 4 * t;
    const unsigned c16 = (unsigned)(((T >> 1) * 4 + 2 * (T & 1) + (g >> 1)) * 8 + (ml & 7)) * 2u + (unsigned)(g & 1), rb = c16 / 5u;
    poff[t] = C_P + (rb / 3u) * 16384u + (rb % 3u) * 2560u + (unsigned)h * 80u + (c16 % 5u) * 16u;
  }
  const bool xat = (w4 == 1) && (j < XATN);
  const int xq = j & 1, xk0 = (j >> 1) * (8 / XATN);
  u32x4 qx[4], pxx, pmx;
  auto qxload = [&](int sn) {
#pragma unroll
    for (int ks = 0; ks < 4; ++ks) qx[ks] = ld128(rw, (unsigned)OFF_QX + (unsigned)(sn * H + h) * 8192u + (unsigned)(xq * 4 + ks) * 1024u + (unsigned)lane * 16u);
  };
  auto xattn1 = [&](const u32x4 (&kv)[4]) {
#pragma unroll
    for (int ks = 0; ks < 4; ++ks) *(u32x4*)(VMS + ks * 1024 + lane * 16) = kv[ks];
    f32x4 s0 = z4, s1 = z4, sm = z4;
#pragma unroll
    for (int ks = 0; ks < 4; ++ks) { s0 = mfma16(kx[0][ks], asb(qx[ks]), s0); s1 = mfma16(kx[1][ks], asb(qx[ks]), s1); sm = mfma16(asb(kv[ks]), asb(qx[ks]), sm); }
    const int qi = 16 * xq + ml;
    float sv[12];
#pragma unroll
    for (int r = 0; r < 4; ++r) {
      sv[r] = (4 * g + r <= qi) ? rbf(rbf(s0[r]) * SCALE) : -INFINITY;
      sv[4 + r] = (16 + 4 * g + r <= qi) ? rbf(rbf(s1[r]) * SCALE) : -INFINITY;
      sv[8 + r] = (g < 2) ? rbf(rbf(sm[r]) * SCALE) : -INFINITY;
    }
    float mxv = sv[0];
#pragma unroll
    for (int r = 1; r < 12; ++r) mxv = fmaxf(mxv, sv[r]);
    mxv = xmax32(xmax16(mxv));
    float sum = 0.f;
#pragma unroll
    for (int r = 0; r < 12; ++r) { sv[r] = __expf(sv[r] - mxv); sum += sv[r]; }
    sum = xsum32(xsum16(sum));
    const float inv = 1.f / sum;
    pxx = (u32x4){pack2(sv[0] * inv, sv[1] * inv), pack2(sv[2] * inv, sv[3] * inv), pack2(sv[4] * inv, sv[5] * inv), pack2(sv[6] * inv, sv[7] * inv)};
    pmx = (u32x4){pack2(sv[8] * inv, sv[9] * inv), pack2(sv[10] * inv, sv[11] * inv), 0u, 0u};
  };
  auto xattn2 = [&](int sn, int slot) {
    const unsigned q4 = (unsigned)ml >> 2, p4 = (unsigned)lane & 3u;
    const unsigned vb = (unsigned)(size_t)(KV + slot * 16384 + 8192) + (4u * g + q4) * 16u + (p4 >> 1) * 256u + (p4 & 1u) * 8u;
    const unsigned mb = (unsigned)(size_t)VMS + (8u + 4u * (g & 1) + q4) * 16u + (p4 >> 1) * 256u + (p4 & 1u) * 8u;
#pragma unroll
    for (int kq = 0; kq < 8 / XATN; ++kq) {
      const int kk = xk0 + kq;
      u32x4 of;
#pragma unroll
      for (int hf = 0; hf < 2; ++hf) {
        const unsigned eo = kk * 1024u + hf * 512u;
        s16x4 v0 = __builtin_amdgcn_ds_read_tr16_b64_v4i16((LAS s16x4*)(vb + eo));
        s16x4 v1 = __builtin_amdgcn_ds_read_tr16_b64_v4i16((LAS s16x4*)(vb + 4096u + eo));
        s16x4 v2 = __builtin_amdgcn_ds_read_tr16_b64_v4i16((LAS s16x4*)(mb + eo));
        s16x8 va = {v0[0], v0[1], v0[2], v0[3], v1[0], v1[1], v1[2], v1[3]};
        s16x8 vm8 = {v2[0], v2[1], v2[2], v2[3], 0, 0, 0, 0};
        f32x4 o = mfma16(__builtin_bit_cast(b16x8, va), asb(pxx), z4);
        o = mfma16(__builtin_bit_cast(b16x8, vm8), asb(pmx), o);
        of[2 * hf] = pack2(o[0], o[1]); of[2 * hf + 1] = pack2(o[2], o[3]);
      }
      *(u32x4*)(ws + OFF_O + (size_t)((sn * 2 + xq) * 128 + h * 4 + kk) * 1024 + lane * 16) = of;
    }
  };
  if (xat) qxload(0);
  const unsigned q_off = (unsigned)(ml >> 3) * 512u + (unsigned)(g * 8 + (ml & 7)) * 16u, kv_off = 2048u + (unsigned)(ml >> 3) * 2048u + (unsigned)(g * 8 + (ml & 7)) * 16u;
  int r3 = 0;
  for (int s = 0; s < NSEG - 1; ++s, r3 = r3 == 2 ? 0 : r3 + 1) {
    const unsigned hb = C_QKV + (unsigned)(r3 * H + h) * 16384u + selA;
    SG(9);
    LBAR();
    SG(0);
    slp<SLP_B>(); SG(1);
    f32x4 a[2];
    u32x4 qf[4], kv[4];
    for (unsigned spin = 0;; ++spin) {
      asm volatile("" ::: "memory");
      qf[0] = ld128(rs, hb + q_off); qf[2] = ld128(rs, hb + q_off + 1024u);
#pragma unroll
      for (int ks = 0; ks < 4; ++ks) kv[ks] = ld128(rs, hb + kv_off + ks * 512u);
      unsigned mx = umax3(umax3(qf[0][0], qf[0][2], qf[2][0]), qf[2][2], 0u);
#pragma unroll
      for (int ks = 0; ks < 4; ++ks) mx = umax3(mx, kv[ks][0], kv[ks][2]);
      if (__all(mx != 0xFFFFFFFFu) || spin >= spin_cap) { SPN(1); if (spin >= spin_cap) spin_cap = 0; break; }
    }
    if (w4 == XFLAG && lane == 0) *FLG = (unsigned)(s + 1);
    qf[1] = ror8x4(qf[0]); qf[3] = ror8x4(qf[2]);
    SG(2);
    {
#define SB() __builtin_amdgcn_sched_barrier(0)
      f32x4 s0 = z4, s1 = z4, sm = z4, dm[2] = {z4, z4};
#pragma unroll
      for (int ks = 0; ks < 4; ++ks) { s0 = mfma16(kx[0][ks], asb(qf[ks]), s0); s1 = mfma16(kx[1][ks], asb(qf[ks]), s1); SB(); }
#pragma unroll
      for (int ks = 0; ks < 4; ++ks) { sm = mfma16(asb(kv[ks]), asb(qf[ks]), sm); SB(); }
      TS(3);
      const float NEG = -INFINITY;
      const float c2 = SCALE * 1.4426950408889634f;
      float tv[12];
      dm[0] = mfma16(asb(kv[0]), w2[0][0], dm[0]);
#pragma unroll
      for (int r = 0; r < 4; ++r) tv[r] = s0[r] * c2;
      SB();
      dm[1] = mfma16(asb(kv[0]), w2[1][0], dm[1]);
#pragma unroll
      for (int r = 0; r < 4; ++r) tv[4 + r] = s1[r] * c2;
      SB();
      float m1 = fmaxf(fmaxf(tv[0], tv[1]), tv[2]), m2 = fmaxf(fmaxf(tv[3], tv[4]), tv[5]);
      m1 = fmaxf(fmaxf(m1, tv[6]), tv[7]);
      SB();
      dm[0] = mfma16(asb(kv[1]), w2[0][1], dm[0]);
#pragma unroll
      for (int r = 0; r < 4; ++r) tv[8 + r] = (g < 2) ? sm[r] * c2 : NEG;
      SB();
      dm[1] = mfma16(asb(kv[1]), w2[1][1], dm[1]);
      m2 = fmaxf(fmaxf(m2, tv[8]), tv[9]); m1 = fmaxf(fmaxf(m1, tv[10]), tv[11]);
      float mxv = fmaxf(m1, m2);
      { unsigned u = __float_as_uint(mxv); auto r = __builtin_amdgcn_permlane16_swap(u, u, false, false); mxv = fmaxf(__uint_as_float(r[0]) + 0.f, __uint_as_float(r[1]) + 0.f); }
      SB();
      { unsigned u = __float_as_uint(mxv); auto r = __builtin_amdgcn_permlane32_swap(u, u, false, false); mxv = fmaxf(__uint_as_float(r[0]) + 0.f, __uint_as_float(r[1]) + 0.f); }
      SB();
      dm[0] = mfma16(asb(kv[2]), w2[0][2], dm[0]);
#pragma unroll
      for (int r = 0; r < 2; ++r) tv[r] = __builtin_amdgcn_exp2f(tv[r] - mxv);
      SB();
      dm[1] = mfma16(asb(kv[2]), w2[1][2], dm[1]);
#pragma unroll
      for (int r = 2; r < 4; ++r) tv[r] = __builtin_amdgcn_exp2f(tv[r] - mxv);
      SB();
#pragma unroll
      for (int r = 4; r < 6; ++r) tv[r] = __builtin_amdgcn_exp2f(tv[r] - mxv);
      SB();
      dm[0] = mfma16(asb(kv[3]), w2[0][3], dm[0]);
#pragma unroll
      for (int r = 6; r < 8; ++r) tv[r] = __builtin_amdgcn_exp2f(tv[r] - mxv);
      SB();
      dm[1] = mfma16(asb(kv[3]), w2[1][3], dm[1]);
#pragma unroll
      for (int r = 8; r < 10; ++r) tv[r] = __builtin_amdgcn_exp2f(tv[r] - mxv);
      SB();
#pragma unroll
      for (int r = 10; r < 12; ++r) tv[r] = __builtin_amdgcn_exp2f(tv[r] - mxv);
      u32x4 px = {pack2(tv[0], tv[1]), pack2(tv[2], tv[3]), pack2(tv[4], tv[5]), pack2(tv[6], tv[7])};
      SB();
      f32x4 sx = mfma16(ones8, asb(px), z4);
      a[0] = mfma16(asb(vwx[0]), asb(px), z4);
      a[1] = mfma16(asb(vwx[1]), asb(px), z4);
      u32x4 pm;
      { const unsigned p0 = pack2(tv[8], tv[9]), p1 = pack2(tv[10], tv[11]);
        auto r0 = __builtin_amdgcn_permlane32_swap(0u, p0, false, false); auto r1 = __builtin_amdgcn_permlane32_swap(0u, p1, false, false);
        pm = (u32x4){r0[0], r1[0], 0u, 0u}; }
      SB();
      sx = mfma16(ones8, asb(pm), sx);
#pragma unroll
      for (int t = 0; t < 2; ++t) {
        u32x4 vwm = {g >= 2 ? pack2(dm[t][0], dm[t][1]) : 0u, g >= 2 ? pack2(dm[t][2], dm[t][3]) : 0u, 0u, 0u};
        a[t] = mfma16(asb(vwm), asb(pm), a[t]);
      }
      SB();
      const float inv = __builtin_amdgcn_rcpf(sx[0]);
      a[0] *= inv; a[1] *= inv;
    }
#pragma unroll
    for (int t = 0; t < 2; ++t) {
      if (t == 1 && !three) break;
      u32x4 v;
#pragma unroll
      for (int r = 0; r < 4; ++r) v[r] = min(__float_as_uint(a[t][r]), 0xFFFFFFFEu);
      const unsigned off = (unsigned)(1 + (s & 1)) * PAGE + poff[t];
      if (ml < 8) { st128(rs, off, v); st128(rs, off + 8192u, v); }
    }
    SG(3);
    TS(4);
    LBAR();
    SG(4);
    const int r3n = r3 == 2 ? 0 : r3 + 1, r3nn = r3 == 0 ? 2 : r3 - 1;
    auto kvwrite = [&] {
      if (s + 2 < NSEG) {
#pragma unroll
        for (int i = 0; i < 4; ++i) *(u32x4*)(KV + r3nn * 16384 + w4 * 4096 + i * 1024 + lane * 16) = stg[i];
      }
    };
    if (w4 == 3) {
      kvwrite();
        const unsigned offw = (unsigned)(1 + (s & 1)) * PAGE + C_P + (unsigned)(cb / 3) * 16384u + (unsigned)(cb % 3) * 2560u + (unsigned)hg * 320u + (unsigned)cc * 16u;
        const unsigned off = cc < 5 ? offw + ((s & 1) ? selP1 : selP0) : OOB;
        slp<SLP_C>(); SG(5);
        u32x4 v[4];
        for (unsigned spin = 0;; ++spin) {
          asm volatile("" ::: "memory");
#pragma unroll
          for (int i = 0; i < 4; ++i) v[i] = ld128(rs, off + i * 80u);
          unsigned mx = 0;
#pragma unroll
          for (int i = 0; i < 4; ++i) { mx = umax3(mx, v[i][0], v[i][1]); mx = umax3(mx, v[i][2], v[i][3]); }
          if (__all(mx != 0xFFFFFFFFu) || spin >= spin_cap) { SPN(2); if (spin >= spin_cap) spin_cap = 0; break; }
        }
        SG(6);
        TS(5);
        float f[4];
#pragma unroll
        for (int r = 0; r < 4; ++r) {
          float x = (__uint_as_float(v[0][r]) + __uint_as_float(v[1][r])) + (__uint_as_float(v[2][r]) + __uint_as_float(v[3][r]));
          x += ror8(x); x = xsum32(xsum16(x)); f[r] = x;
        }
        u32x2 w = {nosent(pack2(f[0], f[1])), nosent(pack2(f[2], f[3]))};
        const unsigned mq = (unsigned)cb >> 6, min_ = (mq >> 1) * 16384u + (mq & 1) * 2560u + (unsigned)(5 * (cb & 63) + lane) * 8u;
        if (lane < 5) { st64(rs, C_MEM + (unsigned)r3n * 32768u + min_, w); st64(rs, C_MEM + (unsigned)r3n * 32768u + 8192u + min_, w); }
        SG(7);
        TS(6);
        LBAR();
        if (cc < 5) {
#pragma unroll
          for (int i = 0; i < 4; ++i) { st128(rs, offw + i * 80u, ones4); st128(rs, offw + 8192u + i * 80u, ones4); }
        }
        if (lane < 5) { st64(rs, C_MEM + (unsigned)r3 * 32768u + min_, ones2); st64(rs, C_MEM + (unsigned)r3 * 32768u + 8192u + min_, ones2); }
      prep(r3n);
    } else if (xat) {
      kvwrite();
      xattn1(kv);
      LBAR();
      xattn2(s, r3);
      prep(r3n);
      qxload(s + 1);
    } else {
      prep(r3n);
      kvwrite();
      LBAR();
    }
    if (s + 3 < NSEG) {
#pragma unroll
      for (int i = 0; i < 4; ++i) stg[i] = ld128(rw, kvs + (unsigned)(s + 3) * (unsigned)(H * 8192) + (unsigned)i * 1024u);
    }
  }
  LBAR();
  if (xat) {
    const unsigned hb = C_QKV + (unsigned)(r3 * H + h) * 16384u + selA;
    slp<SLP_B>();
    u32x4 kv[4];
    for (unsigned spin = 0;; ++spin) {
      asm volatile("" ::: "memory");
      unsigned mx = 0;
#pragma unroll
      for (int ks = 0; ks < 4; ++ks) { kv[ks] = ld128(rs, hb + kv_off + ks * 512u); mx = umax3(mx, kv[ks][0], kv[ks][2]); }
      if (__all(mx != 0xFFFFFFFFu) || spin >= spin_cap) break;
    }
    xattn1(kv);
    asm volatile("s_waitcnt lgkmcnt(0)" ::: "memory");
    xattn2(NSEG - 1, r3);
  }
}

__global__ __launch_bounds__(512, 1) void k_chain(const u16* __restrict__ mem0, const u16* __restrict__ Wq, const u16* __restrict__ Wk,
                                                   const u16* __restrict__ Wv, const u16* __restrict__ Wo, const u16* __restrict__ wg,
                                                   const u16* __restrict__ wu, char* __restrict__ ws) {
  extern __shared__ __attribute__((aligned(16))) char dyn_lds[];
  chain_role(blockIdx.x, mem0, Wq, Wk, Wv, Wo, wg, wu, ws, dyn_lds);
}

__global__ __launch_bounds__(512, 1) void k_post2(const char* __restrict__ ws, u16* __restrict__ out) {
  extern __shared__ __attribute__((aligned(16))) char lds[];
  const int tid = threadIdx.x, lane = tid & 63, wave = __builtin_amdgcn_readfirstlane(tid >> 6), ml = lane & 15, g = lane >> 4;
  const int b = blockIdx.x, xc = b & 7, idx = b >> 3, tt = xc * 4 + (idx >> 3), cgp = idx & 7;
  f32x4* X = (f32x4*)lds;
  f32x4 asum[2][2];
#pragma unroll
  for (int ph = 0; ph < 2; ++ph) {
    const int kt = ph == 0 ? 128 : 54, nk = ph == 0 ? 16 : (wave < 6 ? 7 : 6);
    const char* tb = ws + (ph ? OFF_HH : OFF_O) + (size_t)(tt * 4) * kt * 1024 + (size_t)wave * 1024 + lane * 16;
    const size_t tstr = (size_t)kt * 1024;
    const char* wb = ws + OFF_WF + ((size_t)(cgp * 5) * 182 + (ph ? 128 : 0) + wave) * 1024 + lane * 16;
    f32x4 acc[4][5];
#pragma unroll
    for (int i = 0; i < 4; ++i)
#pragma unroll
      for (int c = 0; c < 5; ++c) acc[i][c] = (f32x4){0, 0, 0, 0};
    b16x8 tf[4][4], wf[4][5];
    auto ldk = [&](int slot, int k) {
      const int kn = k < nk ? k : nk - 1;
#pragma unroll
      for (int i = 0; i < 4; ++i) tf[slot][i] = *(const b16x8*)(tb + i * tstr + (size_t)kn * 8192);
#pragma unroll
      for (int c = 0; c < 5; ++c) wf[slot][c] = *(const b16x8*)(wb + (size_t)c * 182 * 1024 + (size_t)kn * 8192);
    };
    ldk(0, 0); ldk(1, 1); ldk(2, 2);
    for (int k = 0; k < nk; k += 4) {
#pragma unroll
      for (int u = 0; u < 4; ++u) {
        ldk((u + 3) & 3, k + u + 3);
        if (k + u < nk) {
#pragma unroll
          for (int i = 0; i < 4; ++i)
#pragma unroll
            for (int c = 0; c < 5; ++c) acc[i][c] = mfma16(wf[u][c], tf[u][i], acc[i][c]);
        }
      }
    }
#pragma unroll
    for (int rd = 0; rd < 2; ++rd) {
#pragma unroll
      for (int t = 0; t < 10; ++t) { const int tl = rd * 10 + t; X[(wave * 10 + t) * 64 + lane] = acc[tl / 5][tl % 5]; }
      RBAR();
#pragma unroll
      for (int u = 0; u < 2; ++u) {
        const int t = wave + 8 * u;
        if (t < 10) {
          const f32x4* xp = X + t * 64 + lane;
          const f32x4 v = ((xp[0] + xp[640]) + (xp[1280] + xp[1920])) + ((xp[2560] + xp[3200]) + (xp[3840] + xp[4480]));
          if (ph == 0) asum[rd][u] = v;
          else {
            const int tl = rd * 10 + t, i = tl / 5, c = tl % 5;
            const int tok = tt * 64 + 16 * i + ml;
            const float gg = ((const float*)(ws + OFF_G))[tok], og = rbf(1.f - gg);
            float r[4];
#pragma unroll
            for (int q = 0; q < 4; ++q) { float ta = rbf(gg * rbf(asum[rd][u][q])), tb2 = rbf(og * rbf(v[q])); asm volatile("" : "+v"(ta), "+v"(tb2)); r[q] = ta + tb2; }
            *(u32x2*)(out + (size_t)tok * D + 80 * cgp + 16 * c + 4 * g) = (u32x2){pack2(r[0], r[1]), pack2(r[2], r[3])};
          }
        }
      }
      RBAR();
    }
  }
}

extern "C" void kernel_launch(void* const* d_in, const int* in_sizes, int n_in, void* d_out, int out_size, void* d_ws, size_t ws_size,
                              hipStream_t stream) {
  const u16* x = (const u16*)d_in[0];
  const u16* mem0 = (const u16*)d_in[1];
  const u16* Wq = (const u16*)d_in[2];
  const u16* Wk = (const u16*)d_in[3];
  const u16* Wv = (const u16*)d_in[4];
  const u16* Wo = (const u16*)d_in[5];
  const u16* wg = (const u16*)d_in[8];
  const u16* wu = (const u16*)d_in[9];
  const u16* wd = (const u16*)d_in[10];
  const u16* gw = (const u16*)d_in[11];
  char* ws = (char*)d_ws;
  k_init<<<NB_INIT1 + 512, 256, 0, stream>>>(x, Wo, wd, gw, ws);
  hipFuncSetAttribute((const void*)k_chain, hipFuncAttributeMaxDynamicSharedMemorySize, 156 * 1024);
  k_chain<<<256, 512, 156 * 1024, stream>>>(mem0, Wq, Wk, Wv, Wo, wg, wu, ws);
  hipFuncSetAttribute((const void*)k_post2, hipFuncAttributeMaxDynamicSharedMemorySize, 80 * 1024);
  k_post2<<<256, 512, 80 * 1024, stream>>>(ws, (u16*)d_out);
}
```

```cpp
#include <hip/hip_runtime.h>
#include <hip/hip_bf16.h>
#include <stdint.h>

typedef unsigned short u16;
typedef __bf16 b16x8 __attribute__((ext_vector_type(8)));
typedef short s16x4 __attribute__((ext_vector_type(4)));
typedef float f32x4 __attribute__((ext_vector_type(4)));
typedef unsigned u32x4 __attribute__((ext_vector_type(4)));
typedef unsigned u32x2 __attribute__((ext_vector_type(2)));
#define LAS __attribute__((address_space(3)))
#define DI __device__ __forceinline__

constexpr int S = 2048, D = 640, H = 32, E = 128, HE = 4096, MM = 8, SEG = 32, NSEG = 64, F = 1728;
constexpr float SCALE = 0.08838834764831845f;

constexpr size_t OFF_KX = 0;
constexpr size_t OFF_VX = OFF_KX + (size_t)S * HE * 2;
constexpr size_t OFF_QX = OFF_VX + (size_t)S * HE * 2;
constexpr size_t OFF_O  = OFF_QX + (size_t)S * HE * 2;
constexpr size_t OFF_HH = OFF_O + (size_t)S * HE * 2;
constexpr size_t OFF_WF = OFF_HH + (size_t)S * F * 2;
constexpr size_t OFF_XF = OFF_WF + (size_t)40 * 182 * 1024;
constexpr size_t OFF_G  = OFF_XF + (size_t)S * D * 2;
constexpr size_t OFF_COMM = OFF_G + (size_t)S * 4;
constexpr size_t OFF_END = OFF_COMM + (size_t)8 * 1048576;
static_assert(OFF_END <= (size_t)256 * 1048576, "workspace");
constexpr unsigned PAGE = 2097152u, C_MEM = 65536u, C_SEL = 163840u, C_TOK = 180224u, C_QKV = 196608u, C_P = 65536u;
__host__ __device__ inline char* comm_base(char* ws) { return (char*)(((size_t)(ws + OFF_COMM) + (PAGE - 1)) & ~(size_t)(PAGE - 1)); }

DI float rbf(float f) { return (float)(__bf16)f; }
typedef float f32x2 __attribute__((ext_vector_type(2)));
typedef __bf16 b16x2 __attribute__((ext_vector_type(2)));
DI unsigned pack2(float lo, float hi) { f32x2 v = {lo, hi}; return __builtin_bit_cast(unsigned, __builtin_convertvector(v, b16x2)); }
DI f32x4 mfma16(b16x8 a, b16x8 b, f32x4 c) { return __builtin_amdgcn_mfma_f32_16x16x32_bf16(a, b, c, 0, 0, 0); }
DI b16x8 asb(u32x4 v) { return __builtin_bit_cast(b16x8, v); }

DI float gelu_tanh(float x) {
  float y = 0.7978845608028654f * (x + 0.044715f * x * x * x);
  float t = 1.f - 2.f / (__expf(2.f * y) + 1.f);
  return 0.5f * x * (1.f + t);
}

#define RBAR() asm volatile("s_waitcnt lgkmcnt(0)\n\ts_barrier" ::: "memory")

constexpr unsigned NX_INIT = S * D / 8, NF_INIT = (2 * PAGE + C_P + 86 * 16384) / 16, NB_INIT1 = (NX_INIT + NF_INIT + 255) / 256;
__global__ __launch_bounds__(256) void k_init(const u16* __restrict__ x, const u16* __restrict__ Wo, const u16* __restrict__ wd,
                                              const u16* __restrict__ gate_w, char* __restrict__ ws) {
  __shared__ __attribute__((aligned(16))) char lds[4 * 8192];
  if (blockIdx.x >= 512) {
    const unsigned i = (blockIdx.x - 512) * 256 + threadIdx.x;
    if (i < NF_INIT) *(u32x4*)(comm_base(ws) + (size_t)i * 16) = (u32x4){0xFFFFFFFFu, 0xFFFFFFFFu, 0xFFFFFFFFu, 0xFFFFFFFFu};
    else if (i - NF_INIT < NX_INIT) {
      const unsigned k = i - NF_INIT, lane = k & 63, f = k >> 6, kk = f % 20, t16 = f / 20;
      *(u32x4*)(ws + OFF_XF + (size_t)k * 16) = *(const u32x4*)(x + (size_t)(16 * t16 + (lane & 15)) * D + 32 * kk + 8 * (lane >> 4));
    }
    return;
  }
  const int lane = threadIdx.x & 63, wave = __builtin_amdgcn_readfirstlane(threadIdx.x >> 6), ml = lane & 15, g = lane >> 4;
  const int gw = blockIdx.x * 4 + wave;
  char* V = lds + wave * 8192;
  {
    for (int t = gw; t < S; t += 2048) {
      float s = 0.f;
#pragma unroll
      for (int i = 0; i < 5; ++i) {
        const unsigned xv = *(const unsigned*)(x + (size_t)t * D + i * 128 + lane * 2), gv = *(const unsigned*)(gate_w + i * 128 + lane * 2);
        s += rbf(__uint_as_float(xv << 16) * __uint_as_float(gv << 16)) + rbf(__uint_as_float(xv & 0xFFFF0000u) * __uint_as_float(gv & 0xFFFF0000u));
      }
#pragma unroll
      for (int o = 32; o >= 1; o >>= 1) s += __shfl_xor(s, o);
      s = rbf(s);
      float gg = rbf(1.f / (1.f + __expf(-s)));
      if (lane == 0) ((float*)(ws + OFF_G))[t] = gg;
    }
    char* my = V;
    const unsigned trb = (unsigned)(size_t)my + (4 * g + (ml >> 2)) * 32 + (lane & 3) * 8;
    for (int u = gw; u < 182 * 5; u += 2048) {
      const int kk = u / 5, c0 = (u % 5) * 8;
      const u16* W = kk < 128 ? Wo + (size_t)32 * kk * D : wd + (size_t)32 * (kk - 128) * D;
      u32x4 t8[8];
#pragma unroll
      for (int i = 0; i < 8; ++i) t8[i] = *(const u32x4*)(W + (size_t)(4 * i + (lane >> 4)) * D + 16 * c0 + (lane & 15) * 8);
#pragma unroll
      for (int i = 0; i < 8; ++i) *(u32x4*)(my + ((lane & 15) >> 1) * 1024 + (4 * i + (lane >> 4)) * 32 + (lane & 1) * 16) = t8[i];
      asm volatile("s_waitcnt lgkmcnt(0)" ::: "memory");
#pragma unroll
      for (int i = 0; i < 8; ++i) {
        s16x4 lo = __builtin_amdgcn_ds_read_tr16_b64_v4i16((LAS s16x4*)(trb + i * 1024));
        s16x4 hi = __builtin_amdgcn_ds_read_tr16_b64_v4i16((LAS s16x4*)(trb + i * 1024 + 512));
        typedef short s16x8 __attribute__((ext_vector_type(8)));
        s16x8 tt = {lo[0], lo[1], lo[2], lo[3], hi[0], hi[1], hi[2], hi[3]};
        *(s16x8*)(ws + OFF_WF + ((size_t)(c0 + i) * 182 + kk) * 1024 + lane * 16) = tt;
      }
      asm volatile("s_waitcnt lgkmcnt(0)" ::: "memory");
    }
  }
}

#define SC1 16
DI u32x4 ld128(__amdgpu_buffer_rsrc_t rs, unsigned off) { return __builtin_amdgcn_raw_buffer_load_b128(rs, off, 0, SC1); }
DI void st128(__amdgpu_buffer_rsrc_t rs, unsigned off, u32x4 v) { __builtin_amdgcn_raw_buffer_store_b128(v, rs, off, 0, SC1); }
DI void st64(__amdgpu_buffer_rsrc_t rs, unsigned off, u32x2 v) { __builtin_amdgcn_raw_buffer_store_b64(v, rs, off, 0, SC1); }
DI unsigned nosent(unsigned v) { return v == 0xFFFFFFFFu ? 0xFFFEFFFFu : v; }
DI unsigned umax3(unsigned a, unsigned b, unsigned c) { return max(max(a, b), c); }
DI float xsum16(float x) { unsigned u = __float_as_uint(x); auto r = __builtin_amdgcn_permlane16_swap(u, u, false, false); return __uint_as_float(r[0]) + __uint_as_float(r[1]); }
DI float xsum32(float x) { unsigned u = __float_as_uint(x); auto r = __builtin_amdgcn_permlane32_swap(u, u, false, false); return __uint_as_float(r[0]) + __uint_as_float(r[1]); }
DI float xmax16(float x) { unsigned u = __float_as_uint(x); auto r = __builtin_amdgcn_permlane16_swap(u, u, false, false); return fmaxf(__uint_as_float(r[0]), __uint_as_float(r[1])); }
DI float xmax32(float x) { unsigned u = __float_as_uint(x); auto r = __builtin_amdgcn_permlane32_swap(u, u, false, false); return fmaxf(__uint_as_float(r[0]), __uint_as_float(r[1])); }
DI float ror8(float x) { return __uint_as_float(__builtin_amdgcn_update_dpp(0u, __float_as_uint(x), 0x128, 0xf, 0xf, false)); }
DI u32x4 ror8x4(u32x4 v) { u32x4 r; for (int i = 0; i < 4; ++i) r[i] = __builtin_amdgcn_update_dpp(0u, v[i], 0x128, 0xf, 0xf, false); return r; }
template <int N> struct IC { static constexpr int value = N; };
#define LBAR() asm volatile("s_waitcnt lgkmcnt(0)\n\ts_barrier" ::: "memory")
template <int N> DI void slp() { if constexpr (N > 0) { __builtin_amdgcn_s_sleep(N > 15 ? 15 : N); if constexpr (N > 15) slp<N - 15>(); } }

constexpr unsigned SPIN_CAP = 1u << 17;
#define SLP_A 15
#define SLP_B 12
#define XATN 8
#define XFLAG 0
#define SLP_C 9

DI void chain_role(const int cb, const u16* __restrict__ mem0, const u16* __restrict__ Wq, const u16* __restrict__ Wk,
                   const u16* __restrict__ Wv, const u16* __restrict__ Wo, const u16* __restrict__ wg, const u16* __restrict__ wu,
                   char* __restrict__ ws, char* dyn_lds) {
  const int xcd = cb & 7, y = cb >> 3, h = xcd * 4 + (y >> 3), j = y & 7;
  const int tid = threadIdx.x, lane = tid & 63, wave = __builtin_amdgcn_readfirstlane(tid >> 6);
  const int ml = lane & 15, g = lane >> 4;
  const __amdgpu_buffer_rsrc_t rs = __builtin_amdgcn_make_buffer_rsrc(comm_base(ws), 0, (int)(3 * PAGE), 0x00020000);
  unsigned selA, selP0, selP1;
  {
    unsigned* pl = (unsigned*)(dyn_lds + 156 * 1024 - 64);
    if (tid == 64) *(volatile LAS unsigned*)(dyn_lds + 156 * 1024 - 32) = 0u;
    if (wave == 0) {
      {
        const unsigned v = __builtin_amdgcn_raw_buffer_load_b32(rs, (unsigned)(lane < 3 ? lane : 0) * PAGE + (PAGE - 4096u), 0, SC1);
        asm volatile("s_waitcnt vmcnt(0)" :: "v"(v) : "memory");
      }
#pragma unroll 1
      for (int p = 0; p < 4; ++p) {
        const unsigned po = (unsigned)(p < 2 ? 0 : p - 1) * PAGE + (unsigned)(cb >> 6) * 16384u + (unsigned)(p == 1) * 8192u + (unsigned)(cb & 63) * 128u;
        asm volatile("s_waitcnt vmcnt(0) lgkmcnt(0)" ::: "memory");
        const unsigned long long t0 = __builtin_amdgcn_s_memtime();
        asm volatile("s_waitcnt lgkmcnt(0)" ::: "memory");
        const unsigned v = __builtin_amdgcn_raw_buffer_load_b32(rs, po, 0, SC1);
        asm volatile("s_waitcnt vmcnt(0)" :: "v"(v) : "memory");
        const unsigned long long t1 = __builtin_amdgcn_s_memtime();
        if (lane == 0) pl[p] = (unsigned)(t1 - t0);
      }
    }
    LBAR();
    selA = __builtin_amdgcn_readfirstlane(pl[1] < pl[0] ? 8192u : 0u);
    selP0 = __builtin_amdgcn_readfirstlane(2 * pl[2] > pl[0] + pl[1] ? 8192u : 0u);
    selP1 = __builtin_amdgcn_readfirstlane(2 * pl[3] > pl[0] + pl[1] ? 8192u : 0u);
  }
  volatile LAS unsigned* FLG = (volatile LAS unsigned*)(dyn_lds + 156 * 1024 - 32);
  volatile LAS unsigned* NEED = (volatile LAS unsigned*)(dyn_lds + 156 * 1024 - 28);
  if (tid == 0) st128(rs, C_SEL + (unsigned)cb * 16u, (u32x4){1u, selP0, selP1, 1u});
  const u32x4 ones4 = {0xFFFFFFFFu, 0xFFFFFFFFu, 0xFFFFFFFFu, 0xFFFFFFFFu};
  const u32x2 ones2 = {0xFFFFFFFFu, 0xFFFFFFFFu};
  f32x4* X1 = (f32x4*)dyn_lds;
  f32x4* X2 = (f32x4*)(dyn_lds + 61440);
  const __amdgpu_buffer_rsrc_t rw = __builtin_amdgcn_make_buffer_rsrc(ws, 0, (int)OFF_O, 0x00020000);
  char* KV = dyn_lds + 12288;
  char* VMS = dyn_lds + 143360;
  char* WL = dyn_lds + 61440;
  const f32x4 z4 = {0, 0, 0, 0};
  const unsigned OOB = 0x7FFFFF00u;
  const unsigned lo_off = ml < 8 ? (unsigned)(g * 8 + ml) * 16u : OOB;
  unsigned spin_cap = SPIN_CAP;
  typedef short s16x8 __attribute__((ext_vector_type(8)));
  char* my = WL + (wave < 4 ? wave * 15360 : 61440 + (wave - 4) * 8192);
  const unsigned trb = (unsigned)(size_t)my + (8 * g + (ml >> 2)) * 32 + (lane & 3) * 8;
  auto trfrag = [&](int i) {
    s16x4 lo = __builtin_amdgcn_ds_read_tr16_b64_v4i16((LAS s16x4*)(trb + i * 1024));
    s16x4 hi = __builtin_amdgcn_ds_read_tr16_b64_v4i16((LAS s16x4*)(trb + i * 1024 + 128));
    s16x8 tt = {lo[0], lo[1], lo[2], lo[3], hi[0], hi[1], hi[2], hi[3]};
    return __builtin_bit_cast(b16x8, tt);
  };

  if (wave < 4) {
    b16x8 w1[3][5], wgl[2][5];
    const bool glu = cb < 108;
    u32x4 mf0[5];
    u32x4 xf[2][5];
    auto xloadp = [&](int t, auto k0c, auto k1c) {
      constexpr int k0 = decltype(k0c)::value, k1 = decltype(k1c)::value;
#pragma unroll
      for (int qt = 0; qt < 2; ++qt)
#pragma unroll
        for (int kk = k0; kk < k1; ++kk) xf[qt][kk] = *(const u32x4*)(ws + OFF_XF + (size_t)((2 * t + qt) * 20 + 5 * wave + kk) * 1024 + lane * 16);
    };
    auto xload = [&](int t) { xloadp(t, IC<0>{}, IC<5>{}); };
    auto xmma = [&](int pb) {
#pragma unroll
      for (int u = 0; u < 3; ++u)
#pragma unroll
        for (int qt = 0; qt < 2; ++qt) {
          f32x4 a = z4;
#pragma unroll
          for (int kk = 0; kk < 5; ++kk) a = mfma16(w1[u][kk], asb(xf[qt][kk]), a);
          X2[pb * 2560 + (u * 2 + qt) * 256 + tid] = a;
        }
      if (glu) {
#pragma unroll
        for (int u = 0; u < 2; ++u)
#pragma unroll
          for (int qt = 0; qt < 2; ++qt) {
            f32x4 a = z4;
#pragma unroll
            for (int kk = 0; kk < 5; ++kk) a = mfma16(wgl[u][kk], asb(xf[qt][kk]), a);
            X2[pb * 2560 + (6 + u * 2 + qt) * 256 + tid] = a;
          }
      }
    };
    auto xstore = [&](int t, int pb) {
      if (wave < 3) {
        const unsigned ob = (wave == 0 ? (unsigned)OFF_QX : (wave == 1 ? (unsigned)OFF_KX : (unsigned)OFF_VX)) + (unsigned)(t * H + h) * 8192u;
#pragma unroll
        for (int qt = 0; qt < 2; ++qt) {
          const f32x4* xp = X2 + pb * 2560 + (wave * 2 + qt) * 256 + lane;
          const f32x4 a = (xp[0] + xp[64]) + (xp[128] + xp[192]);
          const unsigned oo = ob + (unsigned)(((qt * 4 + (j >> 1)) * 64 + ml + 16 * (2 * (j & 1) + (g >> 1))) * 16 + (g & 1) * 8);
          const u32x2 vv = {pack2(a[0], a[1]), pack2(a[2], a[3])};
          st64(rw, oo, vv);
        }
      }
      if (glu && wave < 2) {
        const f32x4* xp = X2 + pb * 2560 + (6 + wave) * 256 + lane;
        const f32x4 ga = (xp[0] + xp[64]) + (xp[128] + xp[192]), ua = (xp[512] + xp[576]) + (xp[640] + xp[704]);
        float hv[4];
#pragma unroll
        for (int r = 0; r < 4; ++r) hv[r] = rbf(gelu_tanh(rbf(ga[r]))) * rbf(ua[r]);
        *(u32x2*)(ws + OFF_HH + (size_t)((2 * t + wave) * 54 + (cb >> 1)) * 1024 + lane * 16 + (cb & 1) * 8) = (u32x2){pack2(hv[0], hv[1]), pack2(hv[2], hv[3])};
      }
    };
    {
      u32x4 t1[15], tg[10];
#pragma unroll
      for (int i = 0; i < 10; ++i) tg[i] = *(const u32x4*)((i < 5 ? wg : wu) + (size_t)(160 * wave + 32 * (i % 5) + (lane >> 1)) * F + 16 * (glu ? cb : 0) + (lane & 1) * 8);
#pragma unroll
      for (int tt = 0; tt < 3; ++tt) {
        const u16* W = tt == 0 ? Wq : (tt == 1 ? Wk : Wv);
#pragma unroll
        for (int i = 0; i < 5; ++i) t1[tt * 5 + i] = *(const u32x4*)(W + (size_t)(160 * wave + 32 * i + (lane >> 1)) * HE + h * E + 16 * j + (lane & 1) * 8);
      }
#pragma unroll
      for (int kk = 0; kk < 5; ++kk) mf0[kk] = *(const u32x4*)(mem0 + (ml & 7) * D + 32 * (5 * wave + kk) + 8 * g);
      xload(0);
#pragma unroll
      for (int i = 0; i < 15; ++i) *(u32x4*)(my + i * 1024 + lane * 16) = t1[i];
      asm volatile("s_waitcnt lgkmcnt(0)" ::: "memory");
#pragma unroll
      for (int i = 0; i < 15; ++i) w1[i / 5][i % 5] = trfrag(i);
      asm volatile("s_waitcnt lgkmcnt(0)" ::: "memory");
#pragma unroll
      for (int i = 0; i < 10; ++i) *(u32x4*)(my + i * 1024 + lane * 16) = tg[i];
      asm volatile("s_waitcnt lgkmcnt(0)" ::: "memory");
#pragma unroll
      for (int i = 0; i < 10; ++i) wgl[i / 5][i % 5] = trfrag(i);
    }
    LBAR();
#pragma unroll 1
    for (int t = 0; t < 4; ++t) {
      xmma(t & 1);
      xload(t + 1);
      LBAR();
      xstore(t, t & 1);
    }
    asm volatile("s_waitcnt vmcnt(0)" ::: "memory");
    LBAR();
    if (tid == 0) st128(rs, C_TOK + (unsigned)h * 128u + (unsigned)j * 16u, (u32x4){1u, selA, 1u, 1u});
    xmma(1);
    xload(5);
    LBAR();
    const bool both = __builtin_amdgcn_readfirstlane(*NEED) != 0u;
    const unsigned m_off = (unsigned)(ml >> 3) * 512u + (unsigned)(g * 8 + (ml & 7)) * 16u;
    int r3 = 0;
    for (int s = 0; s < NSEG; ++s, r3 = r3 == 2 ? 0 : r3 + 1) {
      const int r3p = r3 == 0 ? 2 : r3 - 1;
      f32x4 ac[3];
      if (s == 0) {
#pragma unroll
        for (int u = 0; u < 3; ++u) ac[u] = z4;
#pragma unroll
        for (int kk = 0; kk < 5; ++kk) {
#pragma unroll
          for (int u = 0; u < 3; ++u) ac[u] = mfma16(w1[u][kk], asb(mf0[kk]), ac[u]);
        }
      } else {
        const unsigned base = C_MEM + (unsigned)r3 * 32768u + (unsigned)(wave >> 1) * 16384u + (unsigned)(wave & 1) * 2560u + selA;
        slp<SLP_A>();
        u32x4 mf[5];
        for (unsigned spin = 0;; ++spin) {
          asm volatile("" ::: "memory");
          mf[0] = ld128(rs, base + m_off); mf[2] = ld128(rs, base + m_off + 1024u); mf[4] = ld128(rs, base + 2048u + lo_off);
          const unsigned mx = umax3(umax3(mf[0][0], mf[0][2], mf[2][0]), mf[2][2], max(mf[4][0], mf[4][2]));
          if (__all(mx != 0xFFFFFFFFu) || spin >= spin_cap) { if (spin >= spin_cap) spin_cap = 0; break; }
        }
        asm volatile("s_waitcnt vmcnt(0)" ::: "memory");
        mf[1] = ror8x4(mf[0]); mf[3] = ror8x4(mf[2]);
#pragma unroll
        for (int u = 0; u < 3; ++u) ac[u] = z4;
#pragma unroll
        for (int kk = 0; kk < 5; ++kk) {
#pragma unroll
          for (int u = 0; u < 3; ++u) ac[u] = mfma16(w1[u][kk], asb(mf[kk]), ac[u]);
        }
      }
#pragma unroll
      for (int u = 0; u < 3; ++u) X1[u * 256 + tid] = ac[u];
      LBAR();
      if (wave < 2) {
        const int tl = 2 * wave + (ml >> 3);
        const f32x4* xp = X1 + (tl < 3 ? tl : 2) * 256 + g * 16 + (ml & 7);
        f32x4 a = (xp[0] + xp[64]) + (xp[128] + xp[192]);
        u32x2 v = {nosent(pack2(a[0], a[1])), nosent(pack2(a[2], a[3]))};
        const unsigned in = (unsigned)tl * 2048u + (unsigned)(((j >> 1) * 4 + 2 * (j & 1) + (g >> 1)) * 8 + (ml & 7)) * 16u + (g & 1) * 8u;
        if (tl < 3) {
          const unsigned off = C_QKV + (unsigned)(r3 * H + h) * 16384u + in;
          st64(rs, off + selA, v);
          if (both) st64(rs, off + (8192u - selA), v);
          const unsigned offp = C_QKV + (unsigned)(r3p * H + h) * 16384u + in;
          st64(rs, offp + selA, ones2);
          if (both) st64(rs, offp + (8192u - selA), ones2);
        }
      }
      if (s == NSEG - 1) break;
      if (s + 5 < NSEG) xmma(s & 1);
      if (s + 6 < NSEG) {
        for (int sp = 0; sp < 4096 && *FLG != (unsigned)(s + 1); ++sp) __builtin_amdgcn_s_sleep(1);
        xload(s + 6);
      }
      LBAR();
      if (s + 4 < NSEG) xstore(s + 4, (s + 1) & 1);
      LBAR();
    }
    return;
  }
  const int w4 = wave - 4;
  const bool three = w4 < 1;
  b16x8 w2[2][4];
  {
    {
      const u32x4 p0 = *(const u32x4*)(ws + OFF_XF + (size_t)((y * 4 + w4) * 2) * 1024 + lane * 16), p1 = *(const u32x4*)(ws + OFF_XF + (size_t)((y * 4 + w4) * 2 + 1) * 1024 + lane * 16);
      asm volatile("" :: "v"(p0), "v"(p1));
    }
    u32x4 t2[8];
#pragma unroll
    for (int t = 0; t < 2; ++t) {
      const int ct = (t < 1 || three) ? w4 + 4 * t : 0;
#pragma unroll
      for (int i = 0; i < 4; ++i) t2[t * 4 + i] = *(const u32x4*)(Wo + (size_t)(h * E + 32 * i + (lane >> 1)) * D + 80 * j + 16 * ct + (lane & 1) * 8);
    }
#pragma unroll
    for (int i = 0; i < 8; ++i) *(u32x4*)(my + i * 1024 + lane * 16) = t2[i];
    asm volatile("s_waitcnt lgkmcnt(0)" ::: "memory");
#pragma unroll
    for (int i = 0; i < 8; ++i) w2[i / 4][i % 4] = trfrag(i);
  }
  b16x8 kx[2][4]; u32x4 vwx[2];
  const b16x8 ones8 = asb((u32x4){0x3F803F80u, 0x3F803F80u, 0x3F803F80u, 0x3F803F80u});
  auto prep = [&](int slot) {
    const char* kb = KV + slot * 16384 + lane * 16;
    b16x8 vf[2][4];
#pragma unroll
    for (int kt = 0; kt < 2; ++kt)
#pragma unroll
      for (int ks = 0; ks < 4; ++ks) { kx[kt][ks] = *(const b16x8*)(kb + (kt * 4 + ks) * 1024); vf[kt][ks] = *(const b16x8*)(kb + 8192 + (kt * 4 + ks) * 1024); }
#pragma unroll
    for (int t = 0; t < 2; ++t) {
      f32x4 d0 = z4, d1 = z4;
#pragma unroll
      for (int ks = 0; ks < 4; ++ks) { d0 = mfma16(vf[0][ks], w2[t][ks], d0); d1 = mfma16(vf[1][ks], w2[t][ks], d1); }
      vwx[t] = (u32x4){pack2(d0[0], d0[1]), pack2(d0[2], d0[3]), pack2(d1[0], d1[1]), pack2(d1[2], d1[3])};
    }
  };
  const unsigned kvs = (w4 < 2 ? (unsigned)OFF_KX : (unsigned)OFF_VX) + (unsigned)h * 8192u + (unsigned)(w4 & 1) * 4096u + (unsigned)lane * 16u;
  u32x4 stg[4];
  LBAR();
#pragma unroll 1
  for (int t = 0; t < 4; ++t) LBAR();
  LBAR();
  for (unsigned spin = 0;; ++spin) {
    asm volatile("" ::: "memory");
    const u32x4 tk = ld128(rs, C_TOK + (unsigned)h * 128u + (unsigned)(lane & 7) * 16u);
    if (__all(tk[0] != 0xFFFFFFFFu && tk[1] != 0xFFFFFFFFu) || spin >= spin_cap) {
      if (spin >= spin_cap) spin_cap = 0;
      const bool same = __all(tk[1] == selA);
      if (w4 == 0 && lane == 0) *NEED = same ? 0u : 1u;
      break;
    }
    __builtin_amdgcn_s_sleep(4);
  }
  {
    u32x4 t8[8];
#pragma unroll
    for (int i = 0; i < 8; ++i) t8[i] = ld128(rw, kvs + (unsigned)(i >> 2) * (unsigned)(H * 8192) + (unsigned)(i & 3) * 1024u);
#pragma unroll
    for (int i = 0; i < 4; ++i) stg[i] = ld128(rw, kvs + 2u * (unsigned)(H * 8192) + (unsigned)i * 1024u);
#pragma unroll
    for (int i = 0; i < 8; ++i) *(u32x4*)(KV + (i >> 2) * 16384 + w4 * 4096 + (i & 3) * 1024 + lane * 16) = t8[i];
  }
  LBAR();
  prep(0);
  const int cc = lane & 7, hg = lane >> 3;
  unsigned poffE[2], poffX[2];
#pragma unroll
  for (int t = 0; t < 2; ++t) {
    const int T = (t == 0 || three) ? 5 * j + w4 + 4 * t : 5 * j;
    const unsigned c16 = (unsigned)(((T >> 1) * 4 + 2 * (T & 1) + (g >> 1)) * 8 + (ml & 7)) * 2u + (unsigned)(g & 1), rb = c16 / 5u;
    const unsigned po = C_P + (rb / 3u) * 16384u + (rb % 3u) * 2560u + (unsigned)h * 80u + (c16 % 5u) * 16u;
    u32x4 se;
    for (unsigned spin = 0;; ++spin) {
      asm volatile("" ::: "memory");
      se = ld128(rs, C_SEL + rb * 16u);
      if (__all(se[0] != 0xFFFFFFFFu && se[1] != 0xFFFFFFFFu && se[2] != 0xFFFFFFFFu) || spin >= spin_cap) { if (spin >= spin_cap) spin_cap = 0; break; }
      __builtin_amdgcn_s_sleep(4);
    }
    poffE[t] = PAGE + po + (se[1] & 8192u); poffX[t] = poffE[t] ^ (2u * PAGE + po + (se[2] & 8192u));
  }
  const bool xat = (w4 == 1) && (j < XATN);
  const int xq = j & 1, xk0 = (j >> 1) * (8 / XATN);
  u32x4 qx[4], pxx, pmx;
  auto qxload = [&](int sn) {
#pragma unroll
    for (int ks = 0; ks < 4; ++ks) qx[ks] = ld128(rw, (unsigned)OFF_QX + (unsigned)(sn * H + h) * 8192u + (unsigned)(xq * 4 + ks) * 1024u + (unsigned)lane * 16u);
  };
  auto xattn1 = [&](const u32x4 (&kv)[4]) {
#pragma unroll
    for (int ks = 0; ks < 4; ++ks) *(u32x4*)(VMS + ks * 1024 + lane * 16) = kv[ks];
    f32x4 s0 = z4, s1 = z4, sm = z4;
#pragma unroll
    for (int ks = 0; ks < 4; ++ks) { s0 = mfma16(kx[0][ks], asb(qx[ks]), s0); s1 = mfma16(kx[1][ks], asb(qx[ks]), s1); sm = mfma16(asb(kv[ks]), asb(qx[ks]), sm); }
    const int qi = 16 * xq + ml;
    float sv[12];
#pragma unroll
    for (int r = 0; r < 4; ++r) {
      sv[r] = (4 * g + r <= qi) ? rbf(rbf(s0[r]) * SCALE) : -INFINITY;
      sv[4 + r] = (16 + 4 * g + r <= qi) ? rbf(rbf(s1[r]) * SCALE) : -INFINITY;
      sv[8 + r] = (g < 2) ? rbf(rbf(sm[r]) * SCALE) : -INFINITY;
    }
    float mxv = sv[0];
#pragma unroll
    for (int r = 1; r < 12; ++r) mxv = fmaxf(mxv, sv[r]);
    mxv = xmax32(xmax16(mxv));
    float sum = 0.f;
#pragma unroll
    for (int r = 0; r < 12; ++r) { sv[r] = __expf(sv[r] - mxv); sum += sv[r]; }
    sum = xsum32(xsum16(sum));
    const float inv = 1.f / sum;
    pxx = (u32x4){pack2(sv[0] * inv, sv[1] * inv), pack2(sv[2] * inv, sv[3] * inv), pack2(sv[4] * inv, sv[5] * inv), pack2(sv[6] * inv, sv[7] * inv)};
    pmx = (u32x4){pack2(sv[8] * inv, sv[9] * inv), pack2(sv[10] * inv, sv[11] * inv), 0u, 0u};
  };
  auto xattn2 = [&](int sn, int slot) {
    const unsigned q4 = (unsigned)ml >> 2, p4 = (unsigned)lane & 3u;
    const unsigned vb = (unsigned)(size_t)(KV + slot * 16384 + 8192) + (4u * g + q4) * 16u + (p4 >> 1) * 256u + (p4 & 1u) * 8u;
    const unsigned mb = (unsigned)(size_t)VMS + (8u + 4u * (g & 1) + q4) * 16u + (p4 >> 1) * 256u + (p4 & 1u) * 8u;
#pragma unroll
    for (int kq = 0; kq < 8 / XATN; ++kq) {
      const int kk = xk0 + kq;
      u32x4 of;
#pragma unroll
      for (int hf = 0; hf < 2; ++hf) {
        const unsigned eo = kk * 1024u + hf * 512u;
        s16x4 v0 = __builtin_amdgcn_ds_read_tr16_b64_v4i16((LAS s16x4*)(vb + eo));
        s16x4 v1 = __builtin_amdgcn_ds_read_tr16_b64_v4i16((LAS s16x4*)(vb + 4096u + eo));
        s16x4 v2 = __builtin_amdgcn_ds_read_tr16_b64_v4i16((LAS s16x4*)(mb + eo));
        s16x8 va = {v0[0], v0[1], v0[2], v0[3], v1[0], v1[1], v1[2], v1[3]};
        s16x8 vm8 = {v2[0], v2[1], v2[2], v2[3], 0, 0, 0, 0};
        f32x4 o = mfma16(__builtin_bit_cast(b16x8, va), asb(pxx), z4);
        o = mfma16(__builtin_bit_cast(b16x8, vm8), asb(pmx), o);
        of[2 * hf] = pack2(o[0], o[1]); of[2 * hf + 1] = pack2(o[2], o[3]);
      }
      *(u32x4*)(ws + OFF_O + (size_t)((sn * 2 + xq) * 128 + h * 4 + kk) * 1024 + lane * 16) = of;
    }
  };
  if (xat) qxload(0);
  const unsigned q_off = (unsigned)(ml >> 3) * 512u + (unsigned)(g * 8 + (ml & 7)) * 16u, kv_off = 2048u + (unsigned)(ml >> 3) * 2048u + (unsigned)(g * 8 + (ml & 7)) * 16u;
  int r3 = 0;
  for (int s = 0; s < NSEG - 1; ++s, r3 = r3 == 2 ? 0 : r3 + 1) {
    const unsigned hb = C_QKV + (unsigned)(r3 * H + h) * 16384u + selA;
    LBAR();
    slp<SLP_B>();
    f32x4 a[2];
    u32x4 qf[4], kv[4];
    for (unsigned spin = 0;; ++spin) {
      asm volatile("" ::: "memory");
      qf[0] = ld128(rs, hb + q_off); qf[2] = ld128(rs, hb + q_off + 1024u);
#pragma unroll
      for (int ks = 0; ks < 4; ++ks) kv[ks] = ld128(rs, hb + kv_off + ks * 512u);
      unsigned mx = umax3(umax3(qf[0][0], qf[0][2], qf[2][0]), qf[2][2], 0u);
#pragma unroll
      for (int ks = 0; ks < 4; ++ks) mx = umax3(mx, kv[ks][0], kv[ks][2]);
      if (__all(mx != 0xFFFFFFFFu) || spin >= spin_cap) { if (spin >= spin_cap) spin_cap = 0; break; }
    }
    if (w4 == XFLAG && lane == 0) *FLG = (unsigned)(s + 1);
    qf[1] = ror8x4(qf[0]); qf[3] = ror8x4(qf[2]);
    {
#define SB() __builtin_amdgcn_sched_barrier(0)
      f32x4 s0 = z4, s1 = z4, sm = z4, dm[2] = {z4, z4};
#pragma unroll
      for (int ks = 0; ks < 4; ++ks) { s0 = mfma16(kx[0][ks], asb(qf[ks]), s0); s1 = mfma16(kx[1][ks], asb(qf[ks]), s1); SB(); }
#pragma unroll
      for (int ks = 0; ks < 4; ++ks) { sm = mfma16(asb(kv[ks]), asb(qf[ks]), sm); SB(); }
      const float NEG = -INFINITY;
      const float c2 = SCALE * 1.4426950408889634f;
      float tv[12];
      dm[0] = mfma16(asb(kv[0]), w2[0][0], dm[0]);
#pragma unroll
      for (int r = 0; r < 4; ++r) tv[r] = s0[r] * c2;
      SB();
      dm[1] = mfma16(asb(kv[0]), w2[1][0], dm[1]);
#pragma unroll
      for (int r = 0; r < 4; ++r) tv[4 + r] = s1[r] * c2;
      SB();
      float m1 = fmaxf(fmaxf(tv[0], tv[1]), tv[2]), m2 = fmaxf(fmaxf(tv[3], tv[4]), tv[5]);
      m1 = fmaxf(fmaxf(m1, tv[6]), tv[7]);
      SB();
      dm[0] = mfma16(asb(kv[1]), w2[0][1], dm[0]);
#pragma unroll
      for (int r = 0; r < 4; ++r) tv[8 + r] = (g < 2) ? sm[r] * c2 : NEG;
      SB();
      dm[1] = mfma16(asb(kv[1]), w2[1][1], dm[1]);
      m2 = fmaxf(fmaxf(m2, tv[8]), tv[9]); m1 = fmaxf(fmaxf(m1, tv[10]), tv[11]);
      float mxv = fmaxf(m1, m2);
      { unsigned u = __float_as_uint(mxv); auto r = __builtin_amdgcn_permlane16_swap(u, u, false, false); mxv = fmaxf(__uint_as_float(r[0]) + 0.f, __uint_as_float(r[1]) + 0.f); }
      SB();
      { unsigned u = __float_as_uint(mxv); auto r = __builtin_amdgcn_permlane32_swap(u, u, false, false); mxv = fmaxf(__uint_as_float(r[0]) + 0.f, __uint_as_float(r[1]) + 0.f); }
      SB();
      dm[0] = mfma16(asb(kv[2]), w2[0][2], dm[0]);
#pragma unroll
      for (int r = 0; r < 2; ++r) tv[r] = __builtin_amdgcn_exp2f(tv[r] - mxv);
      SB();
      dm[1] = mfma16(asb(kv[2]), w2[1][2], dm[1]);
#pragma unroll
      for (int r = 2; r < 4; ++r) tv[r] = __builtin_amdgcn_exp2f(tv[r] - mxv);
      SB();
#pragma unroll
      for (int r = 4; r < 6; ++r) tv[r] = __builtin_amdgcn_exp2f(tv[r] - mxv);
      SB();
      dm[0] = mfma16(asb(kv[3]), w2[0][3], dm[0]);
#pragma unroll
      for (int r = 6; r < 8; ++r) tv[r] = __builtin_amdgcn_exp2f(tv[r] - mxv);
      SB();
      dm[1] = mfma16(asb(kv[3]), w2[1][3], dm[1]);
#pragma unroll
      for (int r = 8; r < 10; ++r) tv[r] = __builtin_amdgcn_exp2f(tv[r] - mxv);
      SB();
#pragma unroll
      for (int r = 10; r < 12; ++r) tv[r] = __builtin_amdgcn_exp2f(tv[r] - mxv);
      u32x4 px = {pack2(tv[0], tv[1]), pack2(tv[2], tv[3]), pack2(tv[4], tv[5]), pack2(tv[6], tv[7])};
      SB();
      f32x4 sx = mfma16(ones8, asb(px), z4);
      a[0] = mfma16(asb(vwx[0]), asb(px), z4);
      a[1] = mfma16(asb(vwx[1]), asb(px), z4);
      u32x4 pm;
      { const unsigned p0 = pack2(tv[8], tv[9]), p1 = pack2(tv[10], tv[11]);
        auto r0 = __builtin_amdgcn_permlane32_swap(0u, p0, false, false); auto r1 = __builtin_amdgcn_permlane32_swap(0u, p1, false, false);
        pm = (u32x4){r0[0], r1[0], 0u, 0u}; }
      SB();
      sx = mfma16(ones8, asb(pm), sx);
#pragma unroll
      for (int t = 0; t < 2; ++t) {
        u32x4 vwm = {g >= 2 ? pack2(dm[t][0], dm[t][1]) : 0u, g >= 2 ? pack2(dm[t][2], dm[t][3]) : 0u, 0u, 0u};
        a[t] = mfma16(asb(vwm), asb(pm), a[t]);
      }
      SB();
      const float inv = __builtin_amdgcn_rcpf(sx[0]);
      a[0] *= inv; a[1] *= inv;
    }
#pragma unroll
    for (int t = 0; t < 2; ++t) {
      if (t == 1 && !three) break;
      u32x4 v;
#pragma unroll
      for (int r = 0; r < 4; ++r) v[r] = min(__float_as_uint(a[t][r]), 0xFFFFFFFEu);
      if (ml < 8) st128(rs, poffE[t] ^ (poffX[t] & (0u - (unsigned)(s & 1))), v);
    }

    LBAR();
    const int r3n = r3 == 2 ? 0 : r3 + 1, r3nn = r3 == 0 ? 2 : r3 - 1;
    auto kvwrite = [&] {
      if (s + 2 < NSEG) {
#pragma unroll
        for (int i = 0; i < 4; ++i) *(u32x4*)(KV + r3nn * 16384 + w4 * 4096 + i * 1024 + lane * 16) = stg[i];
      }
    };
    if (w4 == 3) {
      kvwrite();
        const unsigned offw = (unsigned)(1 + (s & 1)) * PAGE + C_P + (unsigned)(cb / 3) * 16384u + (unsigned)(cb % 3) * 2560u + (unsigned)hg * 320u + (unsigned)cc * 16u;
        const unsigned off = cc < 5 ? offw + ((s & 1) ? selP1 : selP0) : OOB;
        slp<SLP_C>();
        u32x4 v[4];
        for (unsigned spin = 0;; ++spin) {
          asm volatile("" ::: "memory");
#pragma unroll
          for (int i = 0; i < 4; ++i) v[i] = ld128(rs, off + i * 80u);
          unsigned mx = 0;
#pragma unroll
          for (int i = 0; i < 4; ++i) { mx = umax3(mx, v[i][0], v[i][1]); mx = umax3(mx, v[i][2], v[i][3]); }
          if (__all(mx != 0xFFFFFFFFu) || spin >= spin_cap) { if (spin >= spin_cap) spin_cap = 0; break; }
        }

        asm volatile("s_waitcnt vmcnt(0)" ::: "memory");
        float f[4];
#pragma unroll
        for (int r = 0; r < 4; ++r) {
          float x = (__uint_as_float(v[0][r]) + __uint_as_float(v[1][r])) + (__uint_as_float(v[2][r]) + __uint_as_float(v[3][r]));
          x += ror8(x); x = xsum32(xsum16(x)); f[r] = x;
        }
        u32x2 w = {nosent(pack2(f[0], f[1])), nosent(pack2(f[2], f[3]))};
        const unsigned mq = (unsigned)cb >> 6, min_ = (mq >> 1) * 16384u + (mq & 1) * 2560u + (unsigned)(5 * (cb & 63) + lane) * 8u;
        if (lane < 5) { st64(rs, C_MEM + (unsigned)r3n * 32768u + min_, w); st64(rs, C_MEM + (unsigned)r3n * 32768u + 8192u + min_, w); }

        LBAR();
        if (cc < 5) {
#pragma unroll
          for (int i = 0; i < 4; ++i) st128(rs, off + i * 80u, ones4);
        }
        if (lane < 5) { st64(rs, C_MEM + (unsigned)r3 * 32768u + min_, ones2); st64(rs, C_MEM + (unsigned)r3 * 32768u + 8192u + min_, ones2); }
      prep(r3n);
    } else if (xat) {
      kvwrite();
      xattn1(kv);
      LBAR();
      xattn2(s, r3);
      prep(r3n);
      qxload(s + 1);
    } else {
      prep(r3n);
      kvwrite();
      LBAR();
    }
    if (s + 3 < NSEG) {
#pragma unroll
      for (int i = 0; i < 4; ++i) stg[i] = ld128(rw, kvs + (unsigned)(s + 3) * (unsigned)(H * 8192) + (unsigned)i * 1024u);
    }
  }
  LBAR();
  if (xat) {
    const unsigned hb = C_QKV + (unsigned)(r3 * H + h) * 16384u + selA;
    slp<SLP_B>();
    u32x4 kv[4];
    for (unsigned spin = 0;; ++spin) {
      asm volatile("" ::: "memory");
      unsigned mx = 0;
#pragma unroll
      for (int ks = 0; ks < 4; ++ks) { kv[ks] = ld128(rs, hb + kv_off + ks * 512u); mx = umax3(mx, kv[ks][0], kv[ks][2]); }
      if (__all(mx != 0xFFFFFFFFu) || spin >= spin_cap) break;
    }
    xattn1(kv);
    asm volatile("s_waitcnt lgkmcnt(0)" ::: "memory");
    xattn2(NSEG - 1, r3);
  }
}

__global__ __launch_bounds__(512, 1) void k_chain(const u16* __restrict__ mem0, const u16* __restrict__ Wq, const u16* __restrict__ Wk,
                                                   const u16* __restrict__ Wv, const u16* __restrict__ Wo, const u16* __restrict__ wg,
                                                   const u16* __restrict__ wu, char* __restrict__ ws) {
  extern __shared__ __attribute__((aligned(16))) char dyn_lds[];
  chain_role(blockIdx.x, mem0, Wq, Wk, Wv, Wo, wg, wu, ws, dyn_lds);
}

__global__ __launch_bounds__(512, 1) void k_post2(const char* __restrict__ ws, u16* __restrict__ out) {
  extern __shared__ __attribute__((aligned(16))) char lds[];
  const int tid = threadIdx.x, lane = tid & 63, wave = __builtin_amdgcn_readfirstlane(tid >> 6), ml = lane & 15, g = lane >> 4;
  const int b = blockIdx.x, xc = b & 7, idx = b >> 3, tt = xc * 4 + (idx >> 3), cgp = idx & 7;
  f32x4* X = (f32x4*)lds;
  f32x4 asum[2][2];
#pragma unroll
  for (int ph = 0; ph < 2; ++ph) {
    const int kt = ph == 0 ? 128 : 54, nk = ph == 0 ? 16 : (wave < 6 ? 7 : 6);
    const char* tb = ws + (ph ? OFF_HH : OFF_O) + (size_t)(tt * 4) * kt * 1024 + (size_t)wave * 1024 + lane * 16;
    const size_t tstr = (size_t)kt * 1024;
    const char* wb = ws + OFF_WF + ((size_t)(cgp * 5) * 182 + (ph ? 128 : 0) + wave) * 1024 + lane * 16;
    f32x4 acc[4][5];
#pragma unroll
    for (int i = 0; i < 4; ++i)
#pragma unroll
      for (int c = 0; c < 5; ++c) acc[i][c] = (f32x4){0, 0, 0, 0};
    b16x8 tf[4][4], wf[4][5];
    auto ldk = [&](int slot, int k) {
      const int kn = k < nk ? k : nk - 1;
#pragma unroll
      for (int i = 0; i < 4; ++i) tf[slot][i] = *(const b16x8*)(tb + i * tstr + (size_t)kn * 8192);
#pragma unroll
      for (int c = 0; c < 5; ++c) wf[slot][c] = *(const b16x8*)(wb + (size_t)c * 182 * 1024 + (size_t)kn * 8192);
    };
    ldk(0, 0); ldk(1, 1); ldk(2, 2);
    for (int k = 0; k < nk; k += 4) {
#pragma unroll
      for (int u = 0; u < 4; ++u) {
        ldk((u + 3) & 3, k + u + 3);
        if (k + u < nk) {
#pragma unroll
          for (int i = 0; i < 4; ++i)
#pragma unroll
            for (int c = 0; c < 5; ++c) acc[i][c] = mfma16(wf[u][c], tf[u][i], acc[i][c]);
        }
      }
    }
#pragma unroll
    for (int rd = 0; rd < 2; ++rd) {
#pragma unroll
      for (int t = 0; t < 10; ++t) { const int tl = rd * 10 + t; X[(wave * 10 + t) * 64 + lane] = acc[tl / 5][tl % 5]; }
      RBAR();
#pragma unroll
      for (int u = 0; u < 2; ++u) {
        const int t = wave + 8 * u;
        if (t < 10) {
          const f32x4* xp = X + t * 64 + lane;
          const f32x4 v = ((xp[0] + xp[640]) + (xp[1280] + xp[1920])) + ((xp[2560] + xp[3200]) + (xp[3840] + xp[4480]));
          if (ph == 0) asum[rd][u] = v;
          else {
            const int tl = rd * 10 + t, i = tl / 5, c = tl % 5;
            const int tok = tt * 64 + 16 * i + ml;
            const float gg = ((const float*)(ws + OFF_G))[tok], og = rbf(1.f - gg);
            float r[4];
#pragma unroll
            for (int q = 0; q < 4; ++q) { float ta = rbf(gg * rbf(asum[rd][u][q])), tb2 = rbf(og * rbf(v[q])); asm volatile("" : "+v"(ta), "+v"(tb2)); r[q] = ta + tb2; }
            *(u32x2*)(out + (size_t)tok * D + 80 * cgp + 16 * c + 4 * g) = (u32x2){pack2(r[0], r[1]), pack2(r[2], r[3])};
          }
        }
      }
      RBAR();
    }
  }
}

extern "C" void kernel_launch(void* const* d_in, const int* in_sizes, int n_in, void* d_out, int out_size, void* d_ws, size_t ws_size,
                              hipStream_t stream) {
  const u16* x = (const u16*)d_in[0];
  const u16* mem0 = (const u16*)d_in[1];
  const u16* Wq = (const u16*)d_in[2];
  const u16* Wk = (const u16*)d_in[3];
  const u16* Wv = (const u16*)d_in[4];
  const u16* Wo = (const u16*)d_in[5];
  const u16* wg = (const u16*)d_in[8];
  const u16* wu = (const u16*)d_in[9];
  const u16* wd = (const u16*)d_in[10];
  const u16* gw = (const u16*)d_in[11];
  char* ws = (char*)d_ws;
  k_init<<<NB_INIT1 + 512, 256, 0, stream>>>(x, Wo, wd, gw, ws);
  hipFuncSetAttribute((const void*)k_chain, hipFuncAttributeMaxDynamicSharedMemorySize, 156 * 1024);
  k_chain<<<256, 512, 156 * 1024, stream>>>(mem0, Wq, Wk, Wv, Wo, wg, wu, ws);
  hipFuncSetAttribute((const void*)k_post2, hipFuncAttributeMaxDynamicSharedMemorySize, 80 * 1024);
  k_post2<<<256, 512, 80 * 1024, stream>>>(ws, (u16*)d_out);
}
```
